```python
import torch

def _softmax(x, axis=-1):
    return torch.softmax(x, dim=axis)

def _gelu(x):
    return torch.nn.functional.gelu(x, approximate='tanh')

def _silu(x):
    return torch.nn.functional.silu(x)

def _sigmoid(x):
    return torch.sigmoid(x)

def _layernorm(x, axis=-1, eps=1e-05):
    m = x.mean(dim=axis, keepdim=True)
    v = x.var(dim=axis, keepdim=True, unbiased=False)
    return (x - m) / torch.sqrt(v + eps)

def _scatter_add(tgt, idx, src, axis=0):
    return tgt.clone().index_add_(axis, idx, src)

def _segment_sum(src, seg, n):
    out = torch.zeros((n,) + src.shape[1:], dtype=src.dtype)
    return out.index_add_(0, seg, src)

def _pad(x, pw):
    flat = tuple((v for pair in reversed(pw) for v in pair))
    return torch.nn.functional.pad(x, flat)
kernel_name = 'interleave_layers__ssm_conv_h3__mixer_gmlp__prompt'

def setup_inputs():

    _w = lambda a: (lambda t: t.to(torch.bfloat16) if t.is_floating_point() else t)(torch.as_tensor(a))

    def _A_setup():
        B, S, D = (1, 2048, 640)
        ks = 4
        return {'q': _w((lambda *s: torch.randn(*s, dtype=torch.bfloat16) * 0.05)(B, S, D)), 'k': _w((lambda *s: torch.randn(*s, dtype=torch.bfloat16) * 1.6)(B, S, D)), 'v': _w((lambda *s: torch.randn(*s, dtype=torch.bfloat16) * 1.6)(B, S, D)), 'shift_k': _w((lambda *s: torch.randn(*s, dtype=torch.bfloat16) * 1.6)(D, ks) * 0.5), 'ssm_k': _w((lambda *s: torch.randn(*s, dtype=torch.bfloat16) * 1.6)(D, S) * (1.0 / S ** 0.5)), 'ssm_D': _w((lambda *s: torch.randn(*s, dtype=torch.bfloat16) * 1.6)(D))}
    _A_in = _A_setup()

    def _B_setup():
        B, S, D, F = (1, 2048, 640, 1728)
        Fh = F // 2
        return {'x': _w((lambda *s: torch.randn(*s, dtype=torch.bfloat16) * 0.05)(B, S, D)), 'w_in': _w((lambda *s: torch.randn(*s, dtype=torch.bfloat16) * 1.6)(D, 2 * Fh)), 'w_sp': _w(0.01 * (lambda *s: torch.randn(*s, dtype=torch.bfloat16) * 1.6)(S, S)), 'b_sp': _w(1.0 + 0.01 * (lambda *s: torch.randn(*s, dtype=torch.bfloat16) * 0.2)(S)), 'w_out': _w((lambda *s: torch.randn(*s, dtype=torch.bfloat16) * 0.2)(Fh, D))}
    _B_in = _B_setup()
    return {'A_q': _A_in['q'], 'A_k': _A_in['k'], 'A_v': _A_in['v'], 'A_shift_k': _A_in['shift_k'], 'A_ssm_k': _A_in['ssm_k'], 'A_ssm_D': _A_in['ssm_D'], 'B_x': _B_in['x'], 'B_w_in': _B_in['w_in'], 'B_w_sp': _B_in['w_sp'], 'B_b_sp': _B_in['b_sp'], 'B_w_out': _B_in['w_out']}

def reference(A_q, A_k, A_v, A_shift_k, A_ssm_k, A_ssm_D, B_x, B_w_in, B_w_sp, B_b_sp, B_w_out):

    def _A_ref(q, k, v, shift_k, ssm_k, ssm_D):
        B, S, D = q.shape
        ks = shift_k.shape[-1]
        k_pad = _pad(k, ((0, 0), (ks - 1, 0), (0, 0)))
        k_shift = torch.zeros_like(k)
        for t in range(ks):
            k_shift = k_shift + k_pad[:, t:t + S, :] * shift_k[:, ks - 1 - t][None, None, :]
        kv = k_shift * v
        L = 2 * S
        kv_pad = _pad(kv, ((0, 0), (0, S), (0, 0)))
        kf_pad = _pad(ssm_k, ((0, 0), (0, S)))
        kv_f = (lambda x, axes=(-2, -1): torch.fft.rfft2(x.float(), dim=axes))(kv_pad[:, None, :, :], axes=(1, 2))
        kf_f = (lambda x, axes=(-2, -1): torch.fft.rfft2(x.float(), dim=axes))(kf_pad[:, None, :], axes=(1, 2))
        kf_f = (lambda x, *d: x.permute(*d))(kf_f, 1, 2, 0)
        y_f = kv_f * kf_f[None, :, :, :]
        y = (lambda x, s, axes=(-2, -1): torch.fft.irfft2(x, s=s, dim=axes).to(torch.bfloat16))(y_f, (1, L), axes=(1, 2))[:, 0, :S, :]
        y = y + kv * ssm_D[None, None, :]
        return q * y

    def _B_ref(x, w_in, w_sp, b_sp, w_out):
        B, S, D = x.shape
        F = w_in.shape[-1]
        Fh = F // 2
        z = _gelu(torch.einsum('bsd,df->bsf', x, w_in))
        z1 = z[..., :Fh].clone()
        z2 = _layernorm(z[..., Fh:], axis=-1)
        g = torch.einsum('ts,bsf->btf', w_sp, z2) + b_sp[None, :, None]
        return torch.einsum('bsf,fd->bsd', z1 * g, w_out)
    x = A_q
    x = x + _A_ref(_layernorm(x, axis=-1), A_k, A_v, A_shift_k, A_ssm_k, A_ssm_D)
    x = x + _B_ref(_layernorm(x, axis=-1), B_w_in, B_w_sp, B_b_sp, B_w_out)
    x = x + _A_ref(_layernorm(x, axis=-1), A_k, A_v, A_shift_k, A_ssm_k, A_ssm_D)
    x = x + _B_ref(_layernorm(x, axis=-1), B_w_in, B_w_sp, B_b_sp, B_w_out)
    return x
```

```cpp
#include <hip/hip_runtime.h>
#include <hip/hip_bf16.h>
#include <stdint.h>

typedef __attribute__((ext_vector_type(8))) short bf16x8;
typedef __attribute__((ext_vector_type(4))) short s16x4;
typedef __attribute__((ext_vector_type(4))) float f32x4;
typedef __attribute__((ext_vector_type(16))) float f32x16;
typedef __attribute__((ext_vector_type(2))) float f32x2;
typedef __attribute__((ext_vector_type(2))) __bf16 bf16x2_t;
typedef __attribute__((ext_vector_type(4))) uint32_t u32x4;
typedef __attribute__((ext_vector_type(2))) uint32_t u32x2;
typedef uint16_t bf16_t;

constexpr int S_ = 2048, D_ = 640, F_ = 1728, FH_ = 864, FHP_ = 896;

__device__ __forceinline__ uint32_t pk2(float lo, float hi) {
  f32x2 f = {lo, hi};
  bf16x2_t b = __builtin_convertvector(f, bf16x2_t);
  return __builtin_bit_cast(uint32_t, b);
}
__device__ __forceinline__ float lo16(uint32_t v) { return __uint_as_float(v << 16); }
__device__ __forceinline__ float hi16(uint32_t v) { return __uint_as_float(v & 0xffff0000u); }
__device__ __forceinline__ float rbf(float x) { return lo16(pk2(x, x)); }
__device__ __forceinline__ float bf2f(bf16_t v) { return __uint_as_float(((uint32_t)v) << 16); }

__device__ __forceinline__ f32x2 ln_stats(float sum, float sumsq, float inv_n) {
  float mean = sum * inv_n;
  float var = fmaxf(sumsq * inv_n - mean * mean, 0.f);
  float mb = rbf(mean);
  float vb = rbf(var);
  float sq = rbf(sqrtf(rbf(vb + 1e-5f)));
  f32x2 r = {mb, 1.0f / sq};
  return r;
}

constexpr int CV_HP = 4416;
constexpr int CV_HCH = 4 * CV_HP;
constexpr int CV_UCH = 96 * 80;
constexpr int CV_OFF_H = 0;
constexpr int CV_OFF_U = CV_OFF_H + 4 * CV_HCH;
constexpr int CV_OFF_R = CV_OFF_U + 4 * CV_UCH;
constexpr int CV_OFF_Y = CV_OFF_R + 4 * 8192;
constexpr int CV_LDS = CV_OFF_Y + 2048 * 8;

__global__ __launch_bounds__(512, 2) void k0_conv_prep(
    const bf16_t* __restrict__ q, const bf16_t* __restrict__ kin, const bf16_t* __restrict__ vin,
    const bf16_t* __restrict__ shift_k, const bf16_t* __restrict__ ssm_k, const bf16_t* __restrict__ ssm_D,
    const bf16_t* __restrict__ w_in, const bf16_t* __restrict__ w_out,
    bf16_t* __restrict__ Yout, bf16_t* __restrict__ winT, float* __restrict__ cs, bf16_t* __restrict__ woutT,
    float* __restrict__ xst) {
  extern __shared__ __attribute__((aligned(16))) char smem[];
  const int tid = threadIdx.x;
  const int xcd = blockIdx.x & 7, bi = blockIdx.x >> 3;
  if (bi >= 20) {
    const int j = (bi - 20) * 8 + xcd;
    const int r = tid >> 3, c = tid & 7;
    if (j < 27) {
      const int f0 = 64 * j;
      bf16_t* T = (bf16_t*)smem;
      u32x4 v[10];
#pragma unroll
      for (int it = 0; it < 10; ++it) v[it] = *(const u32x4*)(w_in + (size_t)(64 * it + r) * F_ + f0 + 8 * c);
#pragma unroll
      for (int it = 0; it < 10; ++it) {
#pragma unroll
        for (int i = 0; i < 4; ++i) {
          T[(8 * c + 2 * i) * 648 + 64 * it + r] = (bf16_t)(v[it][i] & 0xffff);
          T[(8 * c + 2 * i + 1) * 648 + 64 * it + r] = (bf16_t)(v[it][i] >> 16);
        }
      }
      __syncthreads();
      float sum = 0.f;
#pragma unroll
      for (int it = 0; it < 10; ++it) {
        u32x4 w = *(const u32x4*)(T + r * 648 + 64 * it + 8 * c);
        *(u32x4*)(winT + (size_t)(f0 + r) * D_ + 64 * it + 8 * c) = w;
#pragma unroll
        for (int i = 0; i < 4; ++i) sum += lo16(w[i]) + hi16(w[i]);
      }
      sum += __shfl_xor(sum, 1); sum += __shfl_xor(sum, 2); sum += __shfl_xor(sum, 4);
      if (c == 0) cs[f0 + r] = sum;
    } else if (j < 41) {
      const int f0 = 64 * (j - 27);
      bf16_t* T = (bf16_t*)smem;
      const bool valid = (f0 + r) < FH_;
      u32x4 v[10];
#pragma unroll
      for (int it = 0; it < 10; ++it) {
        u32x4 z = {0u, 0u, 0u, 0u};
        v[it] = valid ? *(const u32x4*)(w_out + (size_t)(f0 + r) * D_ + 64 * it + 8 * c) : z;
      }
#pragma unroll
      for (int it = 0; it < 10; ++it) {
#pragma unroll
        for (int i = 0; i < 4; ++i) {
          T[(64 * it + 8 * c + 2 * i) * 72 + r] = (bf16_t)(v[it][i] & 0xffff);
          T[(64 * it + 8 * c + 2 * i + 1) * 72 + r] = (bf16_t)(v[it][i] >> 16);
        }
      }
      __syncthreads();
#pragma unroll
      for (int it = 0; it < 10; ++it) {
        const int d = 64 * it + r;
        u32x4 w = *(const u32x4*)(T + d * 72 + 8 * c);
        *(u32x4*)(woutT + (size_t)d * FHP_ + f0 + 8 * c) = w;
      }
    } else if (j < 73) {
      const int row = 64 * (j - 41) + r;
      float sum = 0.f, sq = 0.f;
#pragma unroll
      for (int it = 0; it < 10; ++it) {
        u32x4 w = *(const u32x4*)(q + (size_t)row * D_ + 64 * it + 8 * c);
#pragma unroll
        for (int i = 0; i < 4; ++i) {
          float a = lo16(w[i]), b = hi16(w[i]);
          sum += a + b; sq += a * a + b * b;
        }
      }
      sum += __shfl_xor(sum, 1); sum += __shfl_xor(sum, 2); sum += __shfl_xor(sum, 4);
      sq += __shfl_xor(sq, 1); sq += __shfl_xor(sq, 2); sq += __shfl_xor(sq, 4);
      f32x2 o = {c == 0 ? sum : 0.f, c == 0 ? sq : 0.f};
      *(f32x2*)(xst + ((size_t)row * 8 + c) * 2) = o;
    }
    return;
  }
  const int g = xcd * 20 + bi;
  const int c0 = 4 * g;
  u32x2 kr[7], vr[4];
#pragma unroll
  for (int i = 0; i < 7; ++i) {
    int s = 4 * tid - 3 + i;
    u32x2 z = {0u, 0u};
    kr[i] = (s >= 0) ? *(const u32x2*)(kin + (size_t)s * D_ + c0) : z;
  }
#pragma unroll
  for (int i = 0; i < 4; ++i) vr[i] = *(const u32x2*)(vin + (size_t)(4 * tid + i) * D_ + c0);
  u32x2 hr[3][3];
#pragma unroll
  for (int rd = 0; rd < 3; ++rd) {
    int task = tid + 512 * rd;
    int ch = task / 262, c = task % 262 - 6;
#pragma unroll
    for (int i = 0; i < 3; ++i) {
      int idx = 8 * c + 4 * i;
      u32x2 z = {0u, 0u};
      hr[rd][i] = (task < 1048 && idx >= 0 && idx < 2048) ? *(const u32x2*)(ssm_k + (size_t)(c0 + ch) * S_ + idx) : z;
    }
  }
  float wsh[4][4];
#pragma unroll
  for (int ch = 0; ch < 4; ++ch) {
    u32x2 w = *(const u32x2*)(shift_k + (c0 + ch) * 4);
    wsh[ch][0] = lo16(w[0]); wsh[ch][1] = hi16(w[0]); wsh[ch][2] = lo16(w[1]); wsh[ch][3] = hi16(w[1]);
  }
  for (int i = tid; i < 4 * 160; i += 512) {
    int ch = i / 160, o = i % 160;
    u32x4 z = {0u, 0u, 0u, 0u};
    *(u32x4*)(smem + CV_OFF_U + ch * CV_UCH + o * 16) = z;
  }
#pragma unroll
  for (int ch = 0; ch < 4; ++ch) {
    float kv[4];
#pragma unroll
    for (int i = 0; i < 4; ++i) {
      float kk[4];
#pragma unroll
      for (int t = 0; t < 4; ++t) {
        uint32_t w = kr[i + t][ch >> 1];
        kk[t] = (ch & 1) ? hi16(w) : lo16(w);
      }
      float ks = rbf(kk[0] * wsh[ch][3]);
      ks = rbf(ks + rbf(kk[1] * wsh[ch][2]));
      ks = rbf(ks + rbf(kk[2] * wsh[ch][1]));
      ks = rbf(ks + rbf(kk[3] * wsh[ch][0]));
      uint32_t w = vr[i][ch >> 1];
      float vv = (ch & 1) ? hi16(w) : lo16(w);
      kv[i] = ks * vv;
    }
    u32x2 o = {pk2(kv[0], kv[1]), pk2(kv[2], kv[3])};
    int s = 4 * tid;
    *(u32x2*)(smem + CV_OFF_U + ch * CV_UCH + ((s >> 5) + 32) * 80 + (s & 31) * 2) = o;
  }
#pragma unroll
  for (int rd = 0; rd < 3; ++rd) {
    int task = tid + 512 * rd;
    if (task < 1048) {
      int ch = task / 262, c = task % 262 - 6;
      uint32_t w[6] = {hr[rd][0][0], hr[rd][0][1], hr[rd][1][0], hr[rd][1][1], hr[rd][2][0], hr[rd][2][1]};
#pragma unroll
      for (int p = 0; p < 4; ++p) {
#pragma unroll
        for (int gi = 0; gi < 2; ++gi) {
          const int ao = ((1 - p) & 3) + 4 * gi;
          int a = 8 * c + ao;
          int qx = 2045 - p - a;
          auto el = [&](int e) -> uint32_t { return (e & 1) ? (w[e >> 1] >> 16) : (w[e >> 1] & 0xffff); };
          u32x2 o = {el(ao + 3) | (el(ao + 2) << 16), el(ao + 1) | (el(ao) << 16)};
          if (qx >= 0 && qx < 2088) *(u32x2*)(smem + CV_OFF_H + ch * CV_HCH + p * CV_HP + qx * 2) = o;
        }
      }
    }
  }
  __syncthreads();
  const int wid = tid >> 6, lane = tid & 63;
  const int ch = wid & 3, kb = wid >> 2;
  const int m = lane & 31, hh = lane >> 5;
  {
    const int p = (4 - (m & 3)) & 3;
    const char* abase = smem + CV_OFF_H + ch * CV_HCH + p * CV_HP + (2048 - m + 8 * hh - p + 16 * kb) * 2;
    const char* bbase = smem + CV_OFF_U + ch * CV_UCH + (m + 32) * 80 + 32 * kb + 16 * hh;
    f32x16 acc0 = {}, acc1 = {};
#pragma unroll 4
    for (int dl = 0; dl < 32; ++dl) {
      u32x2 a00 = *(const volatile u32x2*)(abase - 64 * dl);
      u32x2 a01 = *(const volatile u32x2*)(abase - 64 * dl + 8);
      u32x2 a10 = *(const volatile u32x2*)(abase - 64 * (dl + 32));
      u32x2 a11 = *(const volatile u32x2*)(abase - 64 * (dl + 32) + 8);
      bf16x8 B0 = *(const bf16x8*)(bbase - 80 * dl);
      bf16x8 B1 = *(const bf16x8*)(bbase + 80 * (32 - dl));
      u32x4 A0u = {a00[0], a00[1], a01[0], a01[1]};
      u32x4 A1u = {a10[0], a10[1], a11[0], a11[1]};
      bf16x8 A0 = __builtin_bit_cast(bf16x8, A0u), A1 = __builtin_bit_cast(bf16x8, A1u);
      acc0 = __builtin_amdgcn_mfma_f32_32x32x16_bf16(A0, B0, acc0, 0, 0, 0);
      acc1 = __builtin_amdgcn_mfma_f32_32x32x16_bf16(A0, B1, acc1, 0, 0, 0);
      acc1 = __builtin_amdgcn_mfma_f32_32x32x16_bf16(A1, B0, acc1, 0, 0, 0);
    }
    float* red = (float*)(smem + CV_OFF_R + ch * 8192);
    if (kb == 1) {
#pragma unroll
      for (int r = 0; r < 16; ++r) { red[r * 64 + lane] = acc0[r]; red[(16 + r) * 64 + lane] = acc1[r]; }
    }
    __syncthreads();
    if (kb == 0) {
      const float Dv = bf2f(ssm_D[c0 + ch]);
      bf16_t* yb = (bf16_t*)(smem + CV_OFF_Y);
#pragma unroll
      for (int half = 0; half < 2; ++half) {
        const int a = m + 32 * half;
#pragma unroll
        for (int rq = 0; rq < 4; ++rq) {
          const int b0 = 8 * rq + 4 * hh;
          u32x2 kvw = *(const u32x2*)(smem + CV_OFF_U + ch * CV_UCH + (a + 32) * 80 + b0 * 2);
          float kvv[4] = {lo16(kvw[0]), hi16(kvw[0]), lo16(kvw[1]), hi16(kvw[1])};
#pragma unroll
          for (int i = 0; i < 4; ++i) {
            const int r = 4 * rq + i;
            float y = (half ? acc1[r] : acc0[r]) + red[(16 * half + r) * 64 + lane];
            float o = rbf(y) + rbf(kvv[i] * Dv);
            yb[(32 * a + b0 + i) * 4 + ch] = (bf16_t)(pk2(o, o) & 0xffff);
          }
        }
      }
    }
    __syncthreads();
#pragma unroll
    for (int i = 0; i < 4; ++i) {
      int s = tid + 512 * i;
      u32x2 y = *(const u32x2*)(smem + CV_OFF_Y + s * 8);
      *(u32x2*)(Yout + (size_t)s * D_ + c0) = y;
    }
  }
}

constexpr int G1_OFF_A = 0;
constexpr int G1_OFF_B = 81920;
constexpr int G1_OFF_RS = G1_OFF_B + 57344;
constexpr int G1_OFF_ZS = G1_OFF_RS + 512;
constexpr int G1_LDS = G1_OFF_ZS + 2048;

__device__ __forceinline__ float gelu_tanh(float x) {
  float y = 0.7978845608028654f * (x + 0.044715f * x * x * x);
  float e = __expf(2.f * y);
  return x - x * __frcp_rn(1.f + e);
}

__global__ __launch_bounds__(512, 2) void g1_kernel(
    const bf16_t* __restrict__ x0, const bf16_t* __restrict__ Yb, const float* __restrict__ xst,
    const bf16_t* __restrict__ winT, const float* __restrict__ cs,
    bf16_t* __restrict__ x1out, bf16_t* __restrict__ z, float* __restrict__ zst) {
  extern __shared__ __attribute__((aligned(16))) char smem[];
  const int tid = threadIdx.x;
  const int xcd = blockIdx.x & 7, idx = blockIdx.x >> 3;
  const int mt = 8 * (xcd >> 1) + (idx & 7);
  const int nt = 4 * (xcd & 1) + (idx >> 3);
  const int fs = (27 * nt + 1) >> 1;
  const int nf = ((27 * (nt + 1) + 1) >> 1) - fs;
  const int m0 = 64 * mt;
  const int r = tid >> 3, c = tid & 7;
  u32x4 xa[10], ya[10];
#pragma unroll
  for (int kt = 0; kt < 10; ++kt) {
    xa[kt] = *(const u32x4*)(x0 + (size_t)(m0 + r) * D_ + 64 * kt + 8 * c);
    ya[kt] = *(const u32x4*)(Yb + (size_t)(m0 + r) * D_ + 64 * kt + 8 * c);
  }
  float s0 = 0.f, s1 = 0.f;
  {
    const f32x4* p = (const f32x4*)(xst + (size_t)(m0 + r) * 16);
#pragma unroll
    for (int i = 0; i < 4; ++i) { f32x4 v = p[i]; s0 += v[0] + v[2]; s1 += v[1] + v[3]; }
  }
  const int nrows = 16 * nf;
  int brow[4];
#pragma unroll
  for (int i = 0; i < 4; ++i) { int rr = r + 64 * i; brow[i] = rr < nrows ? rr : nrows - 1; }
  const bool b3 = tid < 256;
  constexpr int PD = 3;
  u32x4 wb[PD][4];
  auto loadB = [&](int slot, int kt) {
#pragma unroll
    for (int i = 0; i < 4; ++i)
      if (i < 3 || b3) wb[slot][i] = *(const u32x4*)(winT + (size_t)(16 * fs + brow[i]) * D_ + 64 * kt + 8 * c);
  };
  auto writeB = [&](int slot, int buf) {
#pragma unroll
    for (int i = 0; i < 4; ++i) {
      int rr = r + 64 * i;
      if (i < 3 || b3) *(u32x4*)(smem + G1_OFF_B + buf * 28672 + rr * 128 + ((c ^ (rr & 7)) << 4)) = wb[slot][i];
    }
  };
#pragma unroll
  for (int d = 0; d < PD; ++d) loadB(d, d);

  const f32x2 st = ln_stats(s0, s1, 1.0f / D_);
  float t0 = 0.f, t1 = 0.f;
#pragma unroll
  for (int kt = 0; kt < 10; ++kt) {
    u32x4 o;
#pragma unroll
    for (int i = 0; i < 4; ++i) {
      float a = lo16(xa[kt][i]), b = hi16(xa[kt][i]);
      float ya_ = lo16(ya[kt][i]), yb_ = hi16(ya[kt][i]);
      float da = rbf(rbf(a - st[0]) * st[1]), db = rbf(rbf(b - st[0]) * st[1]);
      float ta = rbf(da * ya_), tb = rbf(db * yb_);
      uint32_t w = pk2(a + ta, b + tb);
      o[i] = w;
      float xa_ = lo16(w), xb_ = hi16(w);
      t0 += xa_ + xb_; t1 += xa_ * xa_ + xb_ * xb_;
    }
    *(u32x4*)(smem + G1_OFF_A + kt * 8192 + r * 128 + ((c ^ (r & 7)) << 4)) = o;
    if (nt == 0) *(u32x4*)(x1out + (size_t)(m0 + r) * D_ + 64 * kt + 8 * c) = o;
  }
  t0 += __shfl_xor(t0, 1); t0 += __shfl_xor(t0, 2); t0 += __shfl_xor(t0, 4);
  t1 += __shfl_xor(t1, 1); t1 += __shfl_xor(t1, 2); t1 += __shfl_xor(t1, 4);
  if (c == 0) {
    f32x2 s = ln_stats(t0, t1, 1.0f / D_);
    *(f32x2*)(smem + G1_OFF_RS + r * 8) = s;
  }
  writeB(0, 0);
  loadB(0, PD);
  __syncthreads();

  const int wid = tid >> 6, lane = tid & 63, fr = lane & 15, g = lane >> 4;
  const int wm = wid & 1, wn = wid >> 1;
  f32x4 acc[4][2] = {};
  int wrow[4];
#pragma unroll
  for (int i = 0; i < 4; ++i) { int fi = 4 * wn + i; wrow[i] = 16 * (fi < nf ? fi : nf - 1) + fr; }
#pragma unroll
  for (int kt = 0; kt < 10; ++kt) {
    const int buf = kt & 1;
    if (kt + 1 < 10) {
      writeB((kt + 1) % PD, buf ^ 1);
      if (kt + 1 + PD < 10) loadB((kt + 1) % PD, kt + 1 + PD);
    }
#pragma unroll
    for (int ks = 0; ks < 2; ++ks) {
      bf16x8 X[2], W[4];
#pragma unroll
      for (int i = 0; i < 2; ++i) {
        int rr = 32 * wm + 16 * i + fr;
        X[i] = *(const bf16x8*)(smem + G1_OFF_A + kt * 8192 + rr * 128 + (((4 * ks + g) ^ (rr & 7)) << 4));
      }
#pragma unroll
      for (int i = 0; i < 4; ++i)
        W[i] = *(const bf16x8*)(smem + G1_OFF_B + buf * 28672 + wrow[i] * 128 + (((4 * ks + g) ^ (wrow[i] & 7)) << 4));
#pragma unroll
      for (int i = 0; i < 4; ++i)
#pragma unroll
        for (int j = 0; j < 2; ++j) acc[i][j] = __builtin_amdgcn_mfma_f32_16x16x32_bf16(W[i], X[j], acc[i][j], 0, 0, 0);
    }
    __syncthreads();
  }
  float zs0[2] = {0.f, 0.f}, zs1[2] = {0.f, 0.f};
#pragma unroll
  for (int i = 0; i < 4; ++i) {
    const int fi = 4 * wn + i;
    if (fi < nf) {
      const int f = 16 * (fs + fi) + 4 * g;
      f32x4 csv = *(const f32x4*)(cs + f);
#pragma unroll
      for (int j = 0; j < 2; ++j) {
        const int rr = 32 * wm + 16 * j + fr;
        f32x2 rs = *(const f32x2*)(smem + G1_OFF_RS + rr * 8);
        float o[4];
#pragma unroll
        for (int e = 0; e < 4; ++e) {
          float pre = rbf((acc[i][j][e] - rs[0] * csv[e]) * rs[1]);
          o[e] = gelu_tanh(pre);
        }
        u32x2 w = {pk2(o[0], o[1]), pk2(o[2], o[3])};
        *(u32x2*)(z + (size_t)(m0 + rr) * F_ + f) = w;
        if (nt >= 4) {
          float a = lo16(w[0]), b = hi16(w[0]), cc = lo16(w[1]), d = hi16(w[1]);
          zs0[j] += a + b + cc + d; zs1[j] += a * a + b * b + cc * cc + d * d;
        }
      }
    }
  }
  if (nt >= 4) {
#pragma unroll
    for (int j = 0; j < 2; ++j) {
      zs0[j] += __shfl_xor(zs0[j], 16); zs0[j] += __shfl_xor(zs0[j], 32);
      zs1[j] += __shfl_xor(zs1[j], 16); zs1[j] += __shfl_xor(zs1[j], 32);
      if (g == 0) {
        f32x2 o = {zs0[j], zs1[j]};
        *(f32x2*)(smem + G1_OFF_ZS + (wn * 64 + 32 * wm + 16 * j + fr) * 8) = o;
      }
    }
    __syncthreads();
    if (tid < 64) {
      float a = 0.f, b = 0.f;
#pragma unroll
      for (int w = 0; w < 4; ++w) { f32x2 v = *(const f32x2*)(smem + G1_OFF_ZS + (w * 64 + tid) * 8); a += v[0]; b += v[1]; }
      f32x2 o = {a, b};
      *(f32x2*)(zst + ((size_t)(m0 + tid) * 4 + (nt - 4)) * 2) = o;
    }
  }
}

constexpr int G2_OFF_A = 0;
constexpr int G2_OFF_B = 32768;
constexpr int G2_OFF_ST = 49152;
constexpr int G2_LDS = 65536;

__global__ __launch_bounds__(512, 2) void g2_kernel(
    const bf16_t* __restrict__ wsp, const bf16_t* __restrict__ z, const float* __restrict__ zst,
    const bf16_t* __restrict__ bsp, bf16_t* __restrict__ u) {
  extern __shared__ __attribute__((aligned(16))) char smem[];
  const int tid = threadIdx.x;
  const int xcd = blockIdx.x & 7, idx = blockIdx.x >> 3;
  const int mt = 4 * (xcd >> 1) + (idx & 3);
  const int nt = 7 * (xcd & 1) + (idx >> 2);
  const int t0 = 128 * mt, f0 = 64 * nt;
  const int r = tid >> 3, c = tid & 7;
  constexpr int PD = 4;
  u32x4 ra[PD][2], rb[PD];
  auto loadS = [&](int slot, int kt) {
    ra[slot][0] = *(const u32x4*)(wsp + (size_t)(t0 + r) * S_ + 64 * kt + 8 * c);
    ra[slot][1] = *(const u32x4*)(wsp + (size_t)(t0 + r + 64) * S_ + 64 * kt + 8 * c);
    rb[slot] = *(const u32x4*)(z + (size_t)(64 * kt + r) * F_ + FH_ + f0 + 8 * c);
  };
  const int bswz = (((r >> 1) & 1) | (((r >> 3) & 1) << 1)) << 1;
  auto writeS = [&](int slot, int kt, int buf) {
    *(u32x4*)(smem + G2_OFF_A + buf * 16384 + r * 128 + ((c ^ (r & 7)) << 4)) = ra[slot][0];
    *(u32x4*)(smem + G2_OFF_A + buf * 16384 + (r + 64) * 128 + ((c ^ (r & 7)) << 4)) = ra[slot][1];
    f32x2 st = *(const f32x2*)(smem + G2_OFF_ST + (64 * kt + r) * 8);
    u32x4 o;
#pragma unroll
    for (int i = 0; i < 4; ++i) {
      float a = lo16(rb[slot][i]), b = hi16(rb[slot][i]);
      o[i] = pk2(rbf(a - st[0]) * st[1], rbf(b - st[0]) * st[1]);
    }
    *(u32x4*)(smem + G2_OFF_B + buf * 8192 + r * 128 + ((c ^ bswz) << 4)) = o;
  };
#pragma unroll
  for (int d = 0; d < PD; ++d) loadS(d, d);
  {
    const f32x4* p = (const f32x4*)(zst + (size_t)tid * 32);
#pragma unroll
    for (int i = 0; i < 4; ++i) {
      f32x4 a = p[2 * i], b = p[2 * i + 1];
      f32x2 s = ln_stats(a[0] + a[2] + b[0] + b[2], a[1] + a[3] + b[1] + b[3], 1.0f / FH_);
      *(f32x2*)(smem + G2_OFF_ST + (4 * tid + i) * 8) = s;
    }
  }
  __syncthreads();
  writeS(0, 0, 0);
  loadS(0, PD);
  __syncthreads();

  const int wid = tid >> 6, lane = tid & 63, fr = lane & 15, g = lane >> 4;
  const int wm = wid & 3, wn = wid >> 2;
  f32x4 acc[2][2] = {};
  const int q4 = (lane & 15) >> 2, p4 = lane & 3;
#pragma unroll 1
  for (int kt0 = 0; kt0 < 32; kt0 += PD) {
#pragma unroll
    for (int kk = 0; kk < PD; ++kk) {
      const int kt = kt0 + kk;
      const int buf = kk & 1;
      if (kt + 1 < 32) {
        writeS((kk + 1) % PD, kt + 1, buf ^ 1);
        if (kt + 1 + PD < 32) loadS((kk + 1) % PD, kt + 1 + PD);
      }
#pragma unroll
      for (int ks = 0; ks < 2; ++ks) {
        bf16x8 T[2], Z[2];
#pragma unroll
        for (int i = 0; i < 2; ++i) {
          int rr = 32 * wm + 16 * i + fr;
          T[i] = *(const bf16x8*)(smem + G2_OFF_A + buf * 16384 + rr * 128 + (((4 * ks + g) ^ (rr & 7)) << 4));
        }
#pragma unroll
        for (int i = 0; i < 2; ++i) {
          s16x4 h[2];
#pragma unroll
          for (int t = 0; t < 2; ++t) {
            int row = 32 * ks + 8 * g + 4 * t + q4;
            int ch = 2 * (2 * wn + i) + (p4 >> 1);
            int sw = (((row >> 1) & 1) | (((row >> 3) & 1) << 1)) << 1;
            typedef __attribute__((address_space(3))) s16x4 lds_s16x4;
            h[t] = __builtin_amdgcn_ds_read_tr16_b64_v4i16(
                (lds_s16x4*)(smem + G2_OFF_B + buf * 8192 + row * 128 + ((ch ^ sw) << 4) + 8 * (p4 & 1)));
          }
          Z[i] = __builtin_shufflevector(h[0], h[1], 0, 1, 2, 3, 4, 5, 6, 7);
        }
#pragma unroll
        for (int i = 0; i < 2; ++i)
#pragma unroll
          for (int j = 0; j < 2; ++j) acc[i][j] = __builtin_amdgcn_mfma_f32_16x16x32_bf16(Z[i], T[j], acc[i][j], 0, 0, 0);
      }
      __syncthreads();
    }
  }
#pragma unroll
  for (int j = 0; j < 2; ++j) {
    const int t = t0 + 32 * wm + 16 * j + fr;
    const float bb = bf2f(bsp[t]);
#pragma unroll
    for (int i = 0; i < 2; ++i) {
      const int f = f0 + 32 * wn + 16 * i + 4 * g;
      u32x2 w = {0u, 0u};
      if (f < FH_) {
        u32x2 z1 = *(const u32x2*)(z + (size_t)t * F_ + f);
        float g0 = rbf(rbf(acc[i][j][0]) + bb), g1 = rbf(rbf(acc[i][j][1]) + bb);
        float g2 = rbf(rbf(acc[i][j][2]) + bb), g3 = rbf(rbf(acc[i][j][3]) + bb);
        w[0] = pk2(lo16(z1[0]) * g0, hi16(z1[0]) * g1);
        w[1] = pk2(lo16(z1[1]) * g2, hi16(z1[1]) * g3);
      }
      *(u32x2*)(u + (size_t)t * FHP_ + f) = w;
    }
  }
}

constexpr int G3_OFF_A = 0;
constexpr int G3_OFF_B = 16384;
constexpr int G3_LDS = 36864;

__global__ __launch_bounds__(256, 1) void g3_kernel(
    const bf16_t* __restrict__ u, const bf16_t* __restrict__ woutT, const bf16_t* __restrict__ x1,
    bf16_t* __restrict__ xout, float* __restrict__ xst) {
  extern __shared__ __attribute__((aligned(16))) char smem[];
  const int tid = threadIdx.x;
  const int xcd = blockIdx.x & 7, idx = blockIdx.x >> 3;
  const int mt = 8 * (xcd >> 1) + (idx & 7);
  const int nt = 4 * (xcd & 1) + (idx >> 3);
  const int t0 = 64 * mt, d0 = 80 * nt;
  const int r = tid >> 3, c = tid & 7;
  constexpr int PD = 4;
  u32x4 ra[PD][2], rb[PD][3];
  const bool b2 = tid < 128;
  auto loadS = [&](int slot, int kt) {
    ra[slot][0] = *(const u32x4*)(u + (size_t)(t0 + r) * FHP_ + 64 * kt + 8 * c);
    ra[slot][1] = *(const u32x4*)(u + (size_t)(t0 + r + 32) * FHP_ + 64 * kt + 8 * c);
    rb[slot][0] = *(const u32x4*)(woutT + (size_t)(d0 + r) * FHP_ + 64 * kt + 8 * c);
    rb[slot][1] = *(const u32x4*)(woutT + (size_t)(d0 + r + 32) * FHP_ + 64 * kt + 8 * c);
    if (b2) rb[slot][2] = *(const u32x4*)(woutT + (size_t)(d0 + r + 64) * FHP_ + 64 * kt + 8 * c);
  };
  auto writeS = [&](int slot, int buf) {
    const int sw = (c ^ (r & 7)) << 4;
    *(u32x4*)(smem + G3_OFF_A + buf * 8192 + r * 128 + sw) = ra[slot][0];
    *(u32x4*)(smem + G3_OFF_A + buf * 8192 + (r + 32) * 128 + sw) = ra[slot][1];
    *(u32x4*)(smem + G3_OFF_B + buf * 10240 + r * 128 + sw) = rb[slot][0];
    *(u32x4*)(smem + G3_OFF_B + buf * 10240 + (r + 32) * 128 + sw) = rb[slot][1];
    if (b2) *(u32x4*)(smem + G3_OFF_B + buf * 10240 + (r + 64) * 128 + sw) = rb[slot][2];
  };
#pragma unroll
  for (int d = 0; d < PD; ++d) loadS(d, d);
  writeS(0, 0);
  loadS(0, PD);
  __syncthreads();
  const int wid = tid >> 6, lane = tid & 63, fr = lane & 15, g = lane >> 4;
  f32x4 acc[5] = {};
#pragma unroll
  for (int kt = 0; kt < 14; ++kt) {
    const int buf = kt & 1;
    if (kt + 1 < 14) {
      writeS((kt + 1) % PD, buf ^ 1);
      if (kt + 1 + PD < 14) loadS((kt + 1) % PD, kt + 1 + PD);
    }
#pragma unroll
    for (int ks = 0; ks < 2; ++ks) {
      const int rr = 16 * wid + fr;
      bf16x8 T = *(const bf16x8*)(smem + G3_OFF_A + buf * 8192 + rr * 128 + (((4 * ks + g) ^ (rr & 7)) << 4));
#pragma unroll
      for (int i = 0; i < 5; ++i) {
        const int wr = 16 * i + fr;
        bf16x8 W = *(const bf16x8*)(smem + G3_OFF_B + buf * 10240 + wr * 128 + (((4 * ks + g) ^ (wr & 7)) << 4));
        acc[i] = __builtin_amdgcn_mfma_f32_16x16x32_bf16(W, T, acc[i], 0, 0, 0);
      }
    }
    __syncthreads();
  }
  const int t = t0 + 16 * wid + fr;
  float s0 = 0.f, s1 = 0.f;
#pragma unroll
  for (int i = 0; i < 5; ++i) {
    const int d = d0 + 16 * i + 4 * g;
    u32x2 xr = *(const u32x2*)(x1 + (size_t)t * D_ + d);
    u32x2 w = {pk2(lo16(xr[0]) + rbf(acc[i][0]), hi16(xr[0]) + rbf(acc[i][1])),
               pk2(lo16(xr[1]) + rbf(acc[i][2]), hi16(xr[1]) + rbf(acc[i][3]))};
    *(u32x2*)(xout + (size_t)t * D_ + d) = w;
    float a = lo16(w[0]), b = hi16(w[0]), cc = lo16(w[1]), dd = hi16(w[1]);
    s0 += a + b + cc + dd; s1 += a * a + b * b + cc * cc + dd * dd;
  }
  s0 += __shfl_xor(s0, 16); s0 += __shfl_xor(s0, 32);
  s1 += __shfl_xor(s1, 16); s1 += __shfl_xor(s1, 32);
  if (g == 0) {
    f32x2 o = {s0, s1};
    *(f32x2*)(xst + ((size_t)t * 8 + nt) * 2) = o;
  }
}

extern "C" void kernel_launch(void* const* d_in, const int* in_sizes, int n_in,
                              void* d_out, int out_size, void* d_ws, size_t ws_size,
                              hipStream_t stream) {
  const bf16_t* q = (const bf16_t*)d_in[0];
  const bf16_t* k = (const bf16_t*)d_in[1];
  const bf16_t* v = (const bf16_t*)d_in[2];
  const bf16_t* shift_k = (const bf16_t*)d_in[3];
  const bf16_t* ssm_k = (const bf16_t*)d_in[4];
  const bf16_t* ssm_D = (const bf16_t*)d_in[5];
  const bf16_t* w_in = (const bf16_t*)d_in[7];
  const bf16_t* w_sp = (const bf16_t*)d_in[8];
  const bf16_t* b_sp = (const bf16_t*)d_in[9];
  const bf16_t* w_out = (const bf16_t*)d_in[10];
  bf16_t* out = (bf16_t*)d_out;

  char* ws = (char*)d_ws;
  size_t off = 0;
  auto alloc = [&](size_t bytes) { char* p = ws + off; off += (bytes + 255) & ~(size_t)255; return p; };
  bf16_t* winT = (bf16_t*)alloc((size_t)F_ * D_ * 2);
  float* cs = (float*)alloc(F_ * 4);
  bf16_t* woutT = (bf16_t*)alloc((size_t)D_ * FHP_ * 2);
  bf16_t* Y = (bf16_t*)alloc((size_t)S_ * D_ * 2);
  float* xst0 = (float*)alloc((size_t)S_ * 16 * 4);
  float* xst1 = (float*)alloc((size_t)S_ * 16 * 4);
  float* xst2 = (float*)alloc((size_t)S_ * 16 * 4);
  bf16_t* x1 = (bf16_t*)alloc((size_t)S_ * D_ * 2);
  bf16_t* x2 = (bf16_t*)alloc((size_t)S_ * D_ * 2);
  bf16_t* x3 = (bf16_t*)alloc((size_t)S_ * D_ * 2);
  bf16_t* z = (bf16_t*)alloc((size_t)S_ * F_ * 2 + 4096);
  float* zst = (float*)alloc((size_t)S_ * 8 * 4);
  bf16_t* u = (bf16_t*)alloc((size_t)S_ * FHP_ * 2);

  (void)hipFuncSetAttribute((const void*)k0_conv_prep, hipFuncAttributeMaxDynamicSharedMemorySize, CV_LDS);
  (void)hipFuncSetAttribute((const void*)g1_kernel, hipFuncAttributeMaxDynamicSharedMemorySize, G1_LDS);
  (void)hipFuncSetAttribute((const void*)g2_kernel, hipFuncAttributeMaxDynamicSharedMemorySize, G2_LDS);
  (void)hipFuncSetAttribute((const void*)g3_kernel, hipFuncAttributeMaxDynamicSharedMemorySize, G3_LDS);

  k0_conv_prep<<<256, 512, CV_LDS, stream>>>(q, k, v, shift_k, ssm_k, ssm_D, w_in, w_out, Y, winT, cs, woutT, xst0);
  g1_kernel<<<256, 512, G1_LDS, stream>>>(q, Y, xst0, winT, cs, x1, z, zst);
  g2_kernel<<<224, 512, G2_LDS, stream>>>(w_sp, z, zst, b_sp, u);
  g3_kernel<<<256, 256, G3_LDS, stream>>>(u, woutT, x1, x2, xst1);
  g1_kernel<<<256, 512, G1_LDS, stream>>>(x2, Y, xst1, winT, cs, x3, z, zst);
  g2_kernel<<<224, 512, G2_LDS, stream>>>(w_sp, z, zst, b_sp, u);
  g3_kernel<<<256, 256, G3_LDS, stream>>>(u, woutT, x3, out, xst2);
}
```

```cpp
#include <hip/hip_runtime.h>
#include <hip/hip_bf16.h>
#include <stdint.h>

typedef __attribute__((ext_vector_type(8))) short bf16x8;
typedef __attribute__((ext_vector_type(4))) short s16x4;
typedef __attribute__((ext_vector_type(4))) float f32x4;
typedef __attribute__((ext_vector_type(16))) float f32x16;
typedef __attribute__((ext_vector_type(2))) float f32x2;
typedef __attribute__((ext_vector_type(2))) __bf16 bf16x2_t;
typedef __attribute__((ext_vector_type(4))) uint32_t u32x4;
typedef __attribute__((ext_vector_type(2))) uint32_t u32x2;
typedef uint16_t bf16_t;

#define TSP
#define TSA(k)
#define TS(i)

constexpr int S_ = 2048, D_ = 640, F_ = 1728, FH_ = 864, FHP_ = 896;

__device__ __forceinline__ uint32_t pk2(float lo, float hi) {
  f32x2 f = {lo, hi};
  bf16x2_t b = __builtin_convertvector(f, bf16x2_t);
  return __builtin_bit_cast(uint32_t, b);
}
__device__ __forceinline__ float lo16(uint32_t v) { return __uint_as_float(v << 16); }
__device__ __forceinline__ float hi16(uint32_t v) { return __uint_as_float(v & 0xffff0000u); }
__device__ __forceinline__ float rbf(float x) { return lo16(pk2(x, x)); }
__device__ __forceinline__ float bf2f(bf16_t v) { return __uint_as_float(((uint32_t)v) << 16); }

__device__ __forceinline__ f32x2 ln_stats(float sum, float sumsq, float inv_n) {
  float mean = sum * inv_n;
  float var = fmaxf(sumsq * inv_n - mean * mean, 0.f);
  float mb = rbf(mean);
  float vb = rbf(var);
  float sq = rbf(sqrtf(rbf(vb + 1e-5f)));
  f32x2 r = {mb, 1.0f / sq};
  return r;
}

constexpr int CV_HP = 4416;
constexpr int CV_HCH = 4 * CV_HP;
constexpr int CV_UCH = 96 * 80;
constexpr int CV_OFF_H = 0;
constexpr int CV_OFF_U = CV_OFF_H + 4 * CV_HCH;
constexpr int CV_OFF_R = CV_OFF_U + 4 * CV_UCH;
constexpr int CV_OFF_Y = CV_OFF_R + 4 * 8192;
constexpr int CV_LDS = CV_OFF_Y + 2048 * 8;

__global__ __launch_bounds__(512, 2) void k0_conv_prep(
    const bf16_t* __restrict__ q, const bf16_t* __restrict__ kin, const bf16_t* __restrict__ vin,
    const bf16_t* __restrict__ shift_k, const bf16_t* __restrict__ ssm_k, const bf16_t* __restrict__ ssm_D,
    const bf16_t* __restrict__ w_in, const bf16_t* __restrict__ w_out,
    bf16_t* __restrict__ Yout, bf16_t* __restrict__ winT, float* __restrict__ cs, bf16_t* __restrict__ woutT,
    float* __restrict__ xst TSP) {
  extern __shared__ __attribute__((aligned(16))) char smem[];
  const int tid = threadIdx.x;
  TS(0);
  const int xcd = blockIdx.x & 7, bi = blockIdx.x >> 3;
  if (bi >= 20) {
    const int j = (bi - 20) * 8 + xcd;
    const int r = tid >> 3, c = tid & 7;
    if (j < 27) {
      const int f0 = 64 * j;
      bf16_t* T = (bf16_t*)smem;
      u32x4 v[10];
#pragma unroll
      for (int it = 0; it < 10; ++it) v[it] = *(const u32x4*)(w_in + (size_t)(64 * it + r) * F_ + f0 + 8 * c);
#pragma unroll
      for (int it = 0; it < 10; ++it) {
#pragma unroll
        for (int i = 0; i < 4; ++i) {
          T[(8 * c + 2 * i) * 648 + 64 * it + r] = (bf16_t)(v[it][i] & 0xffff);
          T[(8 * c + 2 * i + 1) * 648 + 64 * it + r] = (bf16_t)(v[it][i] >> 16);
        }
      }
      __syncthreads();
      float sum = 0.f;
#pragma unroll
      for (int it = 0; it < 10; ++it) {
        u32x4 w = *(const u32x4*)(T + r * 648 + 64 * it + 8 * c);
        *(u32x4*)(winT + (size_t)(f0 + r) * D_ + 64 * it + 8 * c) = w;
#pragma unroll
        for (int i = 0; i < 4; ++i) sum += lo16(w[i]) + hi16(w[i]);
      }
      sum += __shfl_xor(sum, 1); sum += __shfl_xor(sum, 2); sum += __shfl_xor(sum, 4);
      if (c == 0) cs[f0 + r] = sum;
    } else if (j < 41) {
      const int f0 = 64 * (j - 27);
      bf16_t* T = (bf16_t*)smem;
      const bool valid = (f0 + r) < FH_;
      u32x4 v[10];
#pragma unroll
      for (int it = 0; it < 10; ++it) {
        u32x4 z = {0u, 0u, 0u, 0u};
        v[it] = valid ? *(const u32x4*)(w_out + (size_t)(f0 + r) * D_ + 64 * it + 8 * c) : z;
      }
#pragma unroll
      for (int it = 0; it < 10; ++it) {
#pragma unroll
        for (int i = 0; i < 4; ++i) {
          T[(64 * it + 8 * c + 2 * i) * 72 + r] = (bf16_t)(v[it][i] & 0xffff);
          T[(64 * it + 8 * c + 2 * i + 1) * 72 + r] = (bf16_t)(v[it][i] >> 16);
        }
      }
      __syncthreads();
#pragma unroll
      for (int it = 0; it < 10; ++it) {
        const int d = 64 * it + r;
        u32x4 w = *(const u32x4*)(T + d * 72 + 8 * c);
        *(u32x4*)(woutT + (size_t)d * FHP_ + f0 + 8 * c) = w;
      }
    } else if (j < 73) {
      const int row = 64 * (j - 41) + r;
      float sum = 0.f, sq = 0.f;
#pragma unroll
      for (int it = 0; it < 10; ++it) {
        u32x4 w = *(const u32x4*)(q + (size_t)row * D_ + 64 * it + 8 * c);
#pragma unroll
        for (int i = 0; i < 4; ++i) {
          float a = lo16(w[i]), b = hi16(w[i]);
          sum += a + b; sq += a * a + b * b;
        }
      }
      sum += __shfl_xor(sum, 1); sum += __shfl_xor(sum, 2); sum += __shfl_xor(sum, 4);
      sq += __shfl_xor(sq, 1); sq += __shfl_xor(sq, 2); sq += __shfl_xor(sq, 4);
      f32x2 o = {c == 0 ? sum : 0.f, c == 0 ? sq : 0.f};
      *(f32x2*)(xst + ((size_t)row * 8 + c) * 2) = o;
    }
    return;
  }
  const int g = xcd * 20 + bi;
  const int c0 = 4 * g;
  u32x2 kr[7], vr[4];
#pragma unroll
  for (int i = 0; i < 7; ++i) {
    int s = 4 * tid - 3 + i;
    u32x2 z = {0u, 0u};
    kr[i] = (s >= 0) ? *(const u32x2*)(kin + (size_t)s * D_ + c0) : z;
  }
#pragma unroll
  for (int i = 0; i < 4; ++i) vr[i] = *(const u32x2*)(vin + (size_t)(4 * tid + i) * D_ + c0);
  u32x2 hr[3][3];
#pragma unroll
  for (int rd = 0; rd < 3; ++rd) {
    int task = tid + 512 * rd;
    int ch = task / 262, c = task % 262 - 6;
#pragma unroll
    for (int i = 0; i < 3; ++i) {
      int idx = 8 * c + 4 * i;
      u32x2 z = {0u, 0u};
      hr[rd][i] = (task < 1048 && idx >= 0 && idx < 2048) ? *(const u32x2*)(ssm_k + (size_t)(c0 + ch) * S_ + idx) : z;
    }
  }
  float wsh[4][4];
#pragma unroll
  for (int ch = 0; ch < 4; ++ch) {
    u32x2 w = *(const u32x2*)(shift_k + (c0 + ch) * 4);
    wsh[ch][0] = lo16(w[0]); wsh[ch][1] = hi16(w[0]); wsh[ch][2] = lo16(w[1]); wsh[ch][3] = hi16(w[1]);
  }
  for (int i = tid; i < 4 * 160; i += 512) {
    int ch = i / 160, o = i % 160;
    u32x4 z = {0u, 0u, 0u, 0u};
    *(u32x4*)(smem + CV_OFF_U + ch * CV_UCH + o * 16) = z;
  }
  TS(1);
#pragma unroll
  for (int ch = 0; ch < 4; ++ch) {
    float kv[4];
#pragma unroll
    for (int i = 0; i < 4; ++i) {
      float kk[4];
#pragma unroll
      for (int t = 0; t < 4; ++t) {
        uint32_t w = kr[i + t][ch >> 1];
        kk[t] = (ch & 1) ? hi16(w) : lo16(w);
      }
      float ks = rbf(kk[0] * wsh[ch][3]);
      ks = rbf(ks + rbf(kk[1] * wsh[ch][2]));
      ks = rbf(ks + rbf(kk[2] * wsh[ch][1]));
      ks = rbf(ks + rbf(kk[3] * wsh[ch][0]));
      uint32_t w = vr[i][ch >> 1];
      float vv = (ch & 1) ? hi16(w) : lo16(w);
      kv[i] = ks * vv;
    }
    u32x2 o = {pk2(kv[0], kv[1]), pk2(kv[2], kv[3])};
    int s = 4 * tid;
    *(u32x2*)(smem + CV_OFF_U + ch * CV_UCH + ((s >> 5) + 32) * 80 + (s & 31) * 2) = o;
  }
#pragma unroll
  for (int rd = 0; rd < 3; ++rd) {
    int task = tid + 512 * rd;
    if (task < 1048) {
      int ch = task / 262, c = task % 262 - 6;
      uint32_t w[6] = {hr[rd][0][0], hr[rd][0][1], hr[rd][1][0], hr[rd][1][1], hr[rd][2][0], hr[rd][2][1]};
#pragma unroll
      for (int p = 0; p < 4; ++p) {
#pragma unroll
        for (int gi = 0; gi < 2; ++gi) {
          const int ao = ((1 - p) & 3) + 4 * gi;
          int a = 8 * c + ao;
          int qx = 2045 - p - a;
          auto el = [&](int e) -> uint32_t { return (e & 1) ? (w[e >> 1] >> 16) : (w[e >> 1] & 0xffff); };
          u32x2 o = {el(ao + 3) | (el(ao + 2) << 16), el(ao + 1) | (el(ao) << 16)};
          if (qx >= 0 && qx < 2088) *(u32x2*)(smem + CV_OFF_H + ch * CV_HCH + p * CV_HP + qx * 2) = o;
        }
      }
    }
  }
  TS(2);
  __syncthreads();
  TS(3);
  const int wid = tid >> 6, lane = tid & 63;
  const int ch = wid & 3, kb = wid >> 2;
  const int m = lane & 31, hh = lane >> 5;
  {
    const int p = (4 - (m & 3)) & 3;
    const char* abase = smem + CV_OFF_H + ch * CV_HCH + p * CV_HP + (2048 - m + 8 * hh - p + 16 * kb) * 2;
    const char* bbase = smem + CV_OFF_U + ch * CV_UCH + (m + 32) * 80 + 32 * kb + 16 * hh;
    f32x16 acc0 = {}, acc1 = {};
#pragma unroll 4
    for (int dl = 0; dl < 32; ++dl) {
      u32x2 a00 = *(const volatile u32x2*)(abase - 64 * dl);
      u32x2 a01 = *(const volatile u32x2*)(abase - 64 * dl + 8);
      u32x2 a10 = *(const volatile u32x2*)(abase - 64 * (dl + 32));
      u32x2 a11 = *(const volatile u32x2*)(abase - 64 * (dl + 32) + 8);
      bf16x8 B0 = *(const bf16x8*)(bbase - 80 * dl);
      bf16x8 B1 = *(const bf16x8*)(bbase + 80 * (32 - dl));
      u32x4 A0u = {a00[0], a00[1], a01[0], a01[1]};
      u32x4 A1u = {a10[0], a10[1], a11[0], a11[1]};
      bf16x8 A0 = __builtin_bit_cast(bf16x8, A0u), A1 = __builtin_bit_cast(bf16x8, A1u);
      acc0 = __builtin_amdgcn_mfma_f32_32x32x16_bf16(A0, B0, acc0, 0, 0, 0);
      acc1 = __builtin_amdgcn_mfma_f32_32x32x16_bf16(A0, B1, acc1, 0, 0, 0);
      acc1 = __builtin_amdgcn_mfma_f32_32x32x16_bf16(A1, B0, acc1, 0, 0, 0);
    }
    TS(4);
    float* red = (float*)(smem + CV_OFF_R + ch * 8192);
    if (kb == 1) {
#pragma unroll
      for (int r = 0; r < 16; ++r) { red[r * 64 + lane] = acc0[r]; red[(16 + r) * 64 + lane] = acc1[r]; }
    }
    __syncthreads();
    if (kb == 0) {
      const float Dv = bf2f(ssm_D[c0 + ch]);
      bf16_t* yb = (bf16_t*)(smem + CV_OFF_Y);
#pragma unroll
      for (int half = 0; half < 2; ++half) {
        const int a = m + 32 * half;
#pragma unroll
        for (int rq = 0; rq < 4; ++rq) {
          const int b0 = 8 * rq + 4 * hh;
          u32x2 kvw = *(const u32x2*)(smem + CV_OFF_U + ch * CV_UCH + (a + 32) * 80 + b0 * 2);
          float kvv[4] = {lo16(kvw[0]), hi16(kvw[0]), lo16(kvw[1]), hi16(kvw[1])};
#pragma unroll
          for (int i = 0; i < 4; ++i) {
            const int r = 4 * rq + i;
            float y = (half ? acc1[r] : acc0[r]) + red[(16 * half + r) * 64 + lane];
            float o = rbf(y) + rbf(kvv[i] * Dv);
            yb[(32 * a + b0 + i) * 4 + ch] = (bf16_t)(pk2(o, o) & 0xffff);
          }
        }
      }
    }
    __syncthreads();
    TS(5);
#pragma unroll
    for (int i = 0; i < 4; ++i) {
      int s = tid + 512 * i;
      u32x2 y = *(const u32x2*)(smem + CV_OFF_Y + s * 8);
      *(u32x2*)(Yout + (size_t)s * D_ + c0) = y;
    }
    TS(6);
  }
}

constexpr int G1_OFF_A = 0;
constexpr int G1_OFF_B = 81920;
constexpr int G1_OFF_RS = G1_OFF_B + 57344;
constexpr int G1_OFF_ZS = G1_OFF_RS + 512;
constexpr int G1_LDS = G1_OFF_ZS + 2048;

__device__ __forceinline__ float gelu_tanh(float x) {
  float y = 0.7978845608028654f * (x + 0.044715f * x * x * x);
  float e = __expf(2.f * y);
  return x - x * __frcp_rn(1.f + e);
}

__global__ __launch_bounds__(512, 2) void g1_kernel(
    const bf16_t* __restrict__ x0, const bf16_t* __restrict__ Yb, const float* __restrict__ xst,
    const bf16_t* __restrict__ winT, const float* __restrict__ cs,
    bf16_t* __restrict__ x1out, bf16_t* __restrict__ z, float* __restrict__ zst TSP) {
  extern __shared__ __attribute__((aligned(16))) char smem[];
  const int tid = threadIdx.x;
  TS(0);
  const int xcd = blockIdx.x & 7, idx = blockIdx.x >> 3;
  const int mt = 8 * (xcd >> 1) + (idx & 7);
  const int nt = 4 * (xcd & 1) + (idx >> 3);
  const int fs = (27 * nt + 1) >> 1;
  const int nf = ((27 * (nt + 1) + 1) >> 1) - fs;
  const int m0 = 64 * mt;
  const int r = tid >> 3, c = tid & 7;
  u32x4 xa[10], ya[10];
#pragma unroll
  for (int kt = 0; kt < 10; ++kt) {
    xa[kt] = *(const u32x4*)(x0 + (size_t)(m0 + r) * D_ + 64 * kt + 8 * c);
    ya[kt] = *(const u32x4*)(Yb + (size_t)(m0 + r) * D_ + 64 * kt + 8 * c);
  }
  float s0 = 0.f, s1 = 0.f;
  {
    const f32x4* p = (const f32x4*)(xst + (size_t)(m0 + r) * 16);
#pragma unroll
    for (int i = 0; i < 4; ++i) { f32x4 v = p[i]; s0 += v[0] + v[2]; s1 += v[1] + v[3]; }
  }
  const int nrows = 16 * nf;
  int brow[4];
#pragma unroll
  for (int i = 0; i < 4; ++i) { int rr = r + 64 * i; brow[i] = rr < nrows ? rr : nrows - 1; }
  const bool b3 = tid < 256;
  constexpr int PD = 3;
  u32x4 wb[PD][4];
  auto loadB = [&](int slot, int kt) {
#pragma unroll
    for (int i = 0; i < 4; ++i)
      if (i < 3 || b3) wb[slot][i] = *(const u32x4*)(winT + (size_t)(16 * fs + brow[i]) * D_ + 64 * kt + 8 * c);
  };
  auto writeB = [&](int slot, int buf) {
#pragma unroll
    for (int i = 0; i < 4; ++i) {
      int rr = r + 64 * i;
      if (i < 3 || b3) *(u32x4*)(smem + G1_OFF_B + buf * 28672 + rr * 128 + ((c ^ (rr & 7)) << 4)) = wb[slot][i];
    }
  };
#pragma unroll
  for (int d = 0; d < PD; ++d) loadB(d, d);

  const f32x2 st = ln_stats(s0, s1, 1.0f / D_);
  TS(1);
  float t0 = 0.f, t1 = 0.f;
#pragma unroll
  for (int kt = 0; kt < 10; ++kt) {
    u32x4 o;
#pragma unroll
    for (int i = 0; i < 4; ++i) {
      float a = lo16(xa[kt][i]), b = hi16(xa[kt][i]);
      float ya_ = lo16(ya[kt][i]), yb_ = hi16(ya[kt][i]);
      float da = rbf(rbf(a - st[0]) * st[1]), db = rbf(rbf(b - st[0]) * st[1]);
      float ta = rbf(da * ya_), tb = rbf(db * yb_);
      uint32_t w = pk2(a + ta, b + tb);
      o[i] = w;
      float xa_ = lo16(w), xb_ = hi16(w);
      t0 += xa_ + xb_; t1 += xa_ * xa_ + xb_ * xb_;
    }
    *(u32x4*)(smem + G1_OFF_A + kt * 8192 + r * 128 + ((c ^ (r & 7)) << 4)) = o;
    if (nt == 0) *(u32x4*)(x1out + (size_t)(m0 + r) * D_ + 64 * kt + 8 * c) = o;
  }
  t0 += __shfl_xor(t0, 1); t0 += __shfl_xor(t0, 2); t0 += __shfl_xor(t0, 4);
  t1 += __shfl_xor(t1, 1); t1 += __shfl_xor(t1, 2); t1 += __shfl_xor(t1, 4);
  if (c == 0) {
    f32x2 s = ln_stats(t0, t1, 1.0f / D_);
    *(f32x2*)(smem + G1_OFF_RS + r * 8) = s;
  }
  TS(2);
  writeB(0, 0);
  loadB(0, PD);
  __syncthreads();
  TS(3);

  const int wid = tid >> 6, lane = tid & 63, fr = lane & 15, g = lane >> 4;
  const int wm = wid & 1, wn = wid >> 1;
  f32x4 acc[4][2] = {};
  int wrow[4];
#pragma unroll
  for (int i = 0; i < 4; ++i) { int fi = 4 * wn + i; wrow[i] = 16 * (fi < nf ? fi : nf - 1) + fr; }
#pragma unroll
  for (int kt = 0; kt < 10; ++kt) {
    const int buf = kt & 1;
    if (kt + 1 < 10) {
      writeB((kt + 1) % PD, buf ^ 1);
      if (kt + 1 + PD < 10) loadB((kt + 1) % PD, kt + 1 + PD);
    }
#pragma unroll
    for (int ks = 0; ks < 2; ++ks) {
      bf16x8 X[2], W[4];
#pragma unroll
      for (int i = 0; i < 2; ++i) {
        int rr = 32 * wm + 16 * i + fr;
        X[i] = *(const bf16x8*)(smem + G1_OFF_A + kt * 8192 + rr * 128 + (((4 * ks + g) ^ (rr & 7)) << 4));
      }
#pragma unroll
      for (int i = 0; i < 4; ++i)
        W[i] = *(const bf16x8*)(smem + G1_OFF_B + buf * 28672 + wrow[i] * 128 + (((4 * ks + g) ^ (wrow[i] & 7)) << 4));
#pragma unroll
      for (int i = 0; i < 4; ++i)
#pragma unroll
        for (int j = 0; j < 2; ++j) acc[i][j] = __builtin_amdgcn_mfma_f32_16x16x32_bf16(W[i], X[j], acc[i][j], 0, 0, 0);
    }
    __syncthreads();
  }
  TS(4);
  float zs0[2] = {0.f, 0.f}, zs1[2] = {0.f, 0.f};
#pragma unroll
  for (int i = 0; i < 4; ++i) {
    const int fi = 4 * wn + i;
    if (fi < nf) {
      const int f = 16 * (fs + fi) + 4 * g;
      f32x4 csv = *(const f32x4*)(cs + f);
#pragma unroll
      for (int j = 0; j < 2; ++j) {
        const int rr = 32 * wm + 16 * j + fr;
        f32x2 rs = *(const f32x2*)(smem + G1_OFF_RS + rr * 8);
        float o[4];
#pragma unroll
        for (int e = 0; e < 4; ++e) {
          float pre = rbf((acc[i][j][e] - rs[0] * csv[e]) * rs[1]);
          o[e] = gelu_tanh(pre);
        }
        u32x2 w = {pk2(o[0], o[1]), pk2(o[2], o[3])};
        *(u32x2*)(z + (size_t)(m0 + rr) * F_ + f) = w;
        if (nt >= 4) {
          float a = lo16(w[0]), b = hi16(w[0]), cc = lo16(w[1]), d = hi16(w[1]);
          zs0[j] += a + b + cc + d; zs1[j] += a * a + b * b + cc * cc + d * d;
        }
      }
    }
  }
  if (nt >= 4) {
#pragma unroll
    for (int j = 0; j < 2; ++j) {
      zs0[j] += __shfl_xor(zs0[j], 16); zs0[j] += __shfl_xor(zs0[j], 32);
      zs1[j] += __shfl_xor(zs1[j], 16); zs1[j] += __shfl_xor(zs1[j], 32);
      if (g == 0) {
        f32x2 o = {zs0[j], zs1[j]};
        *(f32x2*)(smem + G1_OFF_ZS + (wn * 64 + 32 * wm + 16 * j + fr) * 8) = o;
      }
    }
    __syncthreads();
    if (tid < 64) {
      float a = 0.f, b = 0.f;
#pragma unroll
      for (int w = 0; w < 4; ++w) { f32x2 v = *(const f32x2*)(smem + G1_OFF_ZS + (w * 64 + tid) * 8); a += v[0]; b += v[1]; }
      f32x2 o = {a, b};
      *(f32x2*)(zst + ((size_t)(m0 + tid) * 4 + (nt - 4)) * 2) = o;
    }
  }
  TS(5);
}

constexpr int G2_NS = 5;
constexpr int G2_OFF_A = 0;
constexpr int G2_OFF_B = G2_NS * 16384;
constexpr int G2_OFF_ST = G2_OFF_B + 16384;
constexpr int G2_LDS = G2_OFF_ST + 16384;

__device__ __forceinline__ void glds16(const void* gsrc, uint32_t lds_dst) {
  uint32_t keep;
  asm volatile("s_mov_b32 %0, m0\n\ts_mov_b32 m0, %2\n\ts_nop 0\n\tglobal_load_lds_dwordx4 %1, off\n\ts_mov_b32 m0, %0"
               : "=&s"(keep) : "v"(gsrc), "s"(lds_dst) : "memory");
}
__device__ __forceinline__ uint32_t lds_addr(const void* p) {
  return (uint32_t)(uintptr_t)(const __attribute__((address_space(3))) char*)p;
}
#define WAIT_VM(n) asm volatile("s_waitcnt vmcnt(" #n ")" ::: "memory")
#define WAIT_LGKM0() asm volatile("s_waitcnt lgkmcnt(0)" ::: "memory")
#define RAW_BAR() __builtin_amdgcn_s_barrier()

__global__ __launch_bounds__(768, 1) void g2_kernel(
    const bf16_t* __restrict__ wsp, const bf16_t* __restrict__ z, const float* __restrict__ zst,
    const bf16_t* __restrict__ bsp, bf16_t* __restrict__ u TSP) {
  extern __shared__ __attribute__((aligned(16))) char smem[];
  const int tid = threadIdx.x;
  TS(0);
  const int xcd = blockIdx.x & 7, idx = blockIdx.x >> 3;
  const int mt = 4 * (xcd >> 1) + (idx & 3);
  const int nt = 7 * (xcd & 1) + (idx >> 2);
  const int t0 = 128 * mt, f0 = 64 * nt;
  const int wid = tid >> 6, lane = tid & 63;
  if (wid >= 8) {
    const int pt = tid - 512;
    const int r = pt >> 3, c = pt & 7;
    constexpr int PD = 4;
    u32x4 rb[PD][2];
    const bf16_t* zp = z + (size_t)r * F_ + FH_ + f0 + 8 * c;
    auto loadB = [&](int slot, int kt) {
      rb[slot][0] = *(const u32x4*)(zp + (size_t)(64 * kt) * F_);
      rb[slot][1] = *(const u32x4*)(zp + (size_t)(64 * kt + 32) * F_);
    };
    const int bswz0 = (((r >> 1) & 1) | (((r >> 3) & 1) << 1)) << 1;
    auto writeB = [&](int slot, int kt) {
#pragma unroll
      for (int h = 0; h < 2; ++h) {
        f32x2 st = *(const f32x2*)(smem + G2_OFF_ST + (64 * kt + r + 32 * h) * 8);
        u32x4 o;
#pragma unroll
        for (int i = 0; i < 4; ++i) {
          float a = lo16(rb[slot][h][i]), b = hi16(rb[slot][h][i]);
          o[i] = pk2(rbf(a - st[0]) * st[1], rbf(b - st[0]) * st[1]);
        }
        *(u32x4*)(smem + G2_OFF_B + (kt & 1) * 8192 + (r + 32 * h) * 128 + ((c ^ bswz0) << 4)) = o;
      }
    };
    f32x4 sv[16];
    {
      const f32x4* p = (const f32x4*)(zst + (size_t)pt * 64);
#pragma unroll
      for (int i = 0; i < 16; ++i) sv[i] = p[i];
    }
#pragma unroll
    for (int d = 0; d < PD; ++d) loadB(d, d);
#pragma unroll
    for (int i = 0; i < 8; ++i) {
      f32x4 a = sv[2 * i], b = sv[2 * i + 1];
      f32x2 s = ln_stats(a[0] + a[2] + b[0] + b[2], a[1] + a[3] + b[1] + b[3], 1.0f / FH_);
      *(f32x2*)(smem + G2_OFF_ST + (8 * pt + i) * 8) = s;
    }
    WAIT_LGKM0();
    RAW_BAR();
    writeB(0, 0);
    loadB(0, PD);
    WAIT_LGKM0();
#pragma unroll 1
    for (int kt0 = 0; kt0 < 32; kt0 += PD) {
#pragma unroll
      for (int kk = 0; kk < PD; ++kk) {
        const int kt = kt0 + kk;
        RAW_BAR();
        if (kt + 1 < 32) {
          writeB((kk + 1) % PD, kt + 1);
          if (kt + 1 + PD < 32) loadB((kk + 1) % PD, kt + 1 + PD);
        }
        WAIT_LGKM0();
      }
    }
    return;
  }
  const int lr = lane >> 3, lc = (lane & 7) ^ lr;
  const bf16_t* ap0 = wsp + (size_t)(t0 + 16 * wid + lr) * S_ + 8 * lc;
  const bf16_t* ap1 = ap0 + (size_t)8 * S_;
  const uint32_t lbase = lds_addr(smem) + G2_OFF_A + __builtin_amdgcn_readfirstlane(wid) * 2048;
  auto issueA = [&](int kt, int slot) {
    glds16(ap0 + 64 * kt, lbase + slot * 16384);
    glds16(ap1 + 64 * kt, lbase + slot * 16384 + 1024);
  };
#pragma unroll
  for (int s = 0; s < G2_NS - 1; ++s) issueA(s, s);
  RAW_BAR();
  TS(1);
  const int fr = lane & 15, g = lane >> 4;
  const int wm = wid & 3, wn = wid >> 2;
  f32x4 acc[2][2] = {};
  const int q4 = (lane & 15) >> 2, p4 = lane & 3;
  int slot = 0;
#pragma unroll 1
  for (int kt = 0; kt < 32; ++kt) {
    WAIT_VM(6);
    RAW_BAR();
    {
      int kn = kt + G2_NS - 1; kn = kn < 32 ? kn : 31;
      int sn = slot + G2_NS - 1; sn = sn >= G2_NS ? sn - G2_NS : sn;
      issueA(kn, sn);
    }
    const char* abase = smem + G2_OFF_A + slot * 16384;
    const char* bbase = smem + G2_OFF_B + (kt & 1) * 8192;
#pragma unroll
    for (int ks = 0; ks < 2; ++ks) {
      bf16x8 T[2], Z[2];
#pragma unroll
      for (int i = 0; i < 2; ++i) {
        int rr = 32 * wm + 16 * i + fr;
        T[i] = *(const bf16x8*)(abase + rr * 128 + (((4 * ks + g) ^ (rr & 7)) << 4));
      }
#pragma unroll
      for (int i = 0; i < 2; ++i) {
        s16x4 h[2];
#pragma unroll
        for (int t = 0; t < 2; ++t) {
          int row = 32 * ks + 8 * g + 4 * t + q4;
          int ch = 2 * (2 * wn + i) + (p4 >> 1);
          int sw = (((row >> 1) & 1) | (((row >> 3) & 1) << 1)) << 1;
          typedef __attribute__((address_space(3))) s16x4 lds_s16x4;
          h[t] = __builtin_amdgcn_ds_read_tr16_b64_v4i16(
              (lds_s16x4*)(bbase + row * 128 + ((ch ^ sw) << 4) + 8 * (p4 & 1)));
        }
        Z[i] = __builtin_shufflevector(h[0], h[1], 0, 1, 2, 3, 4, 5, 6, 7);
      }
#pragma unroll
      for (int i = 0; i < 2; ++i)
#pragma unroll
        for (int j = 0; j < 2; ++j) acc[i][j] = __builtin_amdgcn_mfma_f32_16x16x32_bf16(Z[i], T[j], acc[i][j], 0, 0, 0);
    }
    WAIT_LGKM0();
    slot = slot + 1 == G2_NS ? 0 : slot + 1;
  }
  WAIT_VM(0);
  TS(3);
#pragma unroll
  for (int j = 0; j < 2; ++j) {
    const int t = t0 + 32 * wm + 16 * j + fr;
    const float bb = bf2f(bsp[t]);
#pragma unroll
    for (int i = 0; i < 2; ++i) {
      const int f = f0 + 32 * wn + 16 * i + 4 * g;
      u32x2 w = {0u, 0u};
      if (f < FH_) {
        u32x2 z1 = *(const u32x2*)(z + (size_t)t * F_ + f);
        float g0 = rbf(rbf(acc[i][j][0]) + bb), g1 = rbf(rbf(acc[i][j][1]) + bb);
        float g2 = rbf(rbf(acc[i][j][2]) + bb), g3 = rbf(rbf(acc[i][j][3]) + bb);
        w[0] = pk2(lo16(z1[0]) * g0, hi16(z1[0]) * g1);
        w[1] = pk2(lo16(z1[1]) * g2, hi16(z1[1]) * g3);
      }
      *(u32x2*)(u + (size_t)t * FHP_ + f) = w;
    }
  }
  TS(4);
}

constexpr int G3_OFF_A = 0;
constexpr int G3_OFF_B = 16384;
constexpr int G3_LDS = 36864;

__global__ __launch_bounds__(256, 1) void g3_kernel(
    const bf16_t* __restrict__ u, const bf16_t* __restrict__ woutT, const bf16_t* __restrict__ x1,
    bf16_t* __restrict__ xout, float* __restrict__ xst TSP) {
  extern __shared__ __attribute__((aligned(16))) char smem[];
  const int tid = threadIdx.x;
  TS(0);
  const int xcd = blockIdx.x & 7, idx = blockIdx.x >> 3;
  const int mt = 8 * (xcd >> 1) + (idx & 7);
  const int nt = 4 * (xcd & 1) + (idx >> 3);
  const int t0 = 64 * mt, d0 = 80 * nt;
  const int r = tid >> 3, c = tid & 7;
  constexpr int PD = 4;
  u32x4 ra[PD][2], rb[PD][3];
  const bool b2 = tid < 128;
  auto loadS = [&](int slot, int kt) {
    ra[slot][0] = *(const u32x4*)(u + (size_t)(t0 + r) * FHP_ + 64 * kt + 8 * c);
    ra[slot][1] = *(const u32x4*)(u + (size_t)(t0 + r + 32) * FHP_ + 64 * kt + 8 * c);
    rb[slot][0] = *(const u32x4*)(woutT + (size_t)(d0 + r) * FHP_ + 64 * kt + 8 * c);
    rb[slot][1] = *(const u32x4*)(woutT + (size_t)(d0 + r + 32) * FHP_ + 64 * kt + 8 * c);
    if (b2) rb[slot][2] = *(const u32x4*)(woutT + (size_t)(d0 + r + 64) * FHP_ + 64 * kt + 8 * c);
  };
  auto writeS = [&](int slot, int buf) {
    const int sw = (c ^ (r & 7)) << 4;
    *(u32x4*)(smem + G3_OFF_A + buf * 8192 + r * 128 + sw) = ra[slot][0];
    *(u32x4*)(smem + G3_OFF_A + buf * 8192 + (r + 32) * 128 + sw) = ra[slot][1];
    *(u32x4*)(smem + G3_OFF_B + buf * 10240 + r * 128 + sw) = rb[slot][0];
    *(u32x4*)(smem + G3_OFF_B + buf * 10240 + (r + 32) * 128 + sw) = rb[slot][1];
    if (b2) *(u32x4*)(smem + G3_OFF_B + buf * 10240 + (r + 64) * 128 + sw) = rb[slot][2];
  };
#pragma unroll
  for (int d = 0; d < PD; ++d) loadS(d, d);
  writeS(0, 0);
  loadS(0, PD);
  __syncthreads();
  TS(1);
  const int wid = tid >> 6, lane = tid & 63, fr = lane & 15, g = lane >> 4;
  f32x4 acc[5] = {};
#pragma unroll
  for (int kt = 0; kt < 14; ++kt) {
    const int buf = kt & 1;
    if (kt + 1 < 14) {
      writeS((kt + 1) % PD, buf ^ 1);
      if (kt + 1 + PD < 14) loadS((kt + 1) % PD, kt + 1 + PD);
    }
#pragma unroll
    for (int ks = 0; ks < 2; ++ks) {
      const int rr = 16 * wid + fr;
      bf16x8 T = *(const bf16x8*)(smem + G3_OFF_A + buf * 8192 + rr * 128 + (((4 * ks + g) ^ (rr & 7)) << 4));
#pragma unroll
      for (int i = 0; i < 5; ++i) {
        const int wr = 16 * i + fr;
        bf16x8 W = *(const bf16x8*)(smem + G3_OFF_B + buf * 10240 + wr * 128 + (((4 * ks + g) ^ (wr & 7)) << 4));
        acc[i] = __builtin_amdgcn_mfma_f32_16x16x32_bf16(W, T, acc[i], 0, 0, 0);
      }
    }
    __syncthreads();
  }
  TS(2);
  const int t = t0 + 16 * wid + fr;
  float s0 = 0.f, s1 = 0.f;
#pragma unroll
  for (int i = 0; i < 5; ++i) {
    const int d = d0 + 16 * i + 4 * g;
    u32x2 xr = *(const u32x2*)(x1 + (size_t)t * D_ + d);
    u32x2 w = {pk2(lo16(xr[0]) + rbf(acc[i][0]), hi16(xr[0]) + rbf(acc[i][1])),
               pk2(lo16(xr[1]) + rbf(acc[i][2]), hi16(xr[1]) + rbf(acc[i][3]))};
    *(u32x2*)(xout + (size_t)t * D_ + d) = w;
    float a = lo16(w[0]), b = hi16(w[0]), cc = lo16(w[1]), dd = hi16(w[1]);
    s0 += a + b + cc + dd; s1 += a * a + b * b + cc * cc + dd * dd;
  }
  s0 += __shfl_xor(s0, 16); s0 += __shfl_xor(s0, 32);
  s1 += __shfl_xor(s1, 16); s1 += __shfl_xor(s1, 32);
  if (g == 0) {
    f32x2 o = {s0, s1};
    *(f32x2*)(xst + ((size_t)t * 8 + nt) * 2) = o;
  }
  TS(3);
}

extern "C" void kernel_launch(void* const* d_in, const int* in_sizes, int n_in,
                              void* d_out, int out_size, void* d_ws, size_t ws_size,
                              hipStream_t stream) {
  const bf16_t* q = (const bf16_t*)d_in[0];
  const bf16_t* k = (const bf16_t*)d_in[1];
  const bf16_t* v = (const bf16_t*)d_in[2];
  const bf16_t* shift_k = (const bf16_t*)d_in[3];
  const bf16_t* ssm_k = (const bf16_t*)d_in[4];
  const bf16_t* ssm_D = (const bf16_t*)d_in[5];
  const bf16_t* w_in = (const bf16_t*)d_in[7];
  const bf16_t* w_sp = (const bf16_t*)d_in[8];
  const bf16_t* b_sp = (const bf16_t*)d_in[9];
  const bf16_t* w_out = (const bf16_t*)d_in[10];
  bf16_t* out = (bf16_t*)d_out;

  char* ws = (char*)d_ws;
  size_t off = 0;
  auto alloc = [&](size_t bytes) { char* p = ws + off; off += (bytes + 255) & ~(size_t)255; return p; };
  bf16_t* winT = (bf16_t*)alloc((size_t)F_ * D_ * 2);
  float* cs = (float*)alloc(F_ * 4);
  bf16_t* woutT = (bf16_t*)alloc((size_t)D_ * FHP_ * 2);
  bf16_t* Y = (bf16_t*)alloc((size_t)S_ * D_ * 2);
  float* xst0 = (float*)alloc((size_t)S_ * 16 * 4);
  float* xst1 = (float*)alloc((size_t)S_ * 16 * 4);
  float* xst2 = (float*)alloc((size_t)S_ * 16 * 4);
  bf16_t* x1 = (bf16_t*)alloc((size_t)S_ * D_ * 2);
  bf16_t* x2 = (bf16_t*)alloc((size_t)S_ * D_ * 2);
  bf16_t* x3 = (bf16_t*)alloc((size_t)S_ * D_ * 2);
  bf16_t* z = (bf16_t*)alloc((size_t)S_ * F_ * 2 + 4096);
  float* zst = (float*)alloc((size_t)S_ * 8 * 4);
  bf16_t* u = (bf16_t*)alloc((size_t)S_ * FHP_ * 2);

  (void)hipFuncSetAttribute((const void*)k0_conv_prep, hipFuncAttributeMaxDynamicSharedMemorySize, CV_LDS);
  (void)hipFuncSetAttribute((const void*)g1_kernel, hipFuncAttributeMaxDynamicSharedMemorySize, G1_LDS);
  (void)hipFuncSetAttribute((const void*)g2_kernel, hipFuncAttributeMaxDynamicSharedMemorySize, G2_LDS);
  (void)hipFuncSetAttribute((const void*)g3_kernel, hipFuncAttributeMaxDynamicSharedMemorySize, G3_LDS);

  k0_conv_prep<<<256, 512, CV_LDS, stream>>>(q, k, v, shift_k, ssm_k, ssm_D, w_in, w_out, Y, winT, cs, woutT, xst0 TSA(0));
  g1_kernel<<<256, 512, G1_LDS, stream>>>(q, Y, xst0, winT, cs, x1, z, zst TSA(1));
  g2_kernel<<<224, 768, G2_LDS, stream>>>(w_sp, z, zst, b_sp, u TSA(2));
  g3_kernel<<<256, 256, G3_LDS, stream>>>(u, woutT, x1, x2, xst1 TSA(3));
  g1_kernel<<<256, 512, G1_LDS, stream>>>(x2, Y, xst1, winT, cs, x3, z, zst TSA(4));
  g2_kernel<<<224, 768, G2_LDS, stream>>>(w_sp, z, zst, b_sp, u TSA(5));
  g3_kernel<<<256, 256, G3_LDS, stream>>>(u, woutT, x3, out, xst2 TSA(6));
}
```

```cpp
#include <hip/hip_runtime.h>
#include <hip/hip_bf16.h>
#include <stdint.h>

typedef __attribute__((ext_vector_type(8))) short bf16x8;
typedef __attribute__((ext_vector_type(4))) short s16x4;
typedef __attribute__((ext_vector_type(4))) float f32x4;
typedef __attribute__((ext_vector_type(16))) float f32x16;
typedef __attribute__((ext_vector_type(2))) float f32x2;
typedef __attribute__((ext_vector_type(2))) __bf16 bf16x2_t;
typedef __attribute__((ext_vector_type(4))) uint32_t u32x4;
typedef __attribute__((ext_vector_type(2))) uint32_t u32x2;
typedef uint16_t bf16_t;

#define TSP
#define TSA(k)
#define TS(i)

constexpr int S_ = 2048, D_ = 640, F_ = 1728, FH_ = 864, FHP_ = 896;

__device__ __forceinline__ uint32_t pk2(float lo, float hi) {
  f32x2 f = {lo, hi};
  bf16x2_t b = __builtin_convertvector(f, bf16x2_t);
  return __builtin_bit_cast(uint32_t, b);
}
__device__ __forceinline__ float lo16(uint32_t v) { return __uint_as_float(v << 16); }
__device__ __forceinline__ float hi16(uint32_t v) { return __uint_as_float(v & 0xffff0000u); }
__device__ __forceinline__ float rbf(float x) { return lo16(pk2(x, x)); }
__device__ __forceinline__ float bf2f(bf16_t v) { return __uint_as_float(((uint32_t)v) << 16); }

__device__ __forceinline__ f32x2 ln_stats(float sum, float sumsq, float inv_n) {
  float mean = sum * inv_n;
  float var = fmaxf(sumsq * inv_n - mean * mean, 0.f);
  float mb = rbf(mean);
  float vb = rbf(var);
  float sq = rbf(sqrtf(rbf(vb + 1e-5f)));
  f32x2 r = {mb, 1.0f / sq};
  return r;
}

constexpr int CV_HP = 4416;
constexpr int CV_HCH = 4 * CV_HP;
constexpr int CV_UCH = 96 * 80;
constexpr int CV_OFF_H = 0;
constexpr int CV_OFF_U = CV_OFF_H + 4 * CV_HCH;
constexpr int CV_OFF_R = CV_OFF_U + 4 * CV_UCH;
constexpr int CV_OFF_Y = CV_OFF_R + 4 * 8192;
constexpr int CV_LDS = CV_OFF_Y + 2048 * 8;

__global__ __launch_bounds__(512, 2) void k0_conv_prep(
    const bf16_t* __restrict__ q, const bf16_t* __restrict__ kin, const bf16_t* __restrict__ vin,
    const bf16_t* __restrict__ shift_k, const bf16_t* __restrict__ ssm_k, const bf16_t* __restrict__ ssm_D,
    const bf16_t* __restrict__ w_in, const bf16_t* __restrict__ w_out,
    bf16_t* __restrict__ Yout, bf16_t* __restrict__ winT, float* __restrict__ cs, bf16_t* __restrict__ woutT,
    float* __restrict__ xst TSP) {
  extern __shared__ __attribute__((aligned(16))) char smem[];
  const int tid = threadIdx.x;
  TS(0);
  const int xcd = blockIdx.x & 7, bi = blockIdx.x >> 3;
  if (bi >= 20) {
    const int j = (bi - 20) * 8 + xcd;
    const int r = tid >> 3, c = tid & 7;
    if (j < 27) {
      const int f0 = 64 * j;
      bf16_t* T = (bf16_t*)smem;
      u32x4 v[10];
#pragma unroll
      for (int it = 0; it < 10; ++it) v[it] = *(const u32x4*)(w_in + (size_t)(64 * it + r) * F_ + f0 + 8 * c);
#pragma unroll
      for (int it = 0; it < 10; ++it) {
#pragma unroll
        for (int i = 0; i < 4; ++i) {
          T[(8 * c + 2 * i) * 648 + 64 * it + r] = (bf16_t)(v[it][i] & 0xffff);
          T[(8 * c + 2 * i + 1) * 648 + 64 * it + r] = (bf16_t)(v[it][i] >> 16);
        }
      }
      __syncthreads();
      float sum = 0.f;
#pragma unroll
      for (int it = 0; it < 10; ++it) {
        u32x4 w = *(const u32x4*)(T + r * 648 + 64 * it + 8 * c);
        *(u32x4*)(winT + (size_t)(f0 + r) * D_ + 64 * it + 8 * c) = w;
#pragma unroll
        for (int i = 0; i < 4; ++i) sum += lo16(w[i]) + hi16(w[i]);
      }
      sum += __shfl_xor(sum, 1); sum += __shfl_xor(sum, 2); sum += __shfl_xor(sum, 4);
      if (c == 0) cs[f0 + r] = sum;
    } else if (j < 41) {
      const int f0 = 64 * (j - 27);
      bf16_t* T = (bf16_t*)smem;
      const bool valid = (f0 + r) < FH_;
      u32x4 v[10];
#pragma unroll
      for (int it = 0; it < 10; ++it) {
        u32x4 z = {0u, 0u, 0u, 0u};
        v[it] = valid ? *(const u32x4*)(w_out + (size_t)(f0 + r) * D_ + 64 * it + 8 * c) : z;
      }
#pragma unroll
      for (int it = 0; it < 10; ++it) {
#pragma unroll
        for (int i = 0; i < 4; ++i) {
          T[(64 * it + 8 * c + 2 * i) * 72 + r] = (bf16_t)(v[it][i] & 0xffff);
          T[(64 * it + 8 * c + 2 * i + 1) * 72 + r] = (bf16_t)(v[it][i] >> 16);
        }
      }
      __syncthreads();
#pragma unroll
      for (int it = 0; it < 10; ++it) {
        const int d = 64 * it + r;
        u32x4 w = *(const u32x4*)(T + d * 72 + 8 * c);
        *(u32x4*)(woutT + (size_t)d * FHP_ + f0 + 8 * c) = w;
      }
    } else if (j < 73) {
      const int row = 64 * (j - 41) + r;
      float sum = 0.f, sq = 0.f;
#pragma unroll
      for (int it = 0; it < 10; ++it) {
        u32x4 w = *(const u32x4*)(q + (size_t)row * D_ + 64 * it + 8 * c);
#pragma unroll
        for (int i = 0; i < 4; ++i) {
          float a = lo16(w[i]), b = hi16(w[i]);
          sum += a + b; sq += a * a + b * b;
        }
      }
      sum += __shfl_xor(sum, 1); sum += __shfl_xor(sum, 2); sum += __shfl_xor(sum, 4);
      sq += __shfl_xor(sq, 1); sq += __shfl_xor(sq, 2); sq += __shfl_xor(sq, 4);
      f32x2 o = {c == 0 ? sum : 0.f, c == 0 ? sq : 0.f};
      *(f32x2*)(xst + ((size_t)row * 8 + c) * 2) = o;
    }
    return;
  }
  const int g = xcd * 20 + bi;
  const int c0 = 4 * g;
  u32x2 kr[7], vr[4];
#pragma unroll
  for (int i = 0; i < 7; ++i) {
    int s = 4 * tid - 3 + i;
    u32x2 z = {0u, 0u};
    kr[i] = (s >= 0) ? *(const u32x2*)(kin + (size_t)s * D_ + c0) : z;
  }
#pragma unroll
  for (int i = 0; i < 4; ++i) vr[i] = *(const u32x2*)(vin + (size_t)(4 * tid + i) * D_ + c0);
  u32x2 hr[3][3];
#pragma unroll
  for (int rd = 0; rd < 3; ++rd) {
    int task = tid + 512 * rd;
    int ch = task / 262, c = task % 262 - 6;
#pragma unroll
    for (int i = 0; i < 3; ++i) {
      int idx = 8 * c + 4 * i;
      u32x2 z = {0u, 0u};
      hr[rd][i] = (task < 1048 && idx >= 0 && idx < 2048) ? *(const u32x2*)(ssm_k + (size_t)(c0 + ch) * S_ + idx) : z;
    }
  }
  float wsh[4][4];
#pragma unroll
  for (int ch = 0; ch < 4; ++ch) {
    u32x2 w = *(const u32x2*)(shift_k + (c0 + ch) * 4);
    wsh[ch][0] = lo16(w[0]); wsh[ch][1] = hi16(w[0]); wsh[ch][2] = lo16(w[1]); wsh[ch][3] = hi16(w[1]);
  }
  for (int i = tid; i < 4 * 160; i += 512) {
    int ch = i / 160, o = i % 160;
    u32x4 z = {0u, 0u, 0u, 0u};
    *(u32x4*)(smem + CV_OFF_U + ch * CV_UCH + o * 16) = z;
  }
  TS(1);
#pragma unroll
  for (int ch = 0; ch < 4; ++ch) {
    float kv[4];
#pragma unroll
    for (int i = 0; i < 4; ++i) {
      float kk[4];
#pragma unroll
      for (int t = 0; t < 4; ++t) {
        uint32_t w = kr[i + t][ch >> 1];
        kk[t] = (ch & 1) ? hi16(w) : lo16(w);
      }
      float ks = rbf(kk[0] * wsh[ch][3]);
      ks = rbf(ks + rbf(kk[1] * wsh[ch][2]));
      ks = rbf(ks + rbf(kk[2] * wsh[ch][1]));
      ks = rbf(ks + rbf(kk[3] * wsh[ch][0]));
      uint32_t w = vr[i][ch >> 1];
      float vv = (ch & 1) ? hi16(w) : lo16(w);
      kv[i] = ks * vv;
    }
    u32x2 o = {pk2(kv[0], kv[1]), pk2(kv[2], kv[3])};
    int s = 4 * tid;
    *(u32x2*)(smem + CV_OFF_U + ch * CV_UCH + ((s >> 5) + 32) * 80 + (s & 31) * 2) = o;
  }
#pragma unroll
  for (int rd = 0; rd < 3; ++rd) {
    int task = tid + 512 * rd;
    if (task < 1048) {
      int ch = task / 262, c = task % 262 - 6;
      uint32_t w[6] = {hr[rd][0][0], hr[rd][0][1], hr[rd][1][0], hr[rd][1][1], hr[rd][2][0], hr[rd][2][1]};
#pragma unroll
      for (int p = 0; p < 4; ++p) {
#pragma unroll
        for (int gi = 0; gi < 2; ++gi) {
          const int ao = ((1 - p) & 3) + 4 * gi;
          int a = 8 * c + ao;
          int qx = 2045 - p - a;
          auto el = [&](int e) -> uint32_t { return (e & 1) ? (w[e >> 1] >> 16) : (w[e >> 1] & 0xffff); };
          u32x2 o = {el(ao + 3) | (el(ao + 2) << 16), el(ao + 1) | (el(ao) << 16)};
          if (qx >= 0 && qx < 2088) *(u32x2*)(smem + CV_OFF_H + ch * CV_HCH + p * CV_HP + qx * 2) = o;
        }
      }
    }
  }
  TS(2);
  __syncthreads();
  TS(3);
  const int wid = tid >> 6, lane = tid & 63;
  const int ch = wid & 3, kb = wid >> 2;
  const int m = lane & 31, hh = lane >> 5;
  {
    const int p = (4 - (m & 3)) & 3;
    const char* abase = smem + CV_OFF_H + ch * CV_HCH + p * CV_HP + (2048 - m + 8 * hh - p + 16 * kb) * 2;
    const char* bbase = smem + CV_OFF_U + ch * CV_UCH + (m + 32) * 80 + 32 * kb + 16 * hh;
    f32x16 acc0 = {}, acc1 = {};
#pragma unroll 4
    for (int dl = 0; dl < 32; ++dl) {
      u32x2 a00 = *(const volatile u32x2*)(abase - 64 * dl);
      u32x2 a01 = *(const volatile u32x2*)(abase - 64 * dl + 8);
      u32x2 a10 = *(const volatile u32x2*)(abase - 64 * (dl + 32));
      u32x2 a11 = *(const volatile u32x2*)(abase - 64 * (dl + 32) + 8);
      bf16x8 B0 = *(const bf16x8*)(bbase - 80 * dl);
      bf16x8 B1 = *(const bf16x8*)(bbase + 80 * (32 - dl));
      u32x4 A0u = {a00[0], a00[1], a01[0], a01[1]};
      u32x4 A1u = {a10[0], a10[1], a11[0], a11[1]};
      bf16x8 A0 = __builtin_bit_cast(bf16x8, A0u), A1 = __builtin_bit_cast(bf16x8, A1u);
      acc0 = __builtin_amdgcn_mfma_f32_32x32x16_bf16(A0, B0, acc0, 0, 0, 0);
      acc1 = __builtin_amdgcn_mfma_f32_32x32x16_bf16(A0, B1, acc1, 0, 0, 0);
      acc1 = __builtin_amdgcn_mfma_f32_32x32x16_bf16(A1, B0, acc1, 0, 0, 0);
    }
    TS(4);
    float* red = (float*)(smem + CV_OFF_R + ch * 8192);
    if (kb == 1) {
#pragma unroll
      for (int r = 0; r < 16; ++r) { red[r * 64 + lane] = acc0[r]; red[(16 + r) * 64 + lane] = acc1[r]; }
    }
    __syncthreads();
    if (kb == 0) {
      const float Dv = bf2f(ssm_D[c0 + ch]);
      bf16_t* yb = (bf16_t*)(smem + CV_OFF_Y);
#pragma unroll
      for (int half = 0; half < 2; ++half) {
        const int a = m + 32 * half;
#pragma unroll
        for (int rq = 0; rq < 4; ++rq) {
          const int b0 = 8 * rq + 4 * hh;
          u32x2 kvw = *(const u32x2*)(smem + CV_OFF_U + ch * CV_UCH + (a + 32) * 80 + b0 * 2);
          float kvv[4] = {lo16(kvw[0]), hi16(kvw[0]), lo16(kvw[1]), hi16(kvw[1])};
#pragma unroll
          for (int i = 0; i < 4; ++i) {
            const int r = 4 * rq + i;
            float y = (half ? acc1[r] : acc0[r]) + red[(16 * half + r) * 64 + lane];
            float o = rbf(y) + rbf(kvv[i] * Dv);
            yb[(32 * a + b0 + i) * 4 + ch] = (bf16_t)(pk2(o, o) & 0xffff);
          }
        }
      }
    }
    __syncthreads();
    TS(5);
#pragma unroll
    for (int i = 0; i < 4; ++i) {
      int s = tid + 512 * i;
      u32x2 y = *(const u32x2*)(smem + CV_OFF_Y + s * 8);
      *(u32x2*)(Yout + (size_t)s * D_ + c0) = y;
    }
    TS(6);
  }
}

constexpr int G1_OFF_A = 0;
constexpr int G1_OFF_B = 81920;
constexpr int G1_OFF_RS = G1_OFF_B + 57344;
constexpr int G1_OFF_ZS = G1_OFF_RS + 512;
constexpr int G1_LDS = G1_OFF_ZS + 2048;

__device__ __forceinline__ float gelu_tanh(float x) {
  float y = 0.7978845608028654f * (x + 0.044715f * x * x * x);
  float e = __expf(2.f * y);
  return x - x * __frcp_rn(1.f + e);
}

__global__ __launch_bounds__(512, 2) void g1_kernel(
    const bf16_t* __restrict__ x0, const bf16_t* __restrict__ Yb, const float* __restrict__ xst,
    const bf16_t* __restrict__ winT, const float* __restrict__ cs,
    bf16_t* __restrict__ x1out, bf16_t* __restrict__ z, float* __restrict__ zst TSP) {
  extern __shared__ __attribute__((aligned(16))) char smem[];
  const int tid = threadIdx.x;
  TS(0);
  const int xcd = blockIdx.x & 7, idx = blockIdx.x >> 3;
  const int mt = 8 * (xcd >> 1) + (idx & 7);
  const int nt = 4 * (xcd & 1) + (idx >> 3);
  const int fs = (27 * nt + 1) >> 1;
  const int nf = ((27 * (nt + 1) + 1) >> 1) - fs;
  const int m0 = 64 * mt;
  const int r = tid >> 3, c = tid & 7;
  u32x4 xa[10], ya[10];
#pragma unroll
  for (int kt = 0; kt < 10; ++kt) {
    xa[kt] = *(const u32x4*)(x0 + (size_t)(m0 + r) * D_ + 64 * kt + 8 * c);
    ya[kt] = *(const u32x4*)(Yb + (size_t)(m0 + r) * D_ + 64 * kt + 8 * c);
  }
  float s0 = 0.f, s1 = 0.f;
  {
    const f32x4* p = (const f32x4*)(xst + (size_t)(m0 + r) * 16);
#pragma unroll
    for (int i = 0; i < 4; ++i) { f32x4 v = p[i]; s0 += v[0] + v[2]; s1 += v[1] + v[3]; }
  }
  const int nrows = 16 * nf;
  int brow[4];
#pragma unroll
  for (int i = 0; i < 4; ++i) { int rr = r + 64 * i; brow[i] = rr < nrows ? rr : nrows - 1; }
  const bool b3 = tid < 256;
  constexpr int PD = 3;
  u32x4 wb[PD][4];
  auto loadB = [&](int slot, int kt) {
#pragma unroll
    for (int i = 0; i < 4; ++i)
      if (i < 3 || b3) wb[slot][i] = *(const u32x4*)(winT + (size_t)(16 * fs + brow[i]) * D_ + 64 * kt + 8 * c);
  };
  auto writeB = [&](int slot, int buf) {
#pragma unroll
    for (int i = 0; i < 4; ++i) {
      int rr = r + 64 * i;
      if (i < 3 || b3) *(u32x4*)(smem + G1_OFF_B + buf * 28672 + rr * 128 + ((c ^ (rr & 7)) << 4)) = wb[slot][i];
    }
  };
#pragma unroll
  for (int d = 0; d < PD; ++d) loadB(d, d);

  const f32x2 st = ln_stats(s0, s1, 1.0f / D_);
  TS(1);
  float t0 = 0.f, t1 = 0.f;
#pragma unroll
  for (int kt = 0; kt < 10; ++kt) {
    u32x4 o;
#pragma unroll
    for (int i = 0; i < 4; ++i) {
      float a = lo16(xa[kt][i]), b = hi16(xa[kt][i]);
      float ya_ = lo16(ya[kt][i]), yb_ = hi16(ya[kt][i]);
      float da = rbf(rbf(a - st[0]) * st[1]), db = rbf(rbf(b - st[0]) * st[1]);
      float ta = rbf(da * ya_), tb = rbf(db * yb_);
      uint32_t w = pk2(a + ta, b + tb);
      o[i] = w;
      float xa_ = lo16(w), xb_ = hi16(w);
      t0 += xa_ + xb_; t1 += xa_ * xa_ + xb_ * xb_;
    }
    *(u32x4*)(smem + G1_OFF_A + kt * 8192 + r * 128 + ((c ^ (r & 7)) << 4)) = o;
    if (nt == 0) *(u32x4*)(x1out + (size_t)(m0 + r) * D_ + 64 * kt + 8 * c) = o;
  }
  t0 += __shfl_xor(t0, 1); t0 += __shfl_xor(t0, 2); t0 += __shfl_xor(t0, 4);
  t1 += __shfl_xor(t1, 1); t1 += __shfl_xor(t1, 2); t1 += __shfl_xor(t1, 4);
  if (c == 0) {
    f32x2 s = ln_stats(t0, t1, 1.0f / D_);
    *(f32x2*)(smem + G1_OFF_RS + r * 8) = s;
  }
  TS(2);
  writeB(0, 0);
  loadB(0, PD);
  __syncthreads();
  TS(3);

  const int wid = tid >> 6, lane = tid & 63, fr = lane & 15, g = lane >> 4;
  const int wm = wid & 1, wn = wid >> 1;
  f32x4 acc[4][2] = {};
  int wrow[4];
#pragma unroll
  for (int i = 0; i < 4; ++i) { int fi = 4 * wn + i; wrow[i] = 16 * (fi < nf ? fi : nf - 1) + fr; }
#pragma unroll
  for (int kt = 0; kt < 10; ++kt) {
    const int buf = kt & 1;
    if (kt + 1 < 10) {
      writeB((kt + 1) % PD, buf ^ 1);
      if (kt + 1 + PD < 10) loadB((kt + 1) % PD, kt + 1 + PD);
    }
#pragma unroll
    for (int ks = 0; ks < 2; ++ks) {
      bf16x8 X[2], W[4];
#pragma unroll
      for (int i = 0; i < 2; ++i) {
        int rr = 32 * wm + 16 * i + fr;
        X[i] = *(const bf16x8*)(smem + G1_OFF_A + kt * 8192 + rr * 128 + (((4 * ks + g) ^ (rr & 7)) << 4));
      }
#pragma unroll
      for (int i = 0; i < 4; ++i)
        W[i] = *(const bf16x8*)(smem + G1_OFF_B + buf * 28672 + wrow[i] * 128 + (((4 * ks + g) ^ (wrow[i] & 7)) << 4));
#pragma unroll
      for (int i = 0; i < 4; ++i)
#pragma unroll
        for (int j = 0; j < 2; ++j) acc[i][j] = __builtin_amdgcn_mfma_f32_16x16x32_bf16(W[i], X[j], acc[i][j], 0, 0, 0);
    }
    __syncthreads();
  }
  TS(4);
  float zs0[2] = {0.f, 0.f}, zs1[2] = {0.f, 0.f};
#pragma unroll
  for (int i = 0; i < 4; ++i) {
    const int fi = 4 * wn + i;
    if (fi < nf) {
      const int f = 16 * (fs + fi) + 4 * g;
      f32x4 csv = *(const f32x4*)(cs + f);
#pragma unroll
      for (int j = 0; j < 2; ++j) {
        const int rr = 32 * wm + 16 * j + fr;
        f32x2 rs = *(const f32x2*)(smem + G1_OFF_RS + rr * 8);
        float o[4];
#pragma unroll
        for (int e = 0; e < 4; ++e) {
          float pre = rbf((acc[i][j][e] - rs[0] * csv[e]) * rs[1]);
          o[e] = gelu_tanh(pre);
        }
        u32x2 w = {pk2(o[0], o[1]), pk2(o[2], o[3])};
        *(u32x2*)(z + (size_t)(m0 + rr) * F_ + f) = w;
        if (nt >= 4) {
          float a = lo16(w[0]), b = hi16(w[0]), cc = lo16(w[1]), d = hi16(w[1]);
          zs0[j] += a + b + cc + d; zs1[j] += a * a + b * b + cc * cc + d * d;
        }
      }
    }
  }
  if (nt >= 4) {
#pragma unroll
    for (int j = 0; j < 2; ++j) {
      zs0[j] += __shfl_xor(zs0[j], 16); zs0[j] += __shfl_xor(zs0[j], 32);
      zs1[j] += __shfl_xor(zs1[j], 16); zs1[j] += __shfl_xor(zs1[j], 32);
      if (g == 0) {
        f32x2 o = {zs0[j], zs1[j]};
        *(f32x2*)(smem + G1_OFF_ZS + (wn * 64 + 32 * wm + 16 * j + fr) * 8) = o;
      }
    }
    __syncthreads();
    if (tid < 64) {
      float a = 0.f, b = 0.f;
#pragma unroll
      for (int w = 0; w < 4; ++w) { f32x2 v = *(const f32x2*)(smem + G1_OFF_ZS + (w * 64 + tid) * 8); a += v[0]; b += v[1]; }
      f32x2 o = {a, b};
      *(f32x2*)(zst + ((size_t)(m0 + tid) * 4 + (nt - 4)) * 2) = o;
    }
  }
  TS(5);
}

constexpr int G2_NS = 6;
constexpr int G2_OFF_A = 0;
constexpr int G2_OFF_B = G2_NS * 16384;
constexpr int G2_OFF_ST = G2_OFF_B + 3 * 8192;
constexpr int G2_LDS = G2_OFF_ST + 16384;

__device__ __forceinline__ void glds16(const void* gsrc, uint32_t lds_dst) {
  uint32_t keep;
  asm volatile("s_mov_b32 %0, m0\n\ts_mov_b32 m0, %2\n\ts_nop 0\n\tglobal_load_lds_dwordx4 %1, off\n\ts_mov_b32 m0, %0"
               : "=&s"(keep) : "v"(gsrc), "s"(lds_dst) : "memory");
}
__device__ __forceinline__ uint32_t lds_addr(const void* p) {
  return (uint32_t)(uintptr_t)(const __attribute__((address_space(3))) char*)p;
}
__device__ __forceinline__ void touch(const void* p, uint32_t& sink) {
  asm volatile("global_load_ushort %0, %1, off" : "+v"(sink) : "v"(p) : "memory");
}
#define WAIT_VM(n) asm volatile("s_waitcnt vmcnt(" #n ")" ::: "memory")
#define WAIT_LGKM0() asm volatile("s_waitcnt lgkmcnt(0)" ::: "memory")
#define RAW_BAR() __builtin_amdgcn_s_barrier()

__global__ __launch_bounds__(768, 1) void g2_kernel(
    const bf16_t* __restrict__ wsp, const bf16_t* __restrict__ z, const float* __restrict__ zst,
    const bf16_t* __restrict__ bsp, bf16_t* __restrict__ u TSP) {
  extern __shared__ __attribute__((aligned(16))) char smem[];
  const int tid = threadIdx.x;
  TS(0);
  const int xcd = blockIdx.x & 7, idx = blockIdx.x >> 3;
  const int mt = 4 * (xcd >> 1) + (idx & 3);
  const int nt = 7 * (xcd & 1) + (idx >> 2);
  const int t0 = 128 * mt, f0 = 64 * nt;
  const int wid = __builtin_amdgcn_readfirstlane(tid >> 6), lane = tid & 63;
  const int rot = idx;
  if (wid >= 8) {
    const int pt = tid - 512;
    const int r = pt >> 3, c = pt & 7;
    constexpr int PD = 8;
    u32x4 rb[PD][2];
    const bf16_t* zp = z + (size_t)r * F_ + FH_ + f0 + 8 * c;
    auto loadB = [&](int slot, int kt) {
      const int ke = (kt + rot) & 31;
      rb[slot][0] = *(const u32x4*)(zp + (size_t)(64 * ke) * F_);
      rb[slot][1] = *(const u32x4*)(zp + (size_t)(64 * ke + 32) * F_);
    };
    const int bswz0 = (((r >> 1) & 1) | (((r >> 3) & 1) << 1)) << 1;
    u32x4 nb[2];
    auto calcB = [&](int slot, int kt) {
#pragma unroll
      for (int h = 0; h < 2; ++h) {
        f32x2 st = *(const f32x2*)(smem + G2_OFF_ST + (64 * ((kt + rot) & 31) + r + 32 * h) * 8);
#pragma unroll
        for (int i = 0; i < 4; ++i) {
          float a = lo16(rb[slot][h][i]), b = hi16(rb[slot][h][i]);
          nb[h][i] = pk2(fmaf(a, st[0], st[1]), fmaf(b, st[0], st[1]));
        }
      }
    };
    auto putB = [&](int bslot) {
#pragma unroll
      for (int h = 0; h < 2; ++h)
        *(u32x4*)(smem + G2_OFF_B + bslot * 8192 + (r + 32 * h) * 128 + ((c ^ bswz0) << 4)) = nb[h];
    };
    {
      f32x4 sv[16];
      const f32x4* p = (const f32x4*)(zst + (size_t)pt * 64);
#pragma unroll
      for (int i = 0; i < 16; ++i) sv[i] = p[i];
#pragma unroll
      for (int i = 0; i < 8; ++i) {
        f32x4 a = sv[2 * i], b = sv[2 * i + 1];
        f32x2 s = ln_stats(a[0] + a[2] + b[0] + b[2], a[1] + a[3] + b[1] + b[3], 1.0f / FH_);
        f32x2 s2 = {s[1], -s[0] * s[1]};
        *(f32x2*)(smem + G2_OFF_ST + (8 * pt + i) * 8) = s2;
      }
    }
#pragma unroll
    for (int d = 0; d < PD; ++d) loadB(d, d);
    WAIT_LGKM0();
    RAW_BAR();
    calcB(0, 0);
    putB(0);
    loadB(0, PD);
    calcB(1, 1);
    loadB(1, PD + 1);
    WAIT_LGKM0();
    RAW_BAR();
    putB(1);
    calcB(2, 2);
    loadB(2, PD + 2);
    WAIT_LGKM0();
    int bs = 2;
#pragma unroll 1
    for (int kt0 = 0; kt0 < 32; kt0 += PD) {
#pragma unroll
      for (int kk = 0; kk < PD; ++kk) {
        const int kt = kt0 + kk;
        RAW_BAR();
        if (kt + 2 < 32) putB(bs);
        if (kt + 3 < 32) {
          calcB((kk + 3) % PD, kt + 3);
          if (kt + 3 + PD < 32) loadB((kk + 3) % PD, kt + 3 + PD);
        }
        bs = bs == 2 ? 0 : bs + 1;
        WAIT_LGKM0();
      }
    }
    return;
  }
  const int lr = lane >> 3, lc = (lane & 7) ^ lr;
  const bf16_t* ap0 = wsp + (size_t)(t0 + 16 * wid + lr) * S_ + 8 * lc;
  const bf16_t* ap1 = ap0 + (size_t)8 * S_;
  const uint32_t lbase = lds_addr(smem) + G2_OFF_A + wid * 2048;
  auto issueA = [&](int kt, int slot) {
    const int ke = (kt + rot) & 31;
    glds16(ap0 + 64 * ke, lbase + slot * 16384);
    glds16(ap1 + 64 * ke, lbase + slot * 16384 + 1024);
  };
#pragma unroll
  for (int s = 0; s < G2_NS - 1; ++s) issueA(s, s);
  RAW_BAR();
  TS(1);
  const int fr = lane & 15, g = lane >> 4;
  const int wm = wid & 3, wn = wid >> 2;
  f32x4 acc[2][2] = {};
  const int q4 = (lane & 15) >> 2, p4 = lane & 3;
  int aoff[2][2], boff[2][2][2];
#pragma unroll
  for (int ks = 0; ks < 2; ++ks) {
#pragma unroll
    for (int i = 0; i < 2; ++i) {
      int rr = 32 * wm + 16 * i + fr;
      aoff[ks][i] = G2_OFF_A + rr * 128 + (((4 * ks + g) ^ (rr & 7)) << 4);
#pragma unroll
      for (int t = 0; t < 2; ++t) {
        int row = 32 * ks + 8 * g + 4 * t + q4;
        int ch = 2 * (2 * wn + i) + (p4 >> 1);
        int sw = (((row >> 1) & 1) | (((row >> 3) & 1) << 1)) << 1;
        boff[ks][i][t] = G2_OFF_B + row * 128 + ((ch ^ sw) << 4) + 8 * (p4 & 1);
      }
    }
  }
  auto readF = [&](bf16x8* T, bf16x8* Z, int ks, int aslot, int bslot) {
#pragma unroll
    for (int i = 0; i < 2; ++i) T[i] = *(const bf16x8*)(smem + aslot * 16384 + aoff[ks][i]);
#pragma unroll
    for (int i = 0; i < 2; ++i) {
      typedef __attribute__((address_space(3))) s16x4 lds_s16x4;
      s16x4 h0 = __builtin_amdgcn_ds_read_tr16_b64_v4i16((lds_s16x4*)(smem + bslot * 8192 + boff[ks][i][0]));
      s16x4 h1 = __builtin_amdgcn_ds_read_tr16_b64_v4i16((lds_s16x4*)(smem + bslot * 8192 + boff[ks][i][1]));
      Z[i] = __builtin_shufflevector(h0, h1, 0, 1, 2, 3, 4, 5, 6, 7);
    }
  };
  auto mm = [&](const bf16x8* T, const bf16x8* Z) {
#pragma unroll
    for (int i = 0; i < 2; ++i)
#pragma unroll
      for (int j = 0; j < 2; ++j) acc[i][j] = __builtin_amdgcn_mfma_f32_16x16x32_bf16(Z[i], T[j], acc[i][j], 0, 0, 0);
  };
  bf16x8 TA[2][2], ZA[2][2], TB[2][2], ZB[2][2];
  WAIT_VM(8);
  RAW_BAR();
  readF(TA[0], ZA[0], 0, 0, 0);
  readF(TA[1], ZA[1], 1, 0, 0);
  int slot = 0, bs = 0;
  auto step = [&](int kt, bf16x8 (*Tc)[2], bf16x8 (*Zc)[2], bf16x8 (*Tn)[2], bf16x8 (*Zn)[2]) {
    WAIT_VM(6);
    RAW_BAR();
    {
      int kn = kt + G2_NS - 1; kn = kn < 32 ? kn : 31;
      int sn = slot == 0 ? G2_NS - 1 : slot - 1;
      issueA(kn, sn);
    }
    const int slot1 = slot + 1 == G2_NS ? 0 : slot + 1;
    const int bs1 = bs == 2 ? 0 : bs + 1;
    if (wn == 0) {
      readF(Tn[0], Zn[0], 0, slot1, bs1);
      readF(Tn[1], Zn[1], 1, slot1, bs1);
      __builtin_amdgcn_sched_barrier(0);
      mm(Tc[0], Zc[0]);
      mm(Tc[1], Zc[1]);
    } else {
      mm(Tc[0], Zc[0]);
      mm(Tc[1], Zc[1]);
      __builtin_amdgcn_sched_barrier(0);
      readF(Tn[0], Zn[0], 0, slot1, bs1);
      readF(Tn[1], Zn[1], 1, slot1, bs1);
    }
    __builtin_amdgcn_sched_barrier(0);
    slot = slot1; bs = bs1;
  };
#pragma unroll 1
  for (int kt = 0; kt < 32; kt += 2) {
    step(kt, TA, ZA, TB, ZB);
    step(kt + 1, TB, ZB, TA, ZA);
  }
  WAIT_VM(0);
  TS(3);
#pragma unroll
  for (int j = 0; j < 2; ++j) {
    const int t = t0 + 32 * wm + 16 * j + fr;
    const float bb = bf2f(bsp[t]);
#pragma unroll
    for (int i = 0; i < 2; ++i) {
      const int f = f0 + 32 * wn + 16 * i + 4 * g;
      u32x2 w = {0u, 0u};
      if (f < FH_) {
        u32x2 z1 = *(const u32x2*)(z + (size_t)t * F_ + f);
        float g0 = rbf(rbf(acc[i][j][0]) + bb), g1 = rbf(rbf(acc[i][j][1]) + bb);
        float g2 = rbf(rbf(acc[i][j][2]) + bb), g3 = rbf(rbf(acc[i][j][3]) + bb);
        w[0] = pk2(lo16(z1[0]) * g0, hi16(z1[0]) * g1);
        w[1] = pk2(lo16(z1[1]) * g2, hi16(z1[1]) * g3);
      }
      *(u32x2*)(u + (size_t)t * FHP_ + f) = w;
    }
  }
  TS(4);
}

constexpr int G3_OFF_A = 0;
constexpr int G3_OFF_B = 16384;
constexpr int G3_LDS = 36864;

__global__ __launch_bounds__(256, 1) void g3_kernel(
    const bf16_t* __restrict__ u, const bf16_t* __restrict__ woutT, const bf16_t* __restrict__ x1,
    bf16_t* __restrict__ xout, float* __restrict__ xst TSP) {
  extern __shared__ __attribute__((aligned(16))) char smem[];
  const int tid = threadIdx.x;
  TS(0);
  const int xcd = blockIdx.x & 7, idx = blockIdx.x >> 3;
  const int mt = 8 * (xcd >> 1) + (idx & 7);
  const int nt = 4 * (xcd & 1) + (idx >> 3);
  const int t0 = 64 * mt, d0 = 80 * nt;
  const int r = tid >> 3, c = tid & 7;
  constexpr int PD = 4;
  u32x4 ra[PD][2], rb[PD][3];
  const bool b2 = tid < 128;
  auto loadS = [&](int slot, int kt) {
    ra[slot][0] = *(const u32x4*)(u + (size_t)(t0 + r) * FHP_ + 64 * kt + 8 * c);
    ra[slot][1] = *(const u32x4*)(u + (size_t)(t0 + r + 32) * FHP_ + 64 * kt + 8 * c);
    rb[slot][0] = *(const u32x4*)(woutT + (size_t)(d0 + r) * FHP_ + 64 * kt + 8 * c);
    rb[slot][1] = *(const u32x4*)(woutT + (size_t)(d0 + r + 32) * FHP_ + 64 * kt + 8 * c);
    if (b2) rb[slot][2] = *(const u32x4*)(woutT + (size_t)(d0 + r + 64) * FHP_ + 64 * kt + 8 * c);
  };
  auto writeS = [&](int slot, int buf) {
    const int sw = (c ^ (r & 7)) << 4;
    *(u32x4*)(smem + G3_OFF_A + buf * 8192 + r * 128 + sw) = ra[slot][0];
    *(u32x4*)(smem + G3_OFF_A + buf * 8192 + (r + 32) * 128 + sw) = ra[slot][1];
    *(u32x4*)(smem + G3_OFF_B + buf * 10240 + r * 128 + sw) = rb[slot][0];
    *(u32x4*)(smem + G3_OFF_B + buf * 10240 + (r + 32) * 128 + sw) = rb[slot][1];
    if (b2) *(u32x4*)(smem + G3_OFF_B + buf * 10240 + (r + 64) * 128 + sw) = rb[slot][2];
  };
#pragma unroll
  for (int d = 0; d < PD; ++d) loadS(d, d);
  writeS(0, 0);
  loadS(0, PD);
  __syncthreads();
  TS(1);
  const int wid = tid >> 6, lane = tid & 63, fr = lane & 15, g = lane >> 4;
  f32x4 acc[5] = {};
#pragma unroll
  for (int kt = 0; kt < 14; ++kt) {
    const int buf = kt & 1;
    if (kt + 1 < 14) {
      writeS((kt + 1) % PD, buf ^ 1);
      if (kt + 1 + PD < 14) loadS((kt + 1) % PD, kt + 1 + PD);
    }
#pragma unroll
    for (int ks = 0; ks < 2; ++ks) {
      const int rr = 16 * wid + fr;
      bf16x8 T = *(const bf16x8*)(smem + G3_OFF_A + buf * 8192 + rr * 128 + (((4 * ks + g) ^ (rr & 7)) << 4));
#pragma unroll
      for (int i = 0; i < 5; ++i) {
        const int wr = 16 * i + fr;
        bf16x8 W = *(const bf16x8*)(smem + G3_OFF_B + buf * 10240 + wr * 128 + (((4 * ks + g) ^ (wr & 7)) << 4));
        acc[i] = __builtin_amdgcn_mfma_f32_16x16x32_bf16(W, T, acc[i], 0, 0, 0);
      }
    }
    __syncthreads();
  }
  TS(2);
  const int t = t0 + 16 * wid + fr;
  float s0 = 0.f, s1 = 0.f;
#pragma unroll
  for (int i = 0; i < 5; ++i) {
    const int d = d0 + 16 * i + 4 * g;
    u32x2 xr = *(const u32x2*)(x1 + (size_t)t * D_ + d);
    u32x2 w = {pk2(lo16(xr[0]) + rbf(acc[i][0]), hi16(xr[0]) + rbf(acc[i][1])),
               pk2(lo16(xr[1]) + rbf(acc[i][2]), hi16(xr[1]) + rbf(acc[i][3]))};
    *(u32x2*)(xout + (size_t)t * D_ + d) = w;
    float a = lo16(w[0]), b = hi16(w[0]), cc = lo16(w[1]), dd = hi16(w[1]);
    s0 += a + b + cc + dd; s1 += a * a + b * b + cc * cc + dd * dd;
  }
  s0 += __shfl_xor(s0, 16); s0 += __shfl_xor(s0, 32);
  s1 += __shfl_xor(s1, 16); s1 += __shfl_xor(s1, 32);
  if (g == 0) {
    f32x2 o = {s0, s1};
    *(f32x2*)(xst + ((size_t)t * 8 + nt) * 2) = o;
  }
  TS(3);
}

extern "C" void kernel_launch(void* const* d_in, const int* in_sizes, int n_in,
                              void* d_out, int out_size, void* d_ws, size_t ws_size,
                              hipStream_t stream) {
  const bf16_t* q = (const bf16_t*)d_in[0];
  const bf16_t* k = (const bf16_t*)d_in[1];
  const bf16_t* v = (const bf16_t*)d_in[2];
  const bf16_t* shift_k = (const bf16_t*)d_in[3];
  const bf16_t* ssm_k = (const bf16_t*)d_in[4];
  const bf16_t* ssm_D = (const bf16_t*)d_in[5];
  const bf16_t* w_in = (const bf16_t*)d_in[7];
  const bf16_t* w_sp = (const bf16_t*)d_in[8];
  const bf16_t* b_sp = (const bf16_t*)d_in[9];
  const bf16_t* w_out = (const bf16_t*)d_in[10];
  bf16_t* out = (bf16_t*)d_out;

  char* ws = (char*)d_ws;
  size_t off = 0;
  auto alloc = [&](size_t bytes) { char* p = ws + off; off += (bytes + 255) & ~(size_t)255; return p; };
  bf16_t* winT = (bf16_t*)alloc((size_t)F_ * D_ * 2);
  float* cs = (float*)alloc(F_ * 4);
  bf16_t* woutT = (bf16_t*)alloc((size_t)D_ * FHP_ * 2);
  bf16_t* Y = (bf16_t*)alloc((size_t)S_ * D_ * 2);
  float* xst0 = (float*)alloc((size_t)S_ * 16 * 4);
  float* xst1 = (float*)alloc((size_t)S_ * 16 * 4);
  float* xst2 = (float*)alloc((size_t)S_ * 16 * 4);
  bf16_t* x1 = (bf16_t*)alloc((size_t)S_ * D_ * 2);
  bf16_t* x2 = (bf16_t*)alloc((size_t)S_ * D_ * 2);
  bf16_t* x3 = (bf16_t*)alloc((size_t)S_ * D_ * 2);
  bf16_t* z = (bf16_t*)alloc((size_t)S_ * F_ * 2 + 4096);
  float* zst = (float*)alloc((size_t)S_ * 8 * 4);
  bf16_t* u = (bf16_t*)alloc((size_t)S_ * FHP_ * 2);

  (void)hipFuncSetAttribute((const void*)k0_conv_prep, hipFuncAttributeMaxDynamicSharedMemorySize, CV_LDS);
  (void)hipFuncSetAttribute((const void*)g1_kernel, hipFuncAttributeMaxDynamicSharedMemorySize, G1_LDS);
  (void)hipFuncSetAttribute((const void*)g2_kernel, hipFuncAttributeMaxDynamicSharedMemorySize, G2_LDS);
  (void)hipFuncSetAttribute((const void*)g3_kernel, hipFuncAttributeMaxDynamicSharedMemorySize, G3_LDS);

  k0_conv_prep<<<256, 512, CV_LDS, stream>>>(q, k, v, shift_k, ssm_k, ssm_D, w_in, w_out, Y, winT, cs, woutT, xst0 TSA(0));
  g1_kernel<<<256, 512, G1_LDS, stream>>>(q, Y, xst0, winT, cs, x1, z, zst TSA(1));
  g2_kernel<<<224, 768, G2_LDS, stream>>>(w_sp, z, zst, b_sp, u TSA(2));
  g3_kernel<<<256, 256, G3_LDS, stream>>>(u, woutT, x1, x2, xst1 TSA(3));
  g1_kernel<<<256, 512, G1_LDS, stream>>>(x2, Y, xst1, winT, cs, x3, z, zst TSA(4));
  g2_kernel<<<224, 768, G2_LDS, stream>>>(w_sp, z, zst, b_sp, u TSA(5));
  g3_kernel<<<256, 256, G3_LDS, stream>>>(u, woutT, x3, out, xst2 TSA(6));
}
```

```cpp
#include <hip/hip_runtime.h>
#include <hip/hip_bf16.h>
#include <stdint.h>

typedef __attribute__((ext_vector_type(8))) short bf16x8;
typedef __attribute__((ext_vector_type(4))) short s16x4;
typedef __attribute__((ext_vector_type(4))) float f32x4;
typedef __attribute__((ext_vector_type(16))) float f32x16;
typedef __attribute__((ext_vector_type(2))) float f32x2;
typedef __attribute__((ext_vector_type(2))) __bf16 bf16x2_t;
typedef __attribute__((ext_vector_type(4))) uint32_t u32x4;
typedef __attribute__((ext_vector_type(2))) uint32_t u32x2;
typedef uint16_t bf16_t;

#define TSP
#define TSA(k)
#define TS(i)

constexpr int S_ = 2048, D_ = 640, F_ = 1728, FH_ = 864, FHP_ = 896;

__device__ __forceinline__ uint32_t pk2(float lo, float hi) {
  f32x2 f = {lo, hi};
  bf16x2_t b = __builtin_convertvector(f, bf16x2_t);
  return __builtin_bit_cast(uint32_t, b);
}
__device__ __forceinline__ float lo16(uint32_t v) { return __uint_as_float(v << 16); }
__device__ __forceinline__ float hi16(uint32_t v) { return __uint_as_float(v & 0xffff0000u); }
__device__ __forceinline__ float rbf(float x) { return lo16(pk2(x, x)); }
__device__ __forceinline__ float bf2f(bf16_t v) { return __uint_as_float(((uint32_t)v) << 16); }

__device__ __forceinline__ f32x2 ln_stats(float sum, float sumsq, float inv_n) {
  float mean = sum * inv_n;
  float var = fmaxf(sumsq * inv_n - mean * mean, 0.f);
  float mb = rbf(mean);
  float vb = rbf(var);
  float sq = rbf(sqrtf(rbf(vb + 1e-5f)));
  f32x2 r = {mb, 1.0f / sq};
  return r;
}

constexpr int TP = 144;
__device__ __forceinline__ void tile_transpose_out(const char* T, bf16_t* __restrict__ dst, size_t dstride, int tid) {
  const int w = tid >> 6, lane = tid & 63, gq = lane >> 4, i = lane & 15;
  typedef __attribute__((address_space(3))) s16x4 lds_s16x4;
#pragma unroll
  for (int h = 0; h < 2; ++h) {
    const int R0 = 8 * gq + 32 * h;
    const char* p = T + (R0 + (i >> 2)) * TP + (16 * w + 4 * (i & 3)) * 2;
    s16x4 a = __builtin_amdgcn_ds_read_tr16_b64_v4i16((lds_s16x4*)p);
    s16x4 b = __builtin_amdgcn_ds_read_tr16_b64_v4i16((lds_s16x4*)(p + 4 * TP));
    bf16x8 o = __builtin_shufflevector(a, b, 0, 1, 2, 3, 4, 5, 6, 7);
    *(bf16x8*)(dst + (size_t)(16 * w + i) * dstride + R0) = o;
  }
}

__global__ __launch_bounds__(256) void kpre_kernel(
    const bf16_t* __restrict__ kin, const bf16_t* __restrict__ vin, const bf16_t* __restrict__ shift_k,
    const bf16_t* __restrict__ w_in, const bf16_t* __restrict__ w_out,
    bf16_t* __restrict__ kvT, bf16_t* __restrict__ winT, float* __restrict__ cs, bf16_t* __restrict__ woutT) {
  __shared__ __attribute__((aligned(16))) char T[64 * TP];
  const int tid = threadIdx.x;
  int j = blockIdx.x;
  const int r = tid >> 2, cq = tid & 3;
  if (j < 320) {
    const int t0 = 64 * (j & 31), d0 = 64 * (j >> 5);
    const int t = t0 + r;
    u32x4 kr[2][4], vr[2];
#pragma unroll
    for (int h = 0; h < 2; ++h) {
      const int d = d0 + 8 * (cq + 4 * h);
#pragma unroll
      for (int i = 0; i < 4; ++i) {
        u32x4 zz = {0u, 0u, 0u, 0u};
        kr[h][i] = (t - 3 + i >= 0) ? *(const u32x4*)(kin + (size_t)(t - 3 + i) * D_ + d) : zz;
      }
      vr[h] = *(const u32x4*)(vin + (size_t)t * D_ + d);
    }
#pragma unroll
    for (int h = 0; h < 2; ++h) {
      const int d = d0 + 8 * (cq + 4 * h);
      u32x4 o;
#pragma unroll
      for (int e2 = 0; e2 < 4; ++e2) {
        u32x4 sk = *(const u32x4*)(shift_k + (size_t)(d + 2 * e2) * 4);
        float res[2];
#pragma unroll
        for (int e = 0; e < 2; ++e) {
          float w0 = lo16(sk[2 * e]), w1 = hi16(sk[2 * e]), w2 = lo16(sk[2 * e + 1]), w3 = hi16(sk[2 * e + 1]);
          float k0 = e ? hi16(kr[h][0][e2]) : lo16(kr[h][0][e2]);
          float k1 = e ? hi16(kr[h][1][e2]) : lo16(kr[h][1][e2]);
          float k2 = e ? hi16(kr[h][2][e2]) : lo16(kr[h][2][e2]);
          float k3 = e ? hi16(kr[h][3][e2]) : lo16(kr[h][3][e2]);
          float ks = rbf(k0 * w3);
          ks = rbf(ks + rbf(k1 * w2));
          ks = rbf(ks + rbf(k2 * w1));
          ks = rbf(ks + rbf(k3 * w0));
          float vv = e ? hi16(vr[h][e2]) : lo16(vr[h][e2]);
          res[e] = ks * vv;
        }
        o[e2] = pk2(res[0], res[1]);
      }
      *(u32x4*)(T + r * TP + (cq + 4 * h) * 16) = o;
    }
    __syncthreads();
    tile_transpose_out(T, kvT + (size_t)d0 * S_ + t0, S_, tid);
    return;
  }
  j -= 320;
  if (j < 270) {
    const int d0 = 64 * (j % 10), f0 = 64 * (j / 10);
#pragma unroll
    for (int h = 0; h < 2; ++h)
      *(u32x4*)(T + r * TP + (cq + 4 * h) * 16) = *(const u32x4*)(w_in + (size_t)(d0 + r) * F_ + f0 + 8 * (cq + 4 * h));
    __syncthreads();
    tile_transpose_out(T, winT + (size_t)f0 * D_ + d0, D_, tid);
    return;
  }
  j -= 270;
  if (j < 140) {
    const int d0 = 64 * (j % 10), f0 = 64 * (j / 10);
#pragma unroll
    for (int h = 0; h < 2; ++h) {
      u32x4 zz = {0u, 0u, 0u, 0u};
      *(u32x4*)(T + r * TP + (cq + 4 * h) * 16) =
          (f0 + r < FH_) ? *(const u32x4*)(w_out + (size_t)(f0 + r) * D_ + d0 + 8 * (cq + 4 * h)) : zz;
    }
    __syncthreads();
    tile_transpose_out(T, woutT + (size_t)d0 * FHP_ + f0, FHP_, tid);
    return;
  }
  j -= 140;
  {
    const int f0 = 64 * j, c8 = tid & 7, rr = tid >> 3;
    float s[8] = {0.f, 0.f, 0.f, 0.f, 0.f, 0.f, 0.f, 0.f};
#pragma unroll 4
    for (int it = 0; it < 20; ++it) {
      u32x4 w = *(const u32x4*)(w_in + (size_t)(32 * it + rr) * F_ + f0 + 8 * c8);
#pragma unroll
      for (int i = 0; i < 4; ++i) { s[2 * i] += lo16(w[i]); s[2 * i + 1] += hi16(w[i]); }
    }
    float* R = (float*)T;
#pragma unroll
    for (int i = 0; i < 8; ++i) R[rr * 64 + 8 * c8 + i] = s[i];
    __syncthreads();
    if (tid < 64) {
      float a = 0.f;
#pragma unroll
      for (int i = 0; i < 32; ++i) a += R[i * 64 + tid];
      cs[f0 + tid] = a;
    }
  }
}

__global__ __launch_bounds__(256) void kt_kernel(const bf16_t* __restrict__ YT, bf16_t* __restrict__ Y,
                                                 const bf16_t* __restrict__ q, float* __restrict__ xst) {
  __shared__ __attribute__((aligned(16))) char T[64 * TP];
  const int tid = threadIdx.x;
  const int j = blockIdx.x;
  if (j < 320) {
    const int r = tid >> 2, cq = tid & 3;
    const int t0 = 64 * (j & 31), d0 = 64 * (j >> 5);
#pragma unroll
    for (int h = 0; h < 2; ++h)
      *(u32x4*)(T + r * TP + (cq + 4 * h) * 16) = *(const u32x4*)(YT + (size_t)(d0 + r) * S_ + t0 + 8 * (cq + 4 * h));
    __syncthreads();
    tile_transpose_out(T, Y + (size_t)t0 * D_ + d0, D_, tid);
    return;
  }
  {
    const int r = tid >> 3, c = tid & 7;
    const int row = 32 * (j - 320) + r;
    float sum = 0.f, sq = 0.f;
#pragma unroll
    for (int it = 0; it < 10; ++it) {
      u32x4 w = *(const u32x4*)(q + (size_t)row * D_ + 64 * it + 8 * c);
#pragma unroll
      for (int i = 0; i < 4; ++i) {
        float a = lo16(w[i]), b = hi16(w[i]);
        sum += a + b; sq += a * a + b * b;
      }
    }
    sum += __shfl_xor(sum, 1); sum += __shfl_xor(sum, 2); sum += __shfl_xor(sum, 4);
    sq += __shfl_xor(sq, 1); sq += __shfl_xor(sq, 2); sq += __shfl_xor(sq, 4);
    f32x2 o = {c == 0 ? sum : 0.f, c == 0 ? sq : 0.f};
    *(f32x2*)(xst + ((size_t)row * 8 + c) * 2) = o;
  }
}

constexpr int CV_HP = 4416;
constexpr int CV_HCH = 4 * CV_HP;
constexpr int CV_UCH = 96 * 80;
constexpr int CV_OFF_H = 0;
constexpr int CV_OFF_U = CV_OFF_H + 2 * CV_HCH;
constexpr int CV_OFF_R = CV_OFF_U + 2 * CV_UCH;
constexpr int CV_OFF_Y = CV_OFF_R + 2 * 8192;
constexpr int CV_LDS = CV_OFF_Y + 2 * 4096;

__global__ __launch_bounds__(256, 2) void kconv_kernel(
    const bf16_t* __restrict__ kvT, const bf16_t* __restrict__ ssm_k, const bf16_t* __restrict__ ssm_D,
    bf16_t* __restrict__ YT TSP) {
  extern __shared__ __attribute__((aligned(16))) char smem[];
  const int tid = threadIdx.x;
  TS(0);
  const int c0 = 2 * blockIdx.x;
  u32x4 ur[2];
#pragma unroll
  for (int i = 0; i < 2; ++i) ur[i] = *(const u32x4*)(kvT + (size_t)c0 * S_ + 8 * (tid + 256 * i));
  u32x2 hr[3][3];
#pragma unroll
  for (int rd = 0; rd < 3; ++rd) {
    int task = tid + 256 * rd;
    int ch = task / 262, c = task % 262 - 6;
#pragma unroll
    for (int i = 0; i < 3; ++i) {
      int idx = 8 * c + 4 * i;
      u32x2 z = {0u, 0u};
      hr[rd][i] = (task < 524 && idx >= 0 && idx < 2048) ? *(const u32x2*)(ssm_k + (size_t)(c0 + ch) * S_ + idx) : z;
    }
  }
  for (int i = tid; i < 2 * 160; i += 256) {
    int ch = i / 160, o = i % 160;
    u32x4 z = {0u, 0u, 0u, 0u};
    *(u32x4*)(smem + CV_OFF_U + ch * CV_UCH + o * 16) = z;
  }
  TS(1);
#pragma unroll
  for (int i = 0; i < 2; ++i) {
    const int e = 8 * (tid + 256 * i), ch = e >> 11, s = e & 2047;
    *(u32x4*)(smem + CV_OFF_U + ch * CV_UCH + ((s >> 5) + 32) * 80 + (s & 31) * 2) = ur[i];
  }
#pragma unroll
  for (int rd = 0; rd < 3; ++rd) {
    int task = tid + 256 * rd;
    if (task < 524) {
      int ch = task / 262, c = task % 262 - 6;
      uint32_t w[6] = {hr[rd][0][0], hr[rd][0][1], hr[rd][1][0], hr[rd][1][1], hr[rd][2][0], hr[rd][2][1]};
#pragma unroll
      for (int p = 0; p < 4; ++p) {
#pragma unroll
        for (int gi = 0; gi < 2; ++gi) {
          const int ao = ((1 - p) & 3) + 4 * gi;
          int a = 8 * c + ao;
          int qx = 2045 - p - a;
          auto el = [&](int e) -> uint32_t { return (e & 1) ? (w[e >> 1] >> 16) : (w[e >> 1] & 0xffff); };
          u32x2 o = {el(ao + 3) | (el(ao + 2) << 16), el(ao + 1) | (el(ao) << 16)};
          if (qx >= 0 && qx < 2088) *(u32x2*)(smem + CV_OFF_H + ch * CV_HCH + p * CV_HP + qx * 2) = o;
        }
      }
    }
  }
  TS(2);
  __syncthreads();
  TS(3);
  const int wid = tid >> 6, lane = tid & 63;
  const int ch = wid & 1, kb = wid >> 1;
  const int m = lane & 31, hh = lane >> 5;
  {
    const int p = (4 - (m & 3)) & 3;
    const char* abase = smem + CV_OFF_H + ch * CV_HCH + p * CV_HP + (2048 - m + 8 * hh - p + 16 * kb) * 2;
    const char* bbase = smem + CV_OFF_U + ch * CV_UCH + (m + 32) * 80 + 32 * kb + 16 * hh;
    f32x16 acc0 = {}, acc1 = {};
#pragma unroll 4
    for (int dl = 0; dl < 32; ++dl) {
      u32x2 a00 = *(const volatile u32x2*)(abase - 64 * dl);
      u32x2 a01 = *(const volatile u32x2*)(abase - 64 * dl + 8);
      u32x2 a10 = *(const volatile u32x2*)(abase - 64 * (dl + 32));
      u32x2 a11 = *(const volatile u32x2*)(abase - 64 * (dl + 32) + 8);
      bf16x8 B0 = *(const bf16x8*)(bbase - 80 * dl);
      bf16x8 B1 = *(const bf16x8*)(bbase + 80 * (32 - dl));
      u32x4 A0u = {a00[0], a00[1], a01[0], a01[1]};
      u32x4 A1u = {a10[0], a10[1], a11[0], a11[1]};
      bf16x8 A0 = __builtin_bit_cast(bf16x8, A0u), A1 = __builtin_bit_cast(bf16x8, A1u);
      acc0 = __builtin_amdgcn_mfma_f32_32x32x16_bf16(A0, B0, acc0, 0, 0, 0);
      acc1 = __builtin_amdgcn_mfma_f32_32x32x16_bf16(A0, B1, acc1, 0, 0, 0);
      acc1 = __builtin_amdgcn_mfma_f32_32x32x16_bf16(A1, B0, acc1, 0, 0, 0);
    }
    TS(4);
    float* red = (float*)(smem + CV_OFF_R + ch * 8192);
    if (kb == 1) {
#pragma unroll
      for (int r = 0; r < 16; ++r) { red[r * 64 + lane] = acc0[r]; red[(16 + r) * 64 + lane] = acc1[r]; }
    }
    __syncthreads();
    if (kb == 0) {
      const float Dv = bf2f(ssm_D[c0 + ch]);
      bf16_t* yb = (bf16_t*)(smem + CV_OFF_Y + ch * 4096);
#pragma unroll
      for (int half = 0; half < 2; ++half) {
        const int a = m + 32 * half;
#pragma unroll
        for (int rq = 0; rq < 4; ++rq) {
          const int b0 = 8 * rq + 4 * hh;
          u32x2 kvw = *(const u32x2*)(smem + CV_OFF_U + ch * CV_UCH + (a + 32) * 80 + b0 * 2);
          float kvv[4] = {lo16(kvw[0]), hi16(kvw[0]), lo16(kvw[1]), hi16(kvw[1])};
          float o[4];
#pragma unroll
          for (int i = 0; i < 4; ++i) {
            const int r = 4 * rq + i;
            float y = (half ? acc1[r] : acc0[r]) + red[(16 * half + r) * 64 + lane];
            o[i] = rbf(y) + rbf(kvv[i] * Dv);
          }
          u32x2 ow = {pk2(o[0], o[1]), pk2(o[2], o[3])};
          *(u32x2*)(yb + 32 * a + b0) = ow;
        }
      }
    }
    __syncthreads();
    TS(5);
#pragma unroll
    for (int i = 0; i < 2; ++i) {
      const int e = tid + 256 * i;
      *(u32x4*)(YT + (size_t)c0 * S_ + 8 * e) = *(const u32x4*)(smem + CV_OFF_Y + 16 * e);
    }
    TS(6);
  }
}

constexpr int G1_OFF_A = 0;
constexpr int G1_OFF_B = 81920;
constexpr int G1_OFF_RS = G1_OFF_B + 57344;
constexpr int G1_OFF_ZS = G1_OFF_RS + 512;
constexpr int G1_LDS = G1_OFF_ZS + 2048;

__device__ __forceinline__ float gelu_tanh(float x) {
  float y = 0.7978845608028654f * (x + 0.044715f * x * x * x);
  float e = __expf(2.f * y);
  return x - x * __frcp_rn(1.f + e);
}

__global__ __launch_bounds__(512, 2) void g1_kernel(
    const bf16_t* __restrict__ x0, const bf16_t* __restrict__ Yb, const float* __restrict__ xst,
    const bf16_t* __restrict__ winT, const float* __restrict__ cs,
    bf16_t* __restrict__ x1out, bf16_t* __restrict__ z, float* __restrict__ zst TSP) {
  extern __shared__ __attribute__((aligned(16))) char smem[];
  const int tid = threadIdx.x;
  TS(0);
  const int xcd = blockIdx.x & 7, idx = blockIdx.x >> 3;
  const int mt = 8 * (xcd >> 1) + (idx & 7);
  const int nt = 4 * (xcd & 1) + (idx >> 3);
  const int fs = (27 * nt + 1) >> 1;
  const int nf = ((27 * (nt + 1) + 1) >> 1) - fs;
  const int m0 = 64 * mt;
  const int r = tid >> 3, c = tid & 7;
  u32x4 xa[10], ya[10];
#pragma unroll
  for (int kt = 0; kt < 10; ++kt) {
    xa[kt] = *(const u32x4*)(x0 + (size_t)(m0 + r) * D_ + 64 * kt + 8 * c);
    ya[kt] = *(const u32x4*)(Yb + (size_t)(m0 + r) * D_ + 64 * kt + 8 * c);
  }
  float s0 = 0.f, s1 = 0.f;
  {
    const f32x4* p = (const f32x4*)(xst + (size_t)(m0 + r) * 16);
#pragma unroll
    for (int i = 0; i < 4; ++i) { f32x4 v = p[i]; s0 += v[0] + v[2]; s1 += v[1] + v[3]; }
  }
  const int nrows = 16 * nf;
  int brow[4];
#pragma unroll
  for (int i = 0; i < 4; ++i) { int rr = r + 64 * i; brow[i] = rr < nrows ? rr : nrows - 1; }
  const bool b3 = tid < 256;
  constexpr int PD = 3;
  u32x4 wb[PD][4];
  auto loadB = [&](int slot, int kt) {
#pragma unroll
    for (int i = 0; i < 4; ++i)
      if (i < 3 || b3) wb[slot][i] = *(const u32x4*)(winT + (size_t)(16 * fs + brow[i]) * D_ + 64 * kt + 8 * c);
  };
  auto writeB = [&](int slot, int buf) {
#pragma unroll
    for (int i = 0; i < 4; ++i) {
      int rr = r + 64 * i;
      if (i < 3 || b3) *(u32x4*)(smem + G1_OFF_B + buf * 28672 + rr * 128 + ((c ^ (rr & 7)) << 4)) = wb[slot][i];
    }
  };
#pragma unroll
  for (int d = 0; d < PD; ++d) loadB(d, d);

  const f32x2 st = ln_stats(s0, s1, 1.0f / D_);
  TS(1);
  float t0 = 0.f, t1 = 0.f;
#pragma unroll
  for (int kt = 0; kt < 10; ++kt) {
    u32x4 o;
#pragma unroll
    for (int i = 0; i < 4; ++i) {
      float a = lo16(xa[kt][i]), b = hi16(xa[kt][i]);
      float ya_ = lo16(ya[kt][i]), yb_ = hi16(ya[kt][i]);
      float da = rbf(rbf(a - st[0]) * st[1]), db = rbf(rbf(b - st[0]) * st[1]);
      float ta = rbf(da * ya_), tb = rbf(db * yb_);
      uint32_t w = pk2(a + ta, b + tb);
      o[i] = w;
      float xa_ = lo16(w), xb_ = hi16(w);
      t0 += xa_ + xb_; t1 += xa_ * xa_ + xb_ * xb_;
    }
    *(u32x4*)(smem + G1_OFF_A + kt * 8192 + r * 128 + ((c ^ (r & 7)) << 4)) = o;
    if (nt == 0) *(u32x4*)(x1out + (size_t)(m0 + r) * D_ + 64 * kt + 8 * c) = o;
  }
  t0 += __shfl_xor(t0, 1); t0 += __shfl_xor(t0, 2); t0 += __shfl_xor(t0, 4);
  t1 += __shfl_xor(t1, 1); t1 += __shfl_xor(t1, 2); t1 += __shfl_xor(t1, 4);
  if (c == 0) {
    f32x2 s = ln_stats(t0, t1, 1.0f / D_);
    *(f32x2*)(smem + G1_OFF_RS + r * 8) = s;
  }
  TS(2);
  writeB(0, 0);
  loadB(0, PD);
  __syncthreads();
  TS(3);

  const int wid = tid >> 6, lane = tid & 63, fr = lane & 15, g = lane >> 4;
  const int wm = wid & 1, wn = wid >> 1;
  f32x4 acc[4][2] = {};
  int wrow[4];
#pragma unroll
  for (int i = 0; i < 4; ++i) { int fi = 4 * wn + i; wrow[i] = 16 * (fi < nf ? fi : nf - 1) + fr; }
#pragma unroll
  for (int kt = 0; kt < 10; ++kt) {
    const int buf = kt & 1;
    if (kt + 1 < 10) {
      writeB((kt + 1) % PD, buf ^ 1);
      if (kt + 1 + PD < 10) loadB((kt + 1) % PD, kt + 1 + PD);
    }
#pragma unroll
    for (int ks = 0; ks < 2; ++ks) {
      bf16x8 X[2], W[4];
#pragma unroll
      for (int i = 0; i < 2; ++i) {
        int rr = 32 * wm + 16 * i + fr;
        X[i] = *(const bf16x8*)(smem + G1_OFF_A + kt * 8192 + rr * 128 + (((4 * ks + g) ^ (rr & 7)) << 4));
      }
#pragma unroll
      for (int i = 0; i < 4; ++i)
        W[i] = *(const bf16x8*)(smem + G1_OFF_B + buf * 28672 + wrow[i] * 128 + (((4 * ks + g) ^ (wrow[i] & 7)) << 4));
#pragma unroll
      for (int i = 0; i < 4; ++i)
#pragma unroll
        for (int j = 0; j < 2; ++j) acc[i][j] = __builtin_amdgcn_mfma_f32_16x16x32_bf16(W[i], X[j], acc[i][j], 0, 0, 0);
    }
    __syncthreads();
  }
  TS(4);
  float zs0[2] = {0.f, 0.f}, zs1[2] = {0.f, 0.f};
#pragma unroll
  for (int i = 0; i < 4; ++i) {
    const int fi = 4 * wn + i;
    if (fi < nf) {
      const int f = 16 * (fs + fi) + 4 * g;
      f32x4 csv = *(const f32x4*)(cs + f);
#pragma unroll
      for (int j = 0; j < 2; ++j) {
        const int rr = 32 * wm + 16 * j + fr;
        f32x2 rs = *(const f32x2*)(smem + G1_OFF_RS + rr * 8);
        float o[4];
#pragma unroll
        for (int e = 0; e < 4; ++e) {
          float pre = rbf((acc[i][j][e] - rs[0] * csv[e]) * rs[1]);
          o[e] = gelu_tanh(pre);
        }
        u32x2 w = {pk2(o[0], o[1]), pk2(o[2], o[3])};
        *(u32x2*)(z + (size_t)(m0 + rr) * F_ + f) = w;
        if (nt >= 4) {
          float a = lo16(w[0]), b = hi16(w[0]), cc = lo16(w[1]), d = hi16(w[1]);
          zs0[j] += a + b + cc + d; zs1[j] += a * a + b * b + cc * cc + d * d;
        }
      }
    }
  }
  if (nt >= 4) {
#pragma unroll
    for (int j = 0; j < 2; ++j) {
      zs0[j] += __shfl_xor(zs0[j], 16); zs0[j] += __shfl_xor(zs0[j], 32);
      zs1[j] += __shfl_xor(zs1[j], 16); zs1[j] += __shfl_xor(zs1[j], 32);
      if (g == 0) {
        f32x2 o = {zs0[j], zs1[j]};
        *(f32x2*)(smem + G1_OFF_ZS + (wn * 64 + 32 * wm + 16 * j + fr) * 8) = o;
      }
    }
    __syncthreads();
    if (tid < 64) {
      float a = 0.f, b = 0.f;
#pragma unroll
      for (int w = 0; w < 4; ++w) { f32x2 v = *(const f32x2*)(smem + G1_OFF_ZS + (w * 64 + tid) * 8); a += v[0]; b += v[1]; }
      f32x2 o = {a, b};
      *(f32x2*)(zst + ((size_t)(m0 + tid) * 4 + (nt - 4)) * 2) = o;
    }
  }
  TS(5);
}

constexpr int G2_NS = 6;
constexpr int G2_OFF_A = 0;
constexpr int G2_OFF_B = G2_NS * 16384;
constexpr int G2_OFF_ST = G2_OFF_B + 3 * 8192;
constexpr int G2_LDS = G2_OFF_ST + 16384;

__device__ __forceinline__ void glds16(const void* gsrc, uint32_t lds_dst) {
  uint32_t keep;
  asm volatile("s_mov_b32 %0, m0\n\ts_mov_b32 m0, %2\n\ts_nop 0\n\tglobal_load_lds_dwordx4 %1, off\n\ts_mov_b32 m0, %0"
               : "=&s"(keep) : "v"(gsrc), "s"(lds_dst) : "memory");
}
__device__ __forceinline__ uint32_t lds_addr(const void* p) {
  return (uint32_t)(uintptr_t)(const __attribute__((address_space(3))) char*)p;
}
__device__ __forceinline__ void touch(const void* p, uint32_t& sink) {
  asm volatile("global_load_ushort %0, %1, off" : "+v"(sink) : "v"(p) : "memory");
}
#define WAIT_VM(n) asm volatile("s_waitcnt vmcnt(" #n ")" ::: "memory")
#define WAIT_LGKM0() asm volatile("s_waitcnt lgkmcnt(0)" ::: "memory")
#define RAW_BAR() __builtin_amdgcn_s_barrier()

__global__ __launch_bounds__(768, 1) void g2_kernel(
    const bf16_t* __restrict__ wsp, const bf16_t* __restrict__ z, const float* __restrict__ zst,
    const bf16_t* __restrict__ bsp, bf16_t* __restrict__ u TSP) {
  extern __shared__ __attribute__((aligned(16))) char smem[];
  const int tid = threadIdx.x;
  TS(0);
  const int xcd = blockIdx.x & 7, idx = blockIdx.x >> 3;
  const int mt = 4 * (xcd >> 1) + (idx & 3);
  const int nt = 7 * (xcd & 1) + (idx >> 2);
  const int t0 = 128 * mt, f0 = 64 * nt;
  const int wid = __builtin_amdgcn_readfirstlane(tid >> 6), lane = tid & 63;
  const int rot = idx;
  if (wid >= 8) {
    const int pt = tid - 512;
    const int r = pt >> 3, c = pt & 7;
    constexpr int PD = 8;
    u32x4 rb[PD][2];
    const bf16_t* zp = z + (size_t)r * F_ + FH_ + f0 + 8 * c;
    auto loadB = [&](int slot, int kt) {
      const int ke = (kt + rot) & 31;
      rb[slot][0] = *(const u32x4*)(zp + (size_t)(64 * ke) * F_);
      rb[slot][1] = *(const u32x4*)(zp + (size_t)(64 * ke + 32) * F_);
    };
    const int bswz0 = (((r >> 1) & 1) | (((r >> 3) & 1) << 1)) << 1;
    u32x4 nb[2];
    auto calcB = [&](int slot, int kt) {
#pragma unroll
      for (int h = 0; h < 2; ++h) {
        f32x2 st = *(const f32x2*)(smem + G2_OFF_ST + (64 * ((kt + rot) & 31) + r + 32 * h) * 8);
#pragma unroll
        for (int i = 0; i < 4; ++i) {
          float a = lo16(rb[slot][h][i]), b = hi16(rb[slot][h][i]);
          nb[h][i] = pk2(fmaf(a, st[0], st[1]), fmaf(b, st[0], st[1]));
        }
      }
    };
    auto putB = [&](int bslot) {
#pragma unroll
      for (int h = 0; h < 2; ++h)
        *(u32x4*)(smem + G2_OFF_B + bslot * 8192 + (r + 32 * h) * 128 + ((c ^ bswz0) << 4)) = nb[h];
    };
    {
      f32x4 sv[16];
      const f32x4* p = (const f32x4*)(zst + (size_t)pt * 64);
#pragma unroll
      for (int i = 0; i < 16; ++i) sv[i] = p[i];
#pragma unroll
      for (int i = 0; i < 8; ++i) {
        f32x4 a = sv[2 * i], b = sv[2 * i + 1];
        f32x2 s = ln_stats(a[0] + a[2] + b[0] + b[2], a[1] + a[3] + b[1] + b[3], 1.0f / FH_);
        f32x2 s2 = {s[1], -s[0] * s[1]};
        *(f32x2*)(smem + G2_OFF_ST + (8 * pt + i) * 8) = s2;
      }
    }
#pragma unroll
    for (int d = 0; d < PD; ++d) loadB(d, d);
    WAIT_LGKM0();
    RAW_BAR();
    calcB(0, 0);
    putB(0);
    loadB(0, PD);
    calcB(1, 1);
    loadB(1, PD + 1);
    WAIT_LGKM0();
    RAW_BAR();
    putB(1);
    calcB(2, 2);
    loadB(2, PD + 2);
    WAIT_LGKM0();
    int bs = 2;
#pragma unroll 1
    for (int kt0 = 0; kt0 < 32; kt0 += PD) {
#pragma unroll
      for (int kk = 0; kk < PD; ++kk) {
        const int kt = kt0 + kk;
        RAW_BAR();
        if (kt + 2 < 32) putB(bs);
        if (kt + 3 < 32) {
          calcB((kk + 3) % PD, kt + 3);
          if (kt + 3 + PD < 32) loadB((kk + 3) % PD, kt + 3 + PD);
        }
        bs = bs == 2 ? 0 : bs + 1;
        WAIT_LGKM0();
      }
    }
    return;
  }
  const int lr = lane >> 3, lc = (lane & 7) ^ lr;
  const bf16_t* ap0 = wsp + (size_t)(t0 + 16 * wid + lr) * S_ + 8 * lc;
  const bf16_t* ap1 = ap0 + (size_t)8 * S_;
  const uint32_t lbase = lds_addr(smem) + G2_OFF_A + wid * 2048;
  auto issueA = [&](int kt, int slot) {
    const int ke = (kt + rot) & 31;
    glds16(ap0 + 64 * ke, lbase + slot * 16384);
    glds16(ap1 + 64 * ke, lbase + slot * 16384 + 1024);
  };
#pragma unroll
  for (int s = 0; s < G2_NS - 1; ++s) issueA(s, s);
  RAW_BAR();
  TS(1);
  const int fr = lane & 15, g = lane >> 4;
  const int wm = wid & 3, wn = wid >> 2;
  f32x4 acc[2][2] = {};
  const int q4 = (lane & 15) >> 2, p4 = lane & 3;
  int aoff[2][2], boff[2][2][2];
#pragma unroll
  for (int ks = 0; ks < 2; ++ks) {
#pragma unroll
    for (int i = 0; i < 2; ++i) {
      int rr = 32 * wm + 16 * i + fr;
      aoff[ks][i] = G2_OFF_A + rr * 128 + (((4 * ks + g) ^ (rr & 7)) << 4);
#pragma unroll
      for (int t = 0; t < 2; ++t) {
        int row = 32 * ks + 8 * g + 4 * t + q4;
        int ch = 2 * (2 * wn + i) + (p4 >> 1);
        int sw = (((row >> 1) & 1) | (((row >> 3) & 1) << 1)) << 1;
        boff[ks][i][t] = G2_OFF_B + row * 128 + ((ch ^ sw) << 4) + 8 * (p4 & 1);
      }
    }
  }
  auto readF = [&](bf16x8* T, bf16x8* Z, int ks, int aslot, int bslot) {
#pragma unroll
    for (int i = 0; i < 2; ++i) T[i] = *(const bf16x8*)(smem + aslot * 16384 + aoff[ks][i]);
#pragma unroll
    for (int i = 0; i < 2; ++i) {
      typedef __attribute__((address_space(3))) s16x4 lds_s16x4;
      s16x4 h0 = __builtin_amdgcn_ds_read_tr16_b64_v4i16((lds_s16x4*)(smem + bslot * 8192 + boff[ks][i][0]));
      s16x4 h1 = __builtin_amdgcn_ds_read_tr16_b64_v4i16((lds_s16x4*)(smem + bslot * 8192 + boff[ks][i][1]));
      Z[i] = __builtin_shufflevector(h0, h1, 0, 1, 2, 3, 4, 5, 6, 7);
    }
  };
  auto mm = [&](const bf16x8* T, const bf16x8* Z) {
#pragma unroll
    for (int i = 0; i < 2; ++i)
#pragma unroll
      for (int j = 0; j < 2; ++j) acc[i][j] = __builtin_amdgcn_mfma_f32_16x16x32_bf16(Z[i], T[j], acc[i][j], 0, 0, 0);
  };
  bf16x8 TA[2][2], ZA[2][2], TB[2][2], ZB[2][2];
  WAIT_VM(8);
  RAW_BAR();
  readF(TA[0], ZA[0], 0, 0, 0);
  readF(TA[1], ZA[1], 1, 0, 0);
  int slot = 0, bs = 0;
  auto step = [&](int kt, bf16x8 (*Tc)[2], bf16x8 (*Zc)[2], bf16x8 (*Tn)[2], bf16x8 (*Zn)[2]) {
    WAIT_VM(6);
    RAW_BAR();
    {
      int kn = kt + G2_NS - 1; kn = kn < 32 ? kn : 31;
      int sn = slot == 0 ? G2_NS - 1 : slot - 1;
      issueA(kn, sn);
    }
    const int slot1 = slot + 1 == G2_NS ? 0 : slot + 1;
    const int bs1 = bs == 2 ? 0 : bs + 1;
    if (wn == 0) {
      readF(Tn[0], Zn[0], 0, slot1, bs1);
      readF(Tn[1], Zn[1], 1, slot1, bs1);
      __builtin_amdgcn_sched_barrier(0);
      mm(Tc[0], Zc[0]);
      mm(Tc[1], Zc[1]);
    } else {
      mm(Tc[0], Zc[0]);
      mm(Tc[1], Zc[1]);
      __builtin_amdgcn_sched_barrier(0);
      readF(Tn[0], Zn[0], 0, slot1, bs1);
      readF(Tn[1], Zn[1], 1, slot1, bs1);
    }
    __builtin_amdgcn_sched_barrier(0);
    slot = slot1; bs = bs1;
  };
#pragma unroll 1
  for (int kt = 0; kt < 32; kt += 2) {
    step(kt, TA, ZA, TB, ZB);
    step(kt + 1, TB, ZB, TA, ZA);
  }
  WAIT_VM(0);
  TS(3);
#pragma unroll
  for (int j = 0; j < 2; ++j) {
    const int t = t0 + 32 * wm + 16 * j + fr;
    const float bb = bf2f(bsp[t]);
#pragma unroll
    for (int i = 0; i < 2; ++i) {
      const int f = f0 + 32 * wn + 16 * i + 4 * g;
      u32x2 w = {0u, 0u};
      if (f < FH_) {
        u32x2 z1 = *(const u32x2*)(z + (size_t)t * F_ + f);
        float g0 = rbf(rbf(acc[i][j][0]) + bb), g1 = rbf(rbf(acc[i][j][1]) + bb);
        float g2 = rbf(rbf(acc[i][j][2]) + bb), g3 = rbf(rbf(acc[i][j][3]) + bb);
        w[0] = pk2(lo16(z1[0]) * g0, hi16(z1[0]) * g1);
        w[1] = pk2(lo16(z1[1]) * g2, hi16(z1[1]) * g3);
      }
      *(u32x2*)(u + (size_t)t * FHP_ + f) = w;
    }
  }
  TS(4);
}

constexpr int G3_OFF_A = 0;
constexpr int G3_OFF_B = 16384;
constexpr int G3_LDS = 36864;

__global__ __launch_bounds__(256, 1) void g3_kernel(
    const bf16_t* __restrict__ u, const bf16_t* __restrict__ woutT, const bf16_t* __restrict__ x1,
    bf16_t* __restrict__ xout, float* __restrict__ xst TSP) {
  extern __shared__ __attribute__((aligned(16))) char smem[];
  const int tid = threadIdx.x;
  TS(0);
  const int xcd = blockIdx.x & 7, idx = blockIdx.x >> 3;
  const int mt = 8 * (xcd >> 1) + (idx & 7);
  const int nt = 4 * (xcd & 1) + (idx >> 3);
  const int t0 = 64 * mt, d0 = 80 * nt;
  const int r = tid >> 3, c = tid & 7;
  constexpr int PD = 4;
  u32x4 ra[PD][2], rb[PD][3];
  const bool b2 = tid < 128;
  auto loadS = [&](int slot, int kt) {
    ra[slot][0] = *(const u32x4*)(u + (size_t)(t0 + r) * FHP_ + 64 * kt + 8 * c);
    ra[slot][1] = *(const u32x4*)(u + (size_t)(t0 + r + 32) * FHP_ + 64 * kt + 8 * c);
    rb[slot][0] = *(const u32x4*)(woutT + (size_t)(d0 + r) * FHP_ + 64 * kt + 8 * c);
    rb[slot][1] = *(const u32x4*)(woutT + (size_t)(d0 + r + 32) * FHP_ + 64 * kt + 8 * c);
    if (b2) rb[slot][2] = *(const u32x4*)(woutT + (size_t)(d0 + r + 64) * FHP_ + 64 * kt + 8 * c);
  };
  auto writeS = [&](int slot, int buf) {
    const int sw = (c ^ (r & 7)) << 4;
    *(u32x4*)(smem + G3_OFF_A + buf * 8192 + r * 128 + sw) = ra[slot][0];
    *(u32x4*)(smem + G3_OFF_A + buf * 8192 + (r + 32) * 128 + sw) = ra[slot][1];
    *(u32x4*)(smem + G3_OFF_B + buf * 10240 + r * 128 + sw) = rb[slot][0];
    *(u32x4*)(smem + G3_OFF_B + buf * 10240 + (r + 32) * 128 + sw) = rb[slot][1];
    if (b2) *(u32x4*)(smem + G3_OFF_B + buf * 10240 + (r + 64) * 128 + sw) = rb[slot][2];
  };
#pragma unroll
  for (int d = 0; d < PD; ++d) loadS(d, d);
  writeS(0, 0);
  loadS(0, PD);
  __syncthreads();
  TS(1);
  const int wid = tid >> 6, lane = tid & 63, fr = lane & 15, g = lane >> 4;
  f32x4 acc[5] = {};
#pragma unroll
  for (int kt = 0; kt < 14; ++kt) {
    const int buf = kt & 1;
    if (kt + 1 < 14) {
      writeS((kt + 1) % PD, buf ^ 1);
      if (kt + 1 + PD < 14) loadS((kt + 1) % PD, kt + 1 + PD);
    }
#pragma unroll
    for (int ks = 0; ks < 2; ++ks) {
      const int rr = 16 * wid + fr;
      bf16x8 T = *(const bf16x8*)(smem + G3_OFF_A + buf * 8192 + rr * 128 + (((4 * ks + g) ^ (rr & 7)) << 4));
#pragma unroll
      for (int i = 0; i < 5; ++i) {
        const int wr = 16 * i + fr;
        bf16x8 W = *(const bf16x8*)(smem + G3_OFF_B + buf * 10240 + wr * 128 + (((4 * ks + g) ^ (wr & 7)) << 4));
        acc[i] = __builtin_amdgcn_mfma_f32_16x16x32_bf16(W, T, acc[i], 0, 0, 0);
      }
    }
    __syncthreads();
  }
  TS(2);
  const int t = t0 + 16 * wid + fr;
  float s0 = 0.f, s1 = 0.f;
#pragma unroll
  for (int i = 0; i < 5; ++i) {
    const int d = d0 + 16 * i + 4 * g;
    u32x2 xr = *(const u32x2*)(x1 + (size_t)t * D_ + d);
    u32x2 w = {pk2(lo16(xr[0]) + rbf(acc[i][0]), hi16(xr[0]) + rbf(acc[i][1])),
               pk2(lo16(xr[1]) + rbf(acc[i][2]), hi16(xr[1]) + rbf(acc[i][3]))};
    *(u32x2*)(xout + (size_t)t * D_ + d) = w;
    float a = lo16(w[0]), b = hi16(w[0]), cc = lo16(w[1]), dd = hi16(w[1]);
    s0 += a + b + cc + dd; s1 += a * a + b * b + cc * cc + dd * dd;
  }
  s0 += __shfl_xor(s0, 16); s0 += __shfl_xor(s0, 32);
  s1 += __shfl_xor(s1, 16); s1 += __shfl_xor(s1, 32);
  if (g == 0) {
    f32x2 o = {s0, s1};
    *(f32x2*)(xst + ((size_t)t * 8 + nt) * 2) = o;
  }
  TS(3);
}

extern "C" void kernel_launch(void* const* d_in, const int* in_sizes, int n_in,
                              void* d_out, int out_size, void* d_ws, size_t ws_size,
                              hipStream_t stream) {
  const bf16_t* q = (const bf16_t*)d_in[0];
  const bf16_t* k = (const bf16_t*)d_in[1];
  const bf16_t* v = (const bf16_t*)d_in[2];
  const bf16_t* shift_k = (const bf16_t*)d_in[3];
  const bf16_t* ssm_k = (const bf16_t*)d_in[4];
  const bf16_t* ssm_D = (const bf16_t*)d_in[5];
  const bf16_t* w_in = (const bf16_t*)d_in[7];
  const bf16_t* w_sp = (const bf16_t*)d_in[8];
  const bf16_t* b_sp = (const bf16_t*)d_in[9];
  const bf16_t* w_out = (const bf16_t*)d_in[10];
  bf16_t* out = (bf16_t*)d_out;

  char* ws = (char*)d_ws;
  size_t off = 0;
  auto alloc = [&](size_t bytes) { char* p = ws + off; off += (bytes + 255) & ~(size_t)255; return p; };
  bf16_t* winT = (bf16_t*)alloc((size_t)F_ * D_ * 2);
  float* cs = (float*)alloc(F_ * 4);
  bf16_t* woutT = (bf16_t*)alloc((size_t)D_ * FHP_ * 2);
  bf16_t* Y = (bf16_t*)alloc((size_t)S_ * D_ * 2);
  bf16_t* kvT = (bf16_t*)alloc((size_t)S_ * D_ * 2);
  bf16_t* YT = (bf16_t*)alloc((size_t)S_ * D_ * 2);
  float* xst0 = (float*)alloc((size_t)S_ * 16 * 4);
  float* xst1 = (float*)alloc((size_t)S_ * 16 * 4);
  float* xst2 = (float*)alloc((size_t)S_ * 16 * 4);
  bf16_t* x1 = (bf16_t*)alloc((size_t)S_ * D_ * 2);
  bf16_t* x2 = (bf16_t*)alloc((size_t)S_ * D_ * 2);
  bf16_t* x3 = (bf16_t*)alloc((size_t)S_ * D_ * 2);
  bf16_t* z = (bf16_t*)alloc((size_t)S_ * F_ * 2 + 4096);
  float* zst = (float*)alloc((size_t)S_ * 8 * 4);
  bf16_t* u = (bf16_t*)alloc((size_t)S_ * FHP_ * 2);

  (void)hipFuncSetAttribute((const void*)kconv_kernel, hipFuncAttributeMaxDynamicSharedMemorySize, CV_LDS);
  (void)hipFuncSetAttribute((const void*)g1_kernel, hipFuncAttributeMaxDynamicSharedMemorySize, G1_LDS);
  (void)hipFuncSetAttribute((const void*)g2_kernel, hipFuncAttributeMaxDynamicSharedMemorySize, G2_LDS);
  (void)hipFuncSetAttribute((const void*)g3_kernel, hipFuncAttributeMaxDynamicSharedMemorySize, G3_LDS);

  kpre_kernel<<<320 + 270 + 140 + 27, 256, 0, stream>>>(k, v, shift_k, w_in, w_out, kvT, winT, cs, woutT);
  kconv_kernel<<<320, 256, CV_LDS, stream>>>(kvT, ssm_k, ssm_D, YT TSA(0));
  kt_kernel<<<320 + 64, 256, 0, stream>>>(YT, Y, q, xst0);
  g1_kernel<<<256, 512, G1_LDS, stream>>>(q, Y, xst0, winT, cs, x1, z, zst TSA(1));
  g2_kernel<<<224, 768, G2_LDS, stream>>>(w_sp, z, zst, b_sp, u TSA(2));
  g3_kernel<<<256, 256, G3_LDS, stream>>>(u, woutT, x1, x2, xst1 TSA(3));
  g1_kernel<<<256, 512, G1_LDS, stream>>>(x2, Y, xst1, winT, cs, x3, z, zst TSA(4));
  g2_kernel<<<224, 768, G2_LDS, stream>>>(w_sp, z, zst, b_sp, u TSA(5));
  g3_kernel<<<256, 256, G3_LDS, stream>>>(u, woutT, x3, out, xst2 TSA(6));
}
```

```cpp
#include <hip/hip_runtime.h>
#include <hip/hip_bf16.h>
#include <stdint.h>

typedef __attribute__((ext_vector_type(8))) short bf16x8;
typedef __attribute__((ext_vector_type(4))) short s16x4;
typedef __attribute__((ext_vector_type(4))) float f32x4;
typedef __attribute__((ext_vector_type(16))) float f32x16;
typedef __attribute__((ext_vector_type(2))) float f32x2;
typedef __attribute__((ext_vector_type(2))) __bf16 bf16x2_t;
typedef __attribute__((ext_vector_type(4))) uint32_t u32x4;
typedef __attribute__((ext_vector_type(2))) uint32_t u32x2;
typedef uint16_t bf16_t;

#define TSP
#define TSA(k)
#define TS(i)

constexpr int S_ = 2048, D_ = 640, F_ = 1728, FH_ = 864, FHP_ = 896;

__device__ __forceinline__ uint32_t pk2(float lo, float hi) {
  f32x2 f = {lo, hi};
  bf16x2_t b = __builtin_convertvector(f, bf16x2_t);
  return __builtin_bit_cast(uint32_t, b);
}
__device__ __forceinline__ float lo16(uint32_t v) { return __uint_as_float(v << 16); }
__device__ __forceinline__ float hi16(uint32_t v) { return __uint_as_float(v & 0xffff0000u); }
__device__ __forceinline__ float rbf(float x) { return lo16(pk2(x, x)); }
__device__ __forceinline__ float bf2f(bf16_t v) { return __uint_as_float(((uint32_t)v) << 16); }

__device__ __forceinline__ f32x2 ln_stats(float sum, float sumsq, float inv_n) {
  float mean = sum * inv_n;
  float var = fmaxf(sumsq * inv_n - mean * mean, 0.f);
  float mb = rbf(mean);
  float vb = rbf(var);
  float sq = rbf(sqrtf(rbf(vb + 1e-5f)));
  f32x2 r = {mb, 1.0f / sq};
  return r;
}

constexpr int TP = 144;
__device__ __forceinline__ void tile_transpose_out(const char* T, bf16_t* __restrict__ dst, size_t dstride, int tid) {
  const int w = tid >> 6, lane = tid & 63, gq = lane >> 4, i = lane & 15;
  typedef __attribute__((address_space(3))) s16x4 lds_s16x4;
#pragma unroll
  for (int h = 0; h < 2; ++h) {
    const int R0 = 8 * gq + 32 * h;
    const char* p = T + (R0 + (i >> 2)) * TP + (16 * w + 4 * (i & 3)) * 2;
    s16x4 a = __builtin_amdgcn_ds_read_tr16_b64_v4i16((lds_s16x4*)p);
    s16x4 b = __builtin_amdgcn_ds_read_tr16_b64_v4i16((lds_s16x4*)(p + 4 * TP));
    bf16x8 o = __builtin_shufflevector(a, b, 0, 1, 2, 3, 4, 5, 6, 7);
    *(bf16x8*)(dst + (size_t)(16 * w + i) * dstride + R0) = o;
  }
}

__global__ __launch_bounds__(256) void kpre_kernel(
    const bf16_t* __restrict__ kin, const bf16_t* __restrict__ vin, const bf16_t* __restrict__ shift_k,
    bf16_t* __restrict__ kvT) {
  __shared__ __attribute__((aligned(16))) char T[64 * TP];
  const int tid = threadIdx.x;
  int j = blockIdx.x;
  const int r = tid >> 2, cq = tid & 3;
  {
    const int t0 = 64 * (j & 31), d0 = 64 * (j >> 5);
    const int t = t0 + r;
    u32x4 kr[2][4], vr[2], skr[2][4];
#pragma unroll
    for (int h = 0; h < 2; ++h) {
      const int d = d0 + 8 * (cq + 4 * h);
#pragma unroll
      for (int i = 0; i < 4; ++i) {
        u32x4 zz = {0u, 0u, 0u, 0u};
        kr[h][i] = (t - 3 + i >= 0) ? *(const u32x4*)(kin + (size_t)(t - 3 + i) * D_ + d) : zz;
      }
      vr[h] = *(const u32x4*)(vin + (size_t)t * D_ + d);
#pragma unroll
      for (int e2 = 0; e2 < 4; ++e2) skr[h][e2] = *(const u32x4*)(shift_k + (size_t)(d + 2 * e2) * 4);
    }
#pragma unroll
    for (int h = 0; h < 2; ++h) {
      u32x4 o;
#pragma unroll
      for (int e2 = 0; e2 < 4; ++e2) {
        const u32x4 sk = skr[h][e2];
        float res[2];
#pragma unroll
        for (int e = 0; e < 2; ++e) {
          float w0 = lo16(sk[2 * e]), w1 = hi16(sk[2 * e]), w2 = lo16(sk[2 * e + 1]), w3 = hi16(sk[2 * e + 1]);
          float k0 = e ? hi16(kr[h][0][e2]) : lo16(kr[h][0][e2]);
          float k1 = e ? hi16(kr[h][1][e2]) : lo16(kr[h][1][e2]);
          float k2 = e ? hi16(kr[h][2][e2]) : lo16(kr[h][2][e2]);
          float k3 = e ? hi16(kr[h][3][e2]) : lo16(kr[h][3][e2]);
          float ks = rbf(k0 * w3);
          ks = rbf(ks + rbf(k1 * w2));
          ks = rbf(ks + rbf(k2 * w1));
          ks = rbf(ks + rbf(k3 * w0));
          float vv = e ? hi16(vr[h][e2]) : lo16(vr[h][e2]);
          res[e] = ks * vv;
        }
        o[e2] = pk2(res[0], res[1]);
      }
      *(u32x4*)(T + r * TP + (cq + 4 * h) * 16) = o;
    }
    __syncthreads();
    tile_transpose_out(T, kvT + (size_t)d0 * S_ + t0, S_, tid);
  }
}

__device__ __forceinline__ void prep_job(int j, int tid, char* T, const bf16_t* __restrict__ w_in, const bf16_t* __restrict__ w_out,
                                         const bf16_t* __restrict__ q, bf16_t* __restrict__ winT, float* __restrict__ cs,
                                         bf16_t* __restrict__ woutT, float* __restrict__ xst) {
  const int r = tid >> 2, cq = tid & 3;
  if (j < 270) {
    const int d0 = 64 * (j % 10), f0 = 64 * (j / 10);
#pragma unroll
    for (int h = 0; h < 2; ++h)
      *(u32x4*)(T + r * TP + (cq + 4 * h) * 16) = *(const u32x4*)(w_in + (size_t)(d0 + r) * F_ + f0 + 8 * (cq + 4 * h));
    __syncthreads();
    tile_transpose_out(T, winT + (size_t)f0 * D_ + d0, D_, tid);
    return;
  }
  j -= 270;
  if (j < 140) {
    const int d0 = 64 * (j % 10), f0 = 64 * (j / 10);
#pragma unroll
    for (int h = 0; h < 2; ++h) {
      u32x4 zz = {0u, 0u, 0u, 0u};
      *(u32x4*)(T + r * TP + (cq + 4 * h) * 16) =
          (f0 + r < FH_) ? *(const u32x4*)(w_out + (size_t)(f0 + r) * D_ + d0 + 8 * (cq + 4 * h)) : zz;
    }
    __syncthreads();
    tile_transpose_out(T, woutT + (size_t)d0 * FHP_ + f0, FHP_, tid);
    return;
  }
  j -= 140;
  if (j < 27) {
    const int f0 = 64 * j, c8 = tid & 7, rr = tid >> 3;
    float s[8] = {0.f, 0.f, 0.f, 0.f, 0.f, 0.f, 0.f, 0.f};
    u32x4 w[20];
#pragma unroll
    for (int it = 0; it < 20; ++it) w[it] = *(const u32x4*)(w_in + (size_t)(32 * it + rr) * F_ + f0 + 8 * c8);
#pragma unroll
    for (int it = 0; it < 20; ++it) {
#pragma unroll
      for (int i = 0; i < 4; ++i) { s[2 * i] += lo16(w[it][i]); s[2 * i + 1] += hi16(w[it][i]); }
    }
    float* R = (float*)T;
#pragma unroll
    for (int i = 0; i < 8; ++i) R[rr * 64 + 8 * c8 + i] = s[i];
    __syncthreads();
    if (tid < 64) {
      float a = 0.f;
#pragma unroll
      for (int i = 0; i < 32; ++i) a += R[i * 64 + tid];
      cs[f0 + tid] = a;
    }
    return;
  }
  j -= 27;
  {
    const int r8 = tid >> 3, c = tid & 7;
    const int row = 32 * j + r8;
    float sum = 0.f, sq = 0.f;
#pragma unroll
    for (int it = 0; it < 10; ++it) {
      u32x4 w = *(const u32x4*)(q + (size_t)row * D_ + 64 * it + 8 * c);
#pragma unroll
      for (int i = 0; i < 4; ++i) {
        float a = lo16(w[i]), b = hi16(w[i]);
        sum += a + b; sq += a * a + b * b;
      }
    }
    sum += __shfl_xor(sum, 1); sum += __shfl_xor(sum, 2); sum += __shfl_xor(sum, 4);
    sq += __shfl_xor(sq, 1); sq += __shfl_xor(sq, 2); sq += __shfl_xor(sq, 4);
    f32x2 o = {c == 0 ? sum : 0.f, c == 0 ? sq : 0.f};
    *(f32x2*)(xst + ((size_t)row * 8 + c) * 2) = o;
  }
}

__global__ __launch_bounds__(256) void kt_kernel(const bf16_t* __restrict__ YT, bf16_t* __restrict__ Y) {
  __shared__ __attribute__((aligned(16))) char T[64 * TP];
  const int tid = threadIdx.x;
  const int j = blockIdx.x;
  const int r = tid >> 2, cq = tid & 3;
  const int t0 = 64 * (j & 31), d0 = 64 * (j >> 5);
#pragma unroll
  for (int h = 0; h < 2; ++h)
    *(u32x4*)(T + r * TP + (cq + 4 * h) * 16) = *(const u32x4*)(YT + (size_t)(d0 + r) * S_ + t0 + 8 * (cq + 4 * h));
  __syncthreads();
  tile_transpose_out(T, Y + (size_t)t0 * D_ + d0, D_, tid);
}

constexpr int CV_HP = 4416;
constexpr int CV_HCH = 4 * CV_HP;
constexpr int CV_UCH = 96 * 80;
constexpr int CV_OFF_H = 0;
constexpr int CV_OFF_U = CV_OFF_H + 2 * CV_HCH;
constexpr int CV_OFF_R = CV_OFF_U + 2 * CV_UCH;
constexpr int CV_OFF_Y = CV_OFF_R + 2 * 8192;
constexpr int CV_LDS = CV_OFF_Y + 2 * 4096;

__global__ __launch_bounds__(256, 2) void kconv_kernel(
    const bf16_t* __restrict__ kvT, const bf16_t* __restrict__ ssm_k, const bf16_t* __restrict__ ssm_D,
    bf16_t* __restrict__ YT, const bf16_t* __restrict__ w_in, const bf16_t* __restrict__ w_out, const bf16_t* __restrict__ q,
    bf16_t* __restrict__ winT, float* __restrict__ cs, bf16_t* __restrict__ woutT, float* __restrict__ xst TSP) {
  extern __shared__ __attribute__((aligned(16))) char smem[];
  const int tid = threadIdx.x;
  if (blockIdx.x >= 320) {
    prep_job(blockIdx.x - 320, tid, smem, w_in, w_out, q, winT, cs, woutT, xst);
    return;
  }
  TS(0);
  const int c0 = 2 * blockIdx.x;
  u32x4 ur[2];
#pragma unroll
  for (int i = 0; i < 2; ++i) ur[i] = *(const u32x4*)(kvT + (size_t)c0 * S_ + 8 * (tid + 256 * i));
  u32x2 hr[3][3];
#pragma unroll
  for (int rd = 0; rd < 3; ++rd) {
    int task = tid + 256 * rd;
    int ch = task / 262, c = task % 262 - 6;
#pragma unroll
    for (int i = 0; i < 3; ++i) {
      int idx = 8 * c + 4 * i;
      u32x2 z = {0u, 0u};
      hr[rd][i] = (task < 524 && idx >= 0 && idx < 2048) ? *(const u32x2*)(ssm_k + (size_t)(c0 + ch) * S_ + idx) : z;
    }
  }
  for (int i = tid; i < 2 * 160; i += 256) {
    int ch = i / 160, o = i % 160;
    u32x4 z = {0u, 0u, 0u, 0u};
    *(u32x4*)(smem + CV_OFF_U + ch * CV_UCH + o * 16) = z;
  }
  TS(1);
#pragma unroll
  for (int i = 0; i < 2; ++i) {
    const int e = 8 * (tid + 256 * i), ch = e >> 11, s = e & 2047;
    *(u32x4*)(smem + CV_OFF_U + ch * CV_UCH + ((s >> 5) + 32) * 80 + (s & 31) * 2) = ur[i];
  }
#pragma unroll
  for (int rd = 0; rd < 3; ++rd) {
    int task = tid + 256 * rd;
    if (task < 524) {
      int ch = task / 262, c = task % 262 - 6;
      uint32_t w[6] = {hr[rd][0][0], hr[rd][0][1], hr[rd][1][0], hr[rd][1][1], hr[rd][2][0], hr[rd][2][1]};
#pragma unroll
      for (int p = 0; p < 4; ++p) {
#pragma unroll
        for (int gi = 0; gi < 2; ++gi) {
          const int ao = ((1 - p) & 3) + 4 * gi;
          int a = 8 * c + ao;
          int qx = 2045 - p - a;
          auto el = [&](int e) -> uint32_t { return (e & 1) ? (w[e >> 1] >> 16) : (w[e >> 1] & 0xffff); };
          u32x2 o = {el(ao + 3) | (el(ao + 2) << 16), el(ao + 1) | (el(ao) << 16)};
          if (qx >= 0 && qx < 2088) *(u32x2*)(smem + CV_OFF_H + ch * CV_HCH + p * CV_HP + qx * 2) = o;
        }
      }
    }
  }
  TS(2);
  __syncthreads();
  TS(3);
  const int wid = tid >> 6, lane = tid & 63;
  const int ch = wid & 1, kb = wid >> 1;
  const int m = lane & 31, hh = lane >> 5;
  {
    const int p = (4 - (m & 3)) & 3;
    const char* abase = smem + CV_OFF_H + ch * CV_HCH + p * CV_HP + (2048 - m + 8 * hh - p + 16 * kb) * 2;
    const char* bbase = smem + CV_OFF_U + ch * CV_UCH + (m + 32) * 80 + 32 * kb + 16 * hh;
    f32x16 acc0 = {}, acc1 = {};
    u32x2 ra[3][4];
    bf16x8 rbB[3][2];
    auto ld = [&](int sl, int dl) {
      typedef const volatile __attribute__((address_space(3))) u32x2* lds_v2;
      ra[sl][0] = *(lds_v2)(abase - 64 * dl);
      ra[sl][1] = *(lds_v2)(abase - 64 * dl + 8);
      ra[sl][2] = *(lds_v2)(abase - 64 * (dl + 32));
      ra[sl][3] = *(lds_v2)(abase - 64 * (dl + 32) + 8);
      rbB[sl][0] = *(const bf16x8*)(bbase - 80 * dl);
      rbB[sl][1] = *(const bf16x8*)(bbase + 80 * (32 - dl));
    };
    ld(0, 0);
    ld(1, 1);
#pragma unroll
    for (int dl = 0; dl < 32; ++dl) {
      if (dl + 2 < 32) ld((dl + 2) % 3, dl + 2);
      __builtin_amdgcn_sched_barrier(0);
      const int sl = dl % 3;
      u32x4 A0u = {ra[sl][0][0], ra[sl][0][1], ra[sl][1][0], ra[sl][1][1]};
      u32x4 A1u = {ra[sl][2][0], ra[sl][2][1], ra[sl][3][0], ra[sl][3][1]};
      bf16x8 A0 = __builtin_bit_cast(bf16x8, A0u), A1 = __builtin_bit_cast(bf16x8, A1u);
      acc0 = __builtin_amdgcn_mfma_f32_32x32x16_bf16(A0, rbB[sl][0], acc0, 0, 0, 0);
      acc1 = __builtin_amdgcn_mfma_f32_32x32x16_bf16(A0, rbB[sl][1], acc1, 0, 0, 0);
      acc1 = __builtin_amdgcn_mfma_f32_32x32x16_bf16(A1, rbB[sl][0], acc1, 0, 0, 0);
      __builtin_amdgcn_sched_barrier(0);
    }
    TS(4);
    float* red = (float*)(smem + CV_OFF_R + ch * 8192);
    if (kb == 1) {
#pragma unroll
      for (int r = 0; r < 16; ++r) { red[r * 64 + lane] = acc0[r]; red[(16 + r) * 64 + lane] = acc1[r]; }
    }
    __syncthreads();
    if (kb == 0) {
      const float Dv = bf2f(ssm_D[c0 + ch]);
      bf16_t* yb = (bf16_t*)(smem + CV_OFF_Y + ch * 4096);
#pragma unroll
      for (int half = 0; half < 2; ++half) {
        const int a = m + 32 * half;
#pragma unroll
        for (int rq = 0; rq < 4; ++rq) {
          const int b0 = 8 * rq + 4 * hh;
          u32x2 kvw = *(const u32x2*)(smem + CV_OFF_U + ch * CV_UCH + (a + 32) * 80 + b0 * 2);
          float kvv[4] = {lo16(kvw[0]), hi16(kvw[0]), lo16(kvw[1]), hi16(kvw[1])};
          float o[4];
#pragma unroll
          for (int i = 0; i < 4; ++i) {
            const int r = 4 * rq + i;
            float y = (half ? acc1[r] : acc0[r]) + red[(16 * half + r) * 64 + lane];
            o[i] = rbf(y) + rbf(kvv[i] * Dv);
          }
          u32x2 ow = {pk2(o[0], o[1]), pk2(o[2], o[3])};
          *(u32x2*)(yb + 32 * a + b0) = ow;
        }
      }
    }
    __syncthreads();
    TS(5);
#pragma unroll
    for (int i = 0; i < 2; ++i) {
      const int e = tid + 256 * i;
      *(u32x4*)(YT + (size_t)c0 * S_ + 8 * e) = *(const u32x4*)(smem + CV_OFF_Y + 16 * e);
    }
    TS(6);
  }
}

constexpr int G1_OFF_A = 0;
constexpr int G1_OFF_B = 81920;
constexpr int G1_OFF_RS = G1_OFF_B + 57344;
constexpr int G1_OFF_ZS = G1_OFF_RS + 512;
constexpr int G1_LDS = G1_OFF_ZS + 2048;

__device__ __forceinline__ float gelu_tanh(float x) {
  float y = 0.7978845608028654f * (x + 0.044715f * x * x * x);
  float e = __expf(2.f * y);
  return x - x * __frcp_rn(1.f + e);
}

__global__ __launch_bounds__(512, 2) void g1_kernel(
    const bf16_t* __restrict__ x0, const bf16_t* __restrict__ Yb, const float* __restrict__ xst,
    const bf16_t* __restrict__ winT, const float* __restrict__ cs,
    bf16_t* __restrict__ x1out, bf16_t* __restrict__ z, float* __restrict__ zst TSP) {
  extern __shared__ __attribute__((aligned(16))) char smem[];
  const int tid = threadIdx.x;
  TS(0);
  const int xcd = blockIdx.x & 7, idx = blockIdx.x >> 3;
  const int mt = 8 * (xcd >> 1) + (idx & 7);
  const int nt = 4 * (xcd & 1) + (idx >> 3);
  const int fs = (27 * nt + 1) >> 1;
  const int nf = ((27 * (nt + 1) + 1) >> 1) - fs;
  const int m0 = 64 * mt;
  const int r = tid >> 3, c = tid & 7;
  u32x4 xa[10], ya[10];
#pragma unroll
  for (int kt = 0; kt < 10; ++kt) {
    xa[kt] = *(const u32x4*)(x0 + (size_t)(m0 + r) * D_ + 64 * kt + 8 * c);
    ya[kt] = *(const u32x4*)(Yb + (size_t)(m0 + r) * D_ + 64 * kt + 8 * c);
  }
  float s0 = 0.f, s1 = 0.f;
  {
    const f32x4* p = (const f32x4*)(xst + (size_t)(m0 + r) * 16);
#pragma unroll
    for (int i = 0; i < 4; ++i) { f32x4 v = p[i]; s0 += v[0] + v[2]; s1 += v[1] + v[3]; }
  }
  const int nrows = 16 * nf;
  int brow[4];
#pragma unroll
  for (int i = 0; i < 4; ++i) { int rr = r + 64 * i; brow[i] = rr < nrows ? rr : nrows - 1; }
  const bool b3 = tid < 256;
  constexpr int PD = 3;
  u32x4 wb[PD][4];
  auto loadB = [&](int slot, int kt) {
#pragma unroll
    for (int i = 0; i < 4; ++i)
      if (i < 3 || b3) wb[slot][i] = *(const u32x4*)(winT + (size_t)(16 * fs + brow[i]) * D_ + 64 * kt + 8 * c);
  };
  auto writeB = [&](int slot, int buf) {
#pragma unroll
    for (int i = 0; i < 4; ++i) {
      int rr = r + 64 * i;
      if (i < 3 || b3) *(u32x4*)(smem + G1_OFF_B + buf * 28672 + rr * 128 + ((c ^ (rr & 7)) << 4)) = wb[slot][i];
    }
  };
#pragma unroll
  for (int d = 0; d < PD; ++d) loadB(d, d);

  const f32x2 st = ln_stats(s0, s1, 1.0f / D_);
  TS(1);
  float t0 = 0.f, t1 = 0.f;
#pragma unroll
  for (int kt = 0; kt < 10; ++kt) {
    u32x4 o;
#pragma unroll
    for (int i = 0; i < 4; ++i) {
      float a = lo16(xa[kt][i]), b = hi16(xa[kt][i]);
      float ya_ = lo16(ya[kt][i]), yb_ = hi16(ya[kt][i]);
      float da = rbf(rbf(a - st[0]) * st[1]), db = rbf(rbf(b - st[0]) * st[1]);
      float ta = rbf(da * ya_), tb = rbf(db * yb_);
      uint32_t w = pk2(a + ta, b + tb);
      o[i] = w;
      float xa_ = lo16(w), xb_ = hi16(w);
      t0 += xa_ + xb_; t1 += xa_ * xa_ + xb_ * xb_;
    }
    *(u32x4*)(smem + G1_OFF_A + kt * 8192 + r * 128 + ((c ^ (r & 7)) << 4)) = o;
    if (nt == 0) *(u32x4*)(x1out + (size_t)(m0 + r) * D_ + 64 * kt + 8 * c) = o;
  }
  t0 += __shfl_xor(t0, 1); t0 += __shfl_xor(t0, 2); t0 += __shfl_xor(t0, 4);
  t1 += __shfl_xor(t1, 1); t1 += __shfl_xor(t1, 2); t1 += __shfl_xor(t1, 4);
  if (c == 0) {
    f32x2 s = ln_stats(t0, t1, 1.0f / D_);
    *(f32x2*)(smem + G1_OFF_RS + r * 8) = s;
  }
  TS(2);
  writeB(0, 0);
  loadB(0, PD);
  __syncthreads();
  TS(3);

  const int wid = tid >> 6, lane = tid & 63, fr = lane & 15, g = lane >> 4;
  const int wm = wid & 1, wn = wid >> 1;
  f32x4 acc[4][2] = {};
  int wrow[4];
#pragma unroll
  for (int i = 0; i < 4; ++i) { int fi = 4 * wn + i; wrow[i] = 16 * (fi < nf ? fi : nf - 1) + fr; }
#pragma unroll
  for (int kt = 0; kt < 10; ++kt) {
    const int buf = kt & 1;
    if (kt + 1 < 10) {
      writeB((kt + 1) % PD, buf ^ 1);
      if (kt + 1 + PD < 10) loadB((kt + 1) % PD, kt + 1 + PD);
    }
#pragma unroll
    for (int ks = 0; ks < 2; ++ks) {
      bf16x8 X[2], W[4];
#pragma unroll
      for (int i = 0; i < 2; ++i) {
        int rr = 32 * wm + 16 * i + fr;
        X[i] = *(const bf16x8*)(smem + G1_OFF_A + kt * 8192 + rr * 128 + (((4 * ks + g) ^ (rr & 7)) << 4));
      }
#pragma unroll
      for (int i = 0; i < 4; ++i)
        W[i] = *(const bf16x8*)(smem + G1_OFF_B + buf * 28672 + wrow[i] * 128 + (((4 * ks + g) ^ (wrow[i] & 7)) << 4));
#pragma unroll
      for (int i = 0; i < 4; ++i)
#pragma unroll
        for (int j = 0; j < 2; ++j) acc[i][j] = __builtin_amdgcn_mfma_f32_16x16x32_bf16(W[i], X[j], acc[i][j], 0, 0, 0);
    }
    __syncthreads();
  }
  TS(4);
  float zs0[2] = {0.f, 0.f}, zs1[2] = {0.f, 0.f};
#pragma unroll
  for (int i = 0; i < 4; ++i) {
    const int fi = 4 * wn + i;
    if (fi < nf) {
      const int f = 16 * (fs + fi) + 4 * g;
      f32x4 csv = *(const f32x4*)(cs + f);
#pragma unroll
      for (int j = 0; j < 2; ++j) {
        const int rr = 32 * wm + 16 * j + fr;
        f32x2 rs = *(const f32x2*)(smem + G1_OFF_RS + rr * 8);
        float o[4];
#pragma unroll
        for (int e = 0; e < 4; ++e) {
          float pre = rbf((acc[i][j][e] - rs[0] * csv[e]) * rs[1]);
          o[e] = gelu_tanh(pre);
        }
        u32x2 w = {pk2(o[0], o[1]), pk2(o[2], o[3])};
        *(u32x2*)(z + (size_t)(m0 + rr) * F_ + f) = w;
        if (nt >= 4) {
          float a = lo16(w[0]), b = hi16(w[0]), cc = lo16(w[1]), d = hi16(w[1]);
          zs0[j] += a + b + cc + d; zs1[j] += a * a + b * b + cc * cc + d * d;
        }
      }
    }
  }
  if (nt >= 4) {
#pragma unroll
    for (int j = 0; j < 2; ++j) {
      zs0[j] += __shfl_xor(zs0[j], 16); zs0[j] += __shfl_xor(zs0[j], 32);
      zs1[j] += __shfl_xor(zs1[j], 16); zs1[j] += __shfl_xor(zs1[j], 32);
      if (g == 0) {
        f32x2 o = {zs0[j], zs1[j]};
        *(f32x2*)(smem + G1_OFF_ZS + (wn * 64 + 32 * wm + 16 * j + fr) * 8) = o;
      }
    }
    __syncthreads();
    if (tid < 64) {
      float a = 0.f, b = 0.f;
#pragma unroll
      for (int w = 0; w < 4; ++w) { f32x2 v = *(const f32x2*)(smem + G1_OFF_ZS + (w * 64 + tid) * 8); a += v[0]; b += v[1]; }
      f32x2 o = {a, b};
      *(f32x2*)(zst + ((size_t)(m0 + tid) * 4 + (nt - 4)) * 2) = o;
    }
  }
  TS(5);
}

constexpr int G2_NS = 6;
constexpr int G2_OFF_A = 0;
constexpr int G2_OFF_B = G2_NS * 16384;
constexpr int G2_OFF_ST = G2_OFF_B + 3 * 8192;
constexpr int G2_LDS = G2_OFF_ST + 16384;

__device__ __forceinline__ void glds16(const void* gsrc, uint32_t lds_dst) {
  uint32_t keep;
  asm volatile("s_mov_b32 %0, m0\n\ts_mov_b32 m0, %2\n\ts_nop 0\n\tglobal_load_lds_dwordx4 %1, off\n\ts_mov_b32 m0, %0"
               : "=&s"(keep) : "v"(gsrc), "s"(lds_dst) : "memory");
}
__device__ __forceinline__ uint32_t lds_addr(const void* p) {
  return (uint32_t)(uintptr_t)(const __attribute__((address_space(3))) char*)p;
}
__device__ __forceinline__ void touch(const void* p, uint32_t& sink) {
  asm volatile("global_load_ushort %0, %1, off" : "+v"(sink) : "v"(p) : "memory");
}
#define WAIT_VM(n) asm volatile("s_waitcnt vmcnt(" #n ")" ::: "memory")
#define WAIT_LGKM0() asm volatile("s_waitcnt lgkmcnt(0)" ::: "memory")
#define RAW_BAR() __builtin_amdgcn_s_barrier()

__global__ __launch_bounds__(768, 1) void g2_kernel(
    const bf16_t* __restrict__ wsp, const bf16_t* __restrict__ z, const float* __restrict__ zst,
    const bf16_t* __restrict__ bsp, bf16_t* __restrict__ u TSP) {
  extern __shared__ __attribute__((aligned(16))) char smem[];
  const int tid = threadIdx.x;
  TS(0);
  const int xcd = blockIdx.x & 7, idx = blockIdx.x >> 3;
  const int mt = 4 * (xcd >> 1) + (idx & 3);
  const int nt = 7 * (xcd & 1) + (idx >> 2);
  const int t0 = 128 * mt, f0 = 64 * nt;
  const int wid = __builtin_amdgcn_readfirstlane(tid >> 6), lane = tid & 63;
  const int rot = idx;
  if (wid >= 8) {
    const int pt = tid - 512;
    const int r = pt >> 3, c = pt & 7;
    constexpr int PD = 8;
    u32x4 rb[PD][2];
    const bf16_t* zp = z + (size_t)r * F_ + FH_ + f0 + 8 * c;
    auto loadB = [&](int slot, int kt) {
      const int ke = (kt + rot) & 31;
      rb[slot][0] = *(const u32x4*)(zp + (size_t)(64 * ke) * F_);
      rb[slot][1] = *(const u32x4*)(zp + (size_t)(64 * ke + 32) * F_);
    };
    const int bswz0 = (((r >> 1) & 1) | (((r >> 3) & 1) << 1)) << 1;
    u32x4 nb[2];
    auto calcB = [&](int slot, int kt) {
#pragma unroll
      for (int h = 0; h < 2; ++h) {
        f32x2 st = *(const f32x2*)(smem + G2_OFF_ST + (64 * ((kt + rot) & 31) + r + 32 * h) * 8);
#pragma unroll
        for (int i = 0; i < 4; ++i) {
          float a = lo16(rb[slot][h][i]), b = hi16(rb[slot][h][i]);
          nb[h][i] = pk2(fmaf(a, st[0], st[1]), fmaf(b, st[0], st[1]));
        }
      }
    };
    auto putB = [&](int bslot) {
#pragma unroll
      for (int h = 0; h < 2; ++h)
        *(u32x4*)(smem + G2_OFF_B + bslot * 8192 + (r + 32 * h) * 128 + ((c ^ bswz0) << 4)) = nb[h];
    };
    {
      f32x4 sv[16];
      const f32x4* p = (const f32x4*)(zst + (size_t)pt * 64);
#pragma unroll
      for (int i = 0; i < 16; ++i) sv[i] = p[i];
#pragma unroll
      for (int i = 0; i < 8; ++i) {
        f32x4 a = sv[2 * i], b = sv[2 * i + 1];
        f32x2 s = ln_stats(a[0] + a[2] + b[0] + b[2], a[1] + a[3] + b[1] + b[3], 1.0f / FH_);
        f32x2 s2 = {s[1], -s[0] * s[1]};
        *(f32x2*)(smem + G2_OFF_ST + (8 * pt + i) * 8) = s2;
      }
    }
#pragma unroll
    for (int d = 0; d < PD; ++d) loadB(d, d);
    WAIT_LGKM0();
    RAW_BAR();
    calcB(0, 0);
    putB(0);
    loadB(0, PD);
    calcB(1, 1);
    loadB(1, PD + 1);
    WAIT_LGKM0();
    RAW_BAR();
    putB(1);
    calcB(2, 2);
    loadB(2, PD + 2);
    WAIT_LGKM0();
    int bs = 2;
#pragma unroll 1
    for (int kt0 = 0; kt0 < 32; kt0 += PD) {
#pragma unroll
      for (int kk = 0; kk < PD; ++kk) {
        const int kt = kt0 + kk;
        RAW_BAR();
        if (kt + 2 < 32) putB(bs);
        if (kt + 3 < 32) {
          calcB((kk + 3) % PD, kt + 3);
          if (kt + 3 + PD < 32) loadB((kk + 3) % PD, kt + 3 + PD);
        }
        bs = bs == 2 ? 0 : bs + 1;
        WAIT_LGKM0();
      }
    }
    return;
  }
  const int lr = lane >> 3, lc = (lane & 7) ^ lr;
  const bf16_t* ap0 = wsp + (size_t)(t0 + 16 * wid + lr) * S_ + 8 * lc;
  const bf16_t* ap1 = ap0 + (size_t)8 * S_;
  const uint32_t lbase = lds_addr(smem) + G2_OFF_A + wid * 2048;
  auto issueA = [&](int kt, int slot) {
    const int ke = (kt + rot) & 31;
    glds16(ap0 + 64 * ke, lbase + slot * 16384);
    glds16(ap1 + 64 * ke, lbase + slot * 16384 + 1024);
  };
#pragma unroll
  for (int s = 0; s < G2_NS - 1; ++s) issueA(s, s);
  RAW_BAR();
  TS(1);
  const int fr = lane & 15, g = lane >> 4;
  const int wm = wid & 3, wn = wid >> 2;
  f32x4 acc[2][2] = {};
  const int q4 = (lane & 15) >> 2, p4 = lane & 3;
  int aoff[2][2], boff[2][2][2];
#pragma unroll
  for (int ks = 0; ks < 2; ++ks) {
#pragma unroll
    for (int i = 0; i < 2; ++i) {
      int rr = 32 * wm + 16 * i + fr;
      aoff[ks][i] = G2_OFF_A + rr * 128 + (((4 * ks + g) ^ (rr & 7)) << 4);
#pragma unroll
      for (int t = 0; t < 2; ++t) {
        int row = 32 * ks + 8 * g + 4 * t + q4;
        int ch = 2 * (2 * wn + i) + (p4 >> 1);
        int sw = (((row >> 1) & 1) | (((row >> 3) & 1) << 1)) << 1;
        boff[ks][i][t] = G2_OFF_B + row * 128 + ((ch ^ sw) << 4) + 8 * (p4 & 1);
      }
    }
  }
  auto readF = [&](bf16x8* T, bf16x8* Z, int ks, int aslot, int bslot) {
#pragma unroll
    for (int i = 0; i < 2; ++i) T[i] = *(const bf16x8*)(smem + aslot * 16384 + aoff[ks][i]);
#pragma unroll
    for (int i = 0; i < 2; ++i) {
      typedef __attribute__((address_space(3))) s16x4 lds_s16x4;
      s16x4 h0 = __builtin_amdgcn_ds_read_tr16_b64_v4i16((lds_s16x4*)(smem + bslot * 8192 + boff[ks][i][0]));
      s16x4 h1 = __builtin_amdgcn_ds_read_tr16_b64_v4i16((lds_s16x4*)(smem + bslot * 8192 + boff[ks][i][1]));
      Z[i] = __builtin_shufflevector(h0, h1, 0, 1, 2, 3, 4, 5, 6, 7);
    }
  };
  auto mm = [&](const bf16x8* T, const bf16x8* Z) {
#pragma unroll
    for (int i = 0; i < 2; ++i)
#pragma unroll
      for (int j = 0; j < 2; ++j) acc[i][j] = __builtin_amdgcn_mfma_f32_16x16x32_bf16(Z[i], T[j], acc[i][j], 0, 0, 0);
  };
  bf16x8 TA[2][2], ZA[2][2], TB[2][2], ZB[2][2];
  WAIT_VM(8);
  RAW_BAR();
  readF(TA[0], ZA[0], 0, 0, 0);
  readF(TA[1], ZA[1], 1, 0, 0);
  int slot = 0, bs = 0;
  auto step = [&](int kt, bf16x8 (*Tc)[2], bf16x8 (*Zc)[2], bf16x8 (*Tn)[2], bf16x8 (*Zn)[2]) {
    WAIT_VM(6);
    RAW_BAR();
    {
      int kn = kt + G2_NS - 1; kn = kn < 32 ? kn : 31;
      int sn = slot == 0 ? G2_NS - 1 : slot - 1;
      issueA(kn, sn);
    }
    const int slot1 = slot + 1 == G2_NS ? 0 : slot + 1;
    const int bs1 = bs == 2 ? 0 : bs + 1;
    if (wn == 0) {
      readF(Tn[0], Zn[0], 0, slot1, bs1);
      readF(Tn[1], Zn[1], 1, slot1, bs1);
      __builtin_amdgcn_sched_barrier(0);
      mm(Tc[0], Zc[0]);
      mm(Tc[1], Zc[1]);
    } else {
      mm(Tc[0], Zc[0]);
      mm(Tc[1], Zc[1]);
      __builtin_amdgcn_sched_barrier(0);
      readF(Tn[0], Zn[0], 0, slot1, bs1);
      readF(Tn[1], Zn[1], 1, slot1, bs1);
    }
    __builtin_amdgcn_sched_barrier(0);
    slot = slot1; bs = bs1;
  };
#pragma unroll 1
  for (int kt = 0; kt < 32; kt += 2) {
    step(kt, TA, ZA, TB, ZB);
    step(kt + 1, TB, ZB, TA, ZA);
  }
  WAIT_VM(0);
  TS(3);
#pragma unroll
  for (int j = 0; j < 2; ++j) {
    const int t = t0 + 32 * wm + 16 * j + fr;
    const float bb = bf2f(bsp[t]);
#pragma unroll
    for (int i = 0; i < 2; ++i) {
      const int f = f0 + 32 * wn + 16 * i + 4 * g;
      u32x2 w = {0u, 0u};
      if (f < FH_) {
        u32x2 z1 = *(const u32x2*)(z + (size_t)t * F_ + f);
        float g0 = rbf(rbf(acc[i][j][0]) + bb), g1 = rbf(rbf(acc[i][j][1]) + bb);
        float g2 = rbf(rbf(acc[i][j][2]) + bb), g3 = rbf(rbf(acc[i][j][3]) + bb);
        w[0] = pk2(lo16(z1[0]) * g0, hi16(z1[0]) * g1);
        w[1] = pk2(lo16(z1[1]) * g2, hi16(z1[1]) * g3);
      }
      *(u32x2*)(u + (size_t)t * FHP_ + f) = w;
    }
  }
  TS(4);
}

constexpr int G3_OFF_A = 0;
constexpr int G3_OFF_B = 16384;
constexpr int G3_LDS = 36864;

__global__ __launch_bounds__(256, 1) void g3_kernel(
    const bf16_t* __restrict__ u, const bf16_t* __restrict__ woutT, const bf16_t* __restrict__ x1,
    bf16_t* __restrict__ xout, float* __restrict__ xst TSP) {
  extern __shared__ __attribute__((aligned(16))) char smem[];
  const int tid = threadIdx.x;
  TS(0);
  const int xcd = blockIdx.x & 7, idx = blockIdx.x >> 3;
  const int mt = 8 * (xcd >> 1) + (idx & 7);
  const int nt = 4 * (xcd & 1) + (idx >> 3);
  const int t0 = 64 * mt, d0 = 80 * nt;
  const int r = tid >> 3, c = tid & 7;
  constexpr int PD = 4;
  u32x4 ra[PD][2], rb[PD][3];
  const bool b2 = tid < 128;
  auto loadS = [&](int slot, int kt) {
    ra[slot][0] = *(const u32x4*)(u + (size_t)(t0 + r) * FHP_ + 64 * kt + 8 * c);
    ra[slot][1] = *(const u32x4*)(u + (size_t)(t0 + r + 32) * FHP_ + 64 * kt + 8 * c);
    rb[slot][0] = *(const u32x4*)(woutT + (size_t)(d0 + r) * FHP_ + 64 * kt + 8 * c);
    rb[slot][1] = *(const u32x4*)(woutT + (size_t)(d0 + r + 32) * FHP_ + 64 * kt + 8 * c);
    if (b2) rb[slot][2] = *(const u32x4*)(woutT + (size_t)(d0 + r + 64) * FHP_ + 64 * kt + 8 * c);
  };
  auto writeS = [&](int slot, int buf) {
    const int sw = (c ^ (r & 7)) << 4;
    *(u32x4*)(smem + G3_OFF_A + buf * 8192 + r * 128 + sw) = ra[slot][0];
    *(u32x4*)(smem + G3_OFF_A + buf * 8192 + (r + 32) * 128 + sw) = ra[slot][1];
    *(u32x4*)(smem + G3_OFF_B + buf * 10240 + r * 128 + sw) = rb[slot][0];
    *(u32x4*)(smem + G3_OFF_B + buf * 10240 + (r + 32) * 128 + sw) = rb[slot][1];
    if (b2) *(u32x4*)(smem + G3_OFF_B + buf * 10240 + (r + 64) * 128 + sw) = rb[slot][2];
  };
#pragma unroll
  for (int d = 0; d < PD; ++d) loadS(d, d);
  writeS(0, 0);
  loadS(0, PD);
  __syncthreads();
  TS(1);
  const int wid = tid >> 6, lane = tid & 63, fr = lane & 15, g = lane >> 4;
  f32x4 acc[5] = {};
#pragma unroll
  for (int kt = 0; kt < 14; ++kt) {
    const int buf = kt & 1;
    if (kt + 1 < 14) {
      writeS((kt + 1) % PD, buf ^ 1);
      if (kt + 1 + PD < 14) loadS((kt + 1) % PD, kt + 1 + PD);
    }
#pragma unroll
    for (int ks = 0; ks < 2; ++ks) {
      const int rr = 16 * wid + fr;
      bf16x8 T = *(const bf16x8*)(smem + G3_OFF_A + buf * 8192 + rr * 128 + (((4 * ks + g) ^ (rr & 7)) << 4));
#pragma unroll
      for (int i = 0; i < 5; ++i) {
        const int wr = 16 * i + fr;
        bf16x8 W = *(const bf16x8*)(smem + G3_OFF_B + buf * 10240 + wr * 128 + (((4 * ks + g) ^ (wr & 7)) << 4));
        acc[i] = __builtin_amdgcn_mfma_f32_16x16x32_bf16(W, T, acc[i], 0, 0, 0);
      }
    }
    __syncthreads();
  }
  TS(2);
  const int t = t0 + 16 * wid + fr;
  float s0 = 0.f, s1 = 0.f;
#pragma unroll
  for (int i = 0; i < 5; ++i) {
    const int d = d0 + 16 * i + 4 * g;
    u32x2 xr = *(const u32x2*)(x1 + (size_t)t * D_ + d);
    u32x2 w = {pk2(lo16(xr[0]) + rbf(acc[i][0]), hi16(xr[0]) + rbf(acc[i][1])),
               pk2(lo16(xr[1]) + rbf(acc[i][2]), hi16(xr[1]) + rbf(acc[i][3]))};
    *(u32x2*)(xout + (size_t)t * D_ + d) = w;
    float a = lo16(w[0]), b = hi16(w[0]), cc = lo16(w[1]), dd = hi16(w[1]);
    s0 += a + b + cc + dd; s1 += a * a + b * b + cc * cc + dd * dd;
  }
  s0 += __shfl_xor(s0, 16); s0 += __shfl_xor(s0, 32);
  s1 += __shfl_xor(s1, 16); s1 += __shfl_xor(s1, 32);
  if (g == 0) {
    f32x2 o = {s0, s1};
    *(f32x2*)(xst + ((size_t)t * 8 + nt) * 2) = o;
  }
  TS(3);
}

extern "C" void kernel_launch(void* const* d_in, const int* in_sizes, int n_in,
                              void* d_out, int out_size, void* d_ws, size_t ws_size,
                              hipStream_t stream) {
  const bf16_t* q = (const bf16_t*)d_in[0];
  const bf16_t* k = (const bf16_t*)d_in[1];
  const bf16_t* v = (const bf16_t*)d_in[2];
  const bf16_t* shift_k = (const bf16_t*)d_in[3];
  const bf16_t* ssm_k = (const bf16_t*)d_in[4];
  const bf16_t* ssm_D = (const bf16_t*)d_in[5];
  const bf16_t* w_in = (const bf16_t*)d_in[7];
  const bf16_t* w_sp = (const bf16_t*)d_in[8];
  const bf16_t* b_sp = (const bf16_t*)d_in[9];
  const bf16_t* w_out = (const bf16_t*)d_in[10];
  bf16_t* out = (bf16_t*)d_out;

  char* ws = (char*)d_ws;
  size_t off = 0;
  auto alloc = [&](size_t bytes) { char* p = ws + off; off += (bytes + 255) & ~(size_t)255; return p; };
  bf16_t* winT = (bf16_t*)alloc((size_t)F_ * D_ * 2);
  float* cs = (float*)alloc(F_ * 4);
  bf16_t* woutT = (bf16_t*)alloc((size_t)D_ * FHP_ * 2);
  bf16_t* Y = (bf16_t*)alloc((size_t)S_ * D_ * 2);
  bf16_t* kvT = (bf16_t*)alloc((size_t)S_ * D_ * 2);
  bf16_t* YT = (bf16_t*)alloc((size_t)S_ * D_ * 2);
  float* xst0 = (float*)alloc((size_t)S_ * 16 * 4);
  float* xst1 = (float*)alloc((size_t)S_ * 16 * 4);
  float* xst2 = (float*)alloc((size_t)S_ * 16 * 4);
  bf16_t* x1 = (bf16_t*)alloc((size_t)S_ * D_ * 2);
  bf16_t* x2 = (bf16_t*)alloc((size_t)S_ * D_ * 2);
  bf16_t* x3 = (bf16_t*)alloc((size_t)S_ * D_ * 2);
  bf16_t* z = (bf16_t*)alloc((size_t)S_ * F_ * 2 + 4096);
  float* zst = (float*)alloc((size_t)S_ * 8 * 4);
  bf16_t* u = (bf16_t*)alloc((size_t)S_ * FHP_ * 2);

  (void)hipFuncSetAttribute((const void*)kconv_kernel, hipFuncAttributeMaxDynamicSharedMemorySize, CV_LDS);
  (void)hipFuncSetAttribute((const void*)g1_kernel, hipFuncAttributeMaxDynamicSharedMemorySize, G1_LDS);
  (void)hipFuncSetAttribute((const void*)g2_kernel, hipFuncAttributeMaxDynamicSharedMemorySize, G2_LDS);
  (void)hipFuncSetAttribute((const void*)g3_kernel, hipFuncAttributeMaxDynamicSharedMemorySize, G3_LDS);

  kpre_kernel<<<320, 256, 0, stream>>>(k, v, shift_k, kvT);
  kconv_kernel<<<320 + 270 + 140 + 27 + 64, 256, CV_LDS, stream>>>(kvT, ssm_k, ssm_D, YT, w_in, w_out, q, winT, cs, woutT, xst0 TSA(0));
  kt_kernel<<<320, 256, 0, stream>>>(YT, Y);
  g1_kernel<<<256, 512, G1_LDS, stream>>>(q, Y, xst0, winT, cs, x1, z, zst TSA(1));
  g2_kernel<<<224, 768, G2_LDS, stream>>>(w_sp, z, zst, b_sp, u TSA(2));
  g3_kernel<<<256, 256, G3_LDS, stream>>>(u, woutT, x1, x2, xst1 TSA(3));
  g1_kernel<<<256, 512, G1_LDS, stream>>>(x2, Y, xst1, winT, cs, x3, z, zst TSA(4));
  g2_kernel<<<224, 768, G2_LDS, stream>>>(w_sp, z, zst, b_sp, u TSA(5));
  g3_kernel<<<256, 256, G3_LDS, stream>>>(u, woutT, x3, out, xst2 TSA(6));
}
```

```cpp
#include <hip/hip_runtime.h>
#include <hip/hip_bf16.h>
#include <stdint.h>

typedef __attribute__((ext_vector_type(8))) short bf16x8;
typedef __attribute__((ext_vector_type(4))) short s16x4;
typedef __attribute__((ext_vector_type(4))) float f32x4;
typedef __attribute__((ext_vector_type(16))) float f32x16;
typedef __attribute__((ext_vector_type(2))) float f32x2;
typedef __attribute__((ext_vector_type(2))) __bf16 bf16x2_t;
typedef __attribute__((ext_vector_type(4))) uint32_t u32x4;
typedef __attribute__((ext_vector_type(2))) uint32_t u32x2;
typedef uint16_t bf16_t;

#define TSP
#define TSA(k)
#define TS(i)

constexpr int S_ = 2048, D_ = 640, F_ = 1728, FH_ = 864, FHP_ = 896;

__device__ __forceinline__ uint32_t pk2(float lo, float hi) {
  f32x2 f = {lo, hi};
  bf16x2_t b = __builtin_convertvector(f, bf16x2_t);
  return __builtin_bit_cast(uint32_t, b);
}
__device__ __forceinline__ float lo16(uint32_t v) { return __uint_as_float(v << 16); }
__device__ __forceinline__ float hi16(uint32_t v) { return __uint_as_float(v & 0xffff0000u); }
__device__ __forceinline__ float rbf(float x) { return lo16(pk2(x, x)); }
__device__ __forceinline__ float bf2f(bf16_t v) { return __uint_as_float(((uint32_t)v) << 16); }

__device__ __forceinline__ f32x2 ln_stats(float sum, float sumsq, float inv_n) {
  float mean = sum * inv_n;
  float var = fmaxf(sumsq * inv_n - mean * mean, 0.f);
  float mb = rbf(mean);
  float vb = rbf(var);
  float sq = rbf(sqrtf(rbf(vb + 1e-5f)));
  f32x2 r = {mb, 1.0f / sq};
  return r;
}

constexpr int TP = 144;
__device__ __forceinline__ void tile_transpose_out(const char* T, bf16_t* __restrict__ dst, size_t dstride, int tid) {
  const int w = tid >> 6, lane = tid & 63, gq = lane >> 4, i = lane & 15;
  typedef __attribute__((address_space(3))) s16x4 lds_s16x4;
#pragma unroll
  for (int h = 0; h < 2; ++h) {
    const int R0 = 8 * gq + 32 * h;
    const char* p = T + (R0 + (i >> 2)) * TP + (16 * w + 4 * (i & 3)) * 2;
    s16x4 a = __builtin_amdgcn_ds_read_tr16_b64_v4i16((lds_s16x4*)p);
    s16x4 b = __builtin_amdgcn_ds_read_tr16_b64_v4i16((lds_s16x4*)(p + 4 * TP));
    bf16x8 o = __builtin_shufflevector(a, b, 0, 1, 2, 3, 4, 5, 6, 7);
    *(bf16x8*)(dst + (size_t)(16 * w + i) * dstride + R0) = o;
  }
}

__global__ __launch_bounds__(256) void kpre_kernel(
    const bf16_t* __restrict__ kin, const bf16_t* __restrict__ vin, const bf16_t* __restrict__ shift_k,
    bf16_t* __restrict__ kvT) {
  __shared__ __attribute__((aligned(16))) char T[64 * TP];
  const int tid = threadIdx.x;
  int j = blockIdx.x;
  const int r = tid >> 2, cq = tid & 3;
  {
    const int t0 = 64 * (j & 31), d0 = 64 * (j >> 5);
    const int t = t0 + r;
    u32x4 kr[2][4], vr[2], skr[2][4];
#pragma unroll
    for (int h = 0; h < 2; ++h) {
      const int d = d0 + 8 * (cq + 4 * h);
#pragma unroll
      for (int i = 0; i < 4; ++i) {
        u32x4 zz = {0u, 0u, 0u, 0u};
        kr[h][i] = (t - 3 + i >= 0) ? *(const u32x4*)(kin + (size_t)(t - 3 + i) * D_ + d) : zz;
      }
      vr[h] = *(const u32x4*)(vin + (size_t)t * D_ + d);
#pragma unroll
      for (int e2 = 0; e2 < 4; ++e2) skr[h][e2] = *(const u32x4*)(shift_k + (size_t)(d + 2 * e2) * 4);
    }
#pragma unroll
    for (int h = 0; h < 2; ++h) {
      u32x4 o;
#pragma unroll
      for (int e2 = 0; e2 < 4; ++e2) {
        const u32x4 sk = skr[h][e2];
        float res[2];
#pragma unroll
        for (int e = 0; e < 2; ++e) {
          float w0 = lo16(sk[2 * e]), w1 = hi16(sk[2 * e]), w2 = lo16(sk[2 * e + 1]), w3 = hi16(sk[2 * e + 1]);
          float k0 = e ? hi16(kr[h][0][e2]) : lo16(kr[h][0][e2]);
          float k1 = e ? hi16(kr[h][1][e2]) : lo16(kr[h][1][e2]);
          float k2 = e ? hi16(kr[h][2][e2]) : lo16(kr[h][2][e2]);
          float k3 = e ? hi16(kr[h][3][e2]) : lo16(kr[h][3][e2]);
          float ks = rbf(k0 * w3);
          ks = rbf(ks + rbf(k1 * w2));
          ks = rbf(ks + rbf(k2 * w1));
          ks = rbf(ks + rbf(k3 * w0));
          float vv = e ? hi16(vr[h][e2]) : lo16(vr[h][e2]);
          res[e] = ks * vv;
        }
        o[e2] = pk2(res[0], res[1]);
      }
      *(u32x4*)(T + r * TP + (cq + 4 * h) * 16) = o;
    }
    __syncthreads();
    tile_transpose_out(T, kvT + (size_t)d0 * S_ + t0, S_, tid);
  }
}

__device__ __forceinline__ void prep_job(int j, int tid, char* T, const bf16_t* __restrict__ w_in, const bf16_t* __restrict__ w_out,
                                         const bf16_t* __restrict__ q, bf16_t* __restrict__ winT, float* __restrict__ cs,
                                         bf16_t* __restrict__ woutT, float* __restrict__ xst) {
  const int r = tid >> 2, cq = tid & 3;
  if (j < 270) {
    const int d0 = 64 * (j % 10), f0 = 64 * (j / 10);
#pragma unroll
    for (int h = 0; h < 2; ++h)
      *(u32x4*)(T + r * TP + (cq + 4 * h) * 16) = *(const u32x4*)(w_in + (size_t)(d0 + r) * F_ + f0 + 8 * (cq + 4 * h));
    __syncthreads();
    tile_transpose_out(T, winT + (size_t)f0 * D_ + d0, D_, tid);
    return;
  }
  j -= 270;
  if (j < 140) {
    const int d0 = 64 * (j % 10), f0 = 64 * (j / 10);
#pragma unroll
    for (int h = 0; h < 2; ++h) {
      u32x4 zz = {0u, 0u, 0u, 0u};
      *(u32x4*)(T + r * TP + (cq + 4 * h) * 16) =
          (f0 + r < FH_) ? *(const u32x4*)(w_out + (size_t)(f0 + r) * D_ + d0 + 8 * (cq + 4 * h)) : zz;
    }
    __syncthreads();
    tile_transpose_out(T, woutT + (size_t)d0 * FHP_ + f0, FHP_, tid);
    return;
  }
  j -= 140;
  if (j < 27) {
    const int f0 = 64 * j, c8 = tid & 7, rr = tid >> 3;
    float s[8] = {0.f, 0.f, 0.f, 0.f, 0.f, 0.f, 0.f, 0.f};
    u32x4 w[20];
#pragma unroll
    for (int it = 0; it < 20; ++it) w[it] = *(const u32x4*)(w_in + (size_t)(32 * it + rr) * F_ + f0 + 8 * c8);
#pragma unroll
    for (int it = 0; it < 20; ++it) {
#pragma unroll
      for (int i = 0; i < 4; ++i) { s[2 * i] += lo16(w[it][i]); s[2 * i + 1] += hi16(w[it][i]); }
    }
    float* R = (float*)T;
#pragma unroll
    for (int i = 0; i < 8; ++i) R[rr * 64 + 8 * c8 + i] = s[i];
    __syncthreads();
    if (tid < 64) {
      float a = 0.f;
#pragma unroll
      for (int i = 0; i < 32; ++i) a += R[i * 64 + tid];
      cs[f0 + tid] = a;
    }
    return;
  }
  j -= 27;
  {
    const int r8 = tid >> 3, c = tid & 7;
    const int row = 32 * j + r8;
    float sum = 0.f, sq = 0.f;
#pragma unroll
    for (int it = 0; it < 10; ++it) {
      u32x4 w = *(const u32x4*)(q + (size_t)row * D_ + 64 * it + 8 * c);
#pragma unroll
      for (int i = 0; i < 4; ++i) {
        float a = lo16(w[i]), b = hi16(w[i]);
        sum += a + b; sq += a * a + b * b;
      }
    }
    sum += __shfl_xor(sum, 1); sum += __shfl_xor(sum, 2); sum += __shfl_xor(sum, 4);
    sq += __shfl_xor(sq, 1); sq += __shfl_xor(sq, 2); sq += __shfl_xor(sq, 4);
    f32x2 o = {c == 0 ? sum : 0.f, c == 0 ? sq : 0.f};
    *(f32x2*)(xst + ((size_t)row * 8 + c) * 2) = o;
  }
}

__global__ __launch_bounds__(256) void kt_kernel(const bf16_t* __restrict__ YT, bf16_t* __restrict__ Y) {
  __shared__ __attribute__((aligned(16))) char T[64 * TP];
  const int tid = threadIdx.x;
  const int j = blockIdx.x;
  const int r = tid >> 2, cq = tid & 3;
  const int t0 = 64 * (j & 31), d0 = 64 * (j >> 5);
#pragma unroll
  for (int h = 0; h < 2; ++h)
    *(u32x4*)(T + r * TP + (cq + 4 * h) * 16) = *(const u32x4*)(YT + (size_t)(d0 + r) * S_ + t0 + 8 * (cq + 4 * h));
  __syncthreads();
  tile_transpose_out(T, Y + (size_t)t0 * D_ + d0, D_, tid);
}

constexpr int CV_HP = 4416;
constexpr int CV_HCH = 4 * CV_HP;
constexpr int CV_UCH = 96 * 80;
constexpr int CV_OFF_H = 0;
constexpr int CV_OFF_U = CV_OFF_H + 2 * CV_HCH;
constexpr int CV_OFF_R = CV_OFF_U + 2 * CV_UCH;
constexpr int CV_OFF_Y = CV_OFF_R + 2 * 8192;
constexpr int CV_LDS = CV_OFF_Y + 2 * 4096;

__global__ __launch_bounds__(256, 2) void kconv_kernel(
    const bf16_t* __restrict__ kvT, const bf16_t* __restrict__ ssm_k, const bf16_t* __restrict__ ssm_D,
    bf16_t* __restrict__ YT, const bf16_t* __restrict__ w_in, const bf16_t* __restrict__ w_out, const bf16_t* __restrict__ q,
    bf16_t* __restrict__ winT, float* __restrict__ cs, bf16_t* __restrict__ woutT, float* __restrict__ xst TSP) {
  extern __shared__ __attribute__((aligned(16))) char smem[];
  const int tid = threadIdx.x;
  if (blockIdx.x >= 320) {
    prep_job(blockIdx.x - 320, tid, smem, w_in, w_out, q, winT, cs, woutT, xst);
    return;
  }
  TS(0);
  const int c0 = 2 * blockIdx.x;
  u32x4 ur[2];
#pragma unroll
  for (int i = 0; i < 2; ++i) ur[i] = *(const u32x4*)(kvT + (size_t)c0 * S_ + 8 * (tid + 256 * i));
  u32x2 hr[3][3];
#pragma unroll
  for (int rd = 0; rd < 3; ++rd) {
    int task = tid + 256 * rd;
    int ch = task / 262, c = task % 262 - 6;
#pragma unroll
    for (int i = 0; i < 3; ++i) {
      int idx = 8 * c + 4 * i;
      u32x2 z = {0u, 0u};
      hr[rd][i] = (task < 524 && idx >= 0 && idx < 2048) ? *(const u32x2*)(ssm_k + (size_t)(c0 + ch) * S_ + idx) : z;
    }
  }
  for (int i = tid; i < 2 * 160; i += 256) {
    int ch = i / 160, o = i % 160;
    u32x4 z = {0u, 0u, 0u, 0u};
    *(u32x4*)(smem + CV_OFF_U + ch * CV_UCH + o * 16) = z;
  }
  TS(1);
#pragma unroll
  for (int i = 0; i < 2; ++i) {
    const int e = 8 * (tid + 256 * i), ch = e >> 11, s = e & 2047;
    *(u32x4*)(smem + CV_OFF_U + ch * CV_UCH + ((s >> 5) + 32) * 80 + (s & 31) * 2) = ur[i];
  }
#pragma unroll
  for (int rd = 0; rd < 3; ++rd) {
    int task = tid + 256 * rd;
    if (task < 524) {
      int ch = task / 262, c = task % 262 - 6;
      uint32_t w[6] = {hr[rd][0][0], hr[rd][0][1], hr[rd][1][0], hr[rd][1][1], hr[rd][2][0], hr[rd][2][1]};
#pragma unroll
      for (int p = 0; p < 4; ++p) {
#pragma unroll
        for (int gi = 0; gi < 2; ++gi) {
          const int ao = ((1 - p) & 3) + 4 * gi;
          int a = 8 * c + ao;
          int qx = 2045 - p - a;
          auto el = [&](int e) -> uint32_t { return (e & 1) ? (w[e >> 1] >> 16) : (w[e >> 1] & 0xffff); };
          u32x2 o = {el(ao + 3) | (el(ao + 2) << 16), el(ao + 1) | (el(ao) << 16)};
          if (qx >= 0 && qx < 2088) *(u32x2*)(smem + CV_OFF_H + ch * CV_HCH + p * CV_HP + qx * 2) = o;
        }
      }
    }
  }
  TS(2);
  __syncthreads();
  TS(3);
  const int wid = tid >> 6, lane = tid & 63;
  const int ch = wid & 1, kb = wid >> 1;
  const int m = lane & 31, hh = lane >> 5;
  {
    const int p = (4 - (m & 3)) & 3;
    const char* abase = smem + CV_OFF_H + ch * CV_HCH + p * CV_HP + (2048 - m + 8 * hh - p + 16 * kb) * 2;
    const char* bbase = smem + CV_OFF_U + ch * CV_UCH + (m + 32) * 80 + 32 * kb + 16 * hh;
    f32x16 acc0 = {}, acc1 = {};
    u32x2 ra[3][4];
    bf16x8 rbB[3][2];
    auto ld = [&](int sl, int dl) {
      typedef const volatile __attribute__((address_space(3))) u32x2* lds_v2;
      ra[sl][0] = *(lds_v2)(abase - 64 * dl);
      ra[sl][1] = *(lds_v2)(abase - 64 * dl + 8);
      ra[sl][2] = *(lds_v2)(abase - 64 * (dl + 32));
      ra[sl][3] = *(lds_v2)(abase - 64 * (dl + 32) + 8);
      rbB[sl][0] = *(const bf16x8*)(bbase - 80 * dl);
      rbB[sl][1] = *(const bf16x8*)(bbase + 80 * (32 - dl));
    };
    ld(0, 0);
    ld(1, 1);
#pragma unroll
    for (int dl = 0; dl < 32; ++dl) {
      if (dl + 2 < 32) ld((dl + 2) % 3, dl + 2);
      __builtin_amdgcn_sched_barrier(0);
      const int sl = dl % 3;
      u32x4 A0u = {ra[sl][0][0], ra[sl][0][1], ra[sl][1][0], ra[sl][1][1]};
      u32x4 A1u = {ra[sl][2][0], ra[sl][2][1], ra[sl][3][0], ra[sl][3][1]};
      bf16x8 A0 = __builtin_bit_cast(bf16x8, A0u), A1 = __builtin_bit_cast(bf16x8, A1u);
      acc0 = __builtin_amdgcn_mfma_f32_32x32x16_bf16(A0, rbB[sl][0], acc0, 0, 0, 0);
      acc1 = __builtin_amdgcn_mfma_f32_32x32x16_bf16(A0, rbB[sl][1], acc1, 0, 0, 0);
      acc1 = __builtin_amdgcn_mfma_f32_32x32x16_bf16(A1, rbB[sl][0], acc1, 0, 0, 0);
      __builtin_amdgcn_sched_barrier(0);
    }
    TS(4);
    float* red = (float*)(smem + CV_OFF_R + ch * 8192);
    if (kb == 1) {
#pragma unroll
      for (int r = 0; r < 16; ++r) { red[r * 64 + lane] = acc0[r]; red[(16 + r) * 64 + lane] = acc1[r]; }
    }
    __syncthreads();
    if (kb == 0) {
      const float Dv = bf2f(ssm_D[c0 + ch]);
      bf16_t* yb = (bf16_t*)(smem + CV_OFF_Y + ch * 4096);
#pragma unroll
      for (int half = 0; half < 2; ++half) {
        const int a = m + 32 * half;
#pragma unroll
        for (int rq = 0; rq < 4; ++rq) {
          const int b0 = 8 * rq + 4 * hh;
          u32x2 kvw = *(const u32x2*)(smem + CV_OFF_U + ch * CV_UCH + (a + 32) * 80 + b0 * 2);
          float kvv[4] = {lo16(kvw[0]), hi16(kvw[0]), lo16(kvw[1]), hi16(kvw[1])};
          float o[4];
#pragma unroll
          for (int i = 0; i < 4; ++i) {
            const int r = 4 * rq + i;
            float y = (half ? acc1[r] : acc0[r]) + red[(16 * half + r) * 64 + lane];
            o[i] = rbf(y) + rbf(kvv[i] * Dv);
          }
          u32x2 ow = {pk2(o[0], o[1]), pk2(o[2], o[3])};
          *(u32x2*)(yb + 32 * a + b0) = ow;
        }
      }
    }
    __syncthreads();
    TS(5);
#pragma unroll
    for (int i = 0; i < 2; ++i) {
      const int e = tid + 256 * i;
      *(u32x4*)(YT + (size_t)c0 * S_ + 8 * e) = *(const u32x4*)(smem + CV_OFF_Y + 16 * e);
    }
    TS(6);
  }
}

constexpr int G1_OFF_A = 0;
constexpr int G1_OFF_B = 81920;
constexpr int G1_OFF_RS = G1_OFF_B + 57344;
constexpr int G1_OFF_ZS = G1_OFF_RS + 512;
constexpr int G1_LDS = G1_OFF_ZS + 2048;

__device__ __forceinline__ float gelu_tanh(float x) {
  float y = 0.7978845608028654f * (x + 0.044715f * x * x * x);
  float e = __expf(2.f * y);
  return x - x * __frcp_rn(1.f + e);
}

__global__ __launch_bounds__(512, 2) void g1_kernel(
    const bf16_t* __restrict__ x0, const bf16_t* __restrict__ Yb, const float* __restrict__ xst,
    const bf16_t* __restrict__ winT, const float* __restrict__ cs,
    bf16_t* __restrict__ x1out, bf16_t* __restrict__ z, float* __restrict__ zst TSP) {
  extern __shared__ __attribute__((aligned(16))) char smem[];
  const int tid = threadIdx.x;
  TS(0);
  const int xcd = blockIdx.x & 7, idx = blockIdx.x >> 3;
  const int mt = 8 * (xcd >> 1) + (idx & 7);
  const int nt = 4 * (xcd & 1) + (idx >> 3);
  const int fs = (27 * nt + 1) >> 1;
  const int nf = ((27 * (nt + 1) + 1) >> 1) - fs;
  const int m0 = 64 * mt;
  const int r = tid >> 3, c = tid & 7;
  u32x4 xa[10], ya[10];
#pragma unroll
  for (int kt = 0; kt < 10; ++kt) {
    xa[kt] = *(const u32x4*)(x0 + (size_t)(m0 + r) * D_ + 64 * kt + 8 * c);
    ya[kt] = *(const u32x4*)(Yb + (size_t)(m0 + r) * D_ + 64 * kt + 8 * c);
  }
  float s0 = 0.f, s1 = 0.f;
  {
    const f32x4* p = (const f32x4*)(xst + (size_t)(m0 + r) * 16);
#pragma unroll
    for (int i = 0; i < 4; ++i) { f32x4 v = p[i]; s0 += v[0] + v[2]; s1 += v[1] + v[3]; }
  }
  const int nrows = 16 * nf;
  int brow[4];
#pragma unroll
  for (int i = 0; i < 4; ++i) { int rr = r + 64 * i; brow[i] = rr < nrows ? rr : nrows - 1; }
  const bool b3 = tid < 256;
  constexpr int PD = 3;
  u32x4 wb[PD][4];
  auto loadB = [&](int slot, int kt) {
#pragma unroll
    for (int i = 0; i < 4; ++i)
      if (i < 3 || b3) wb[slot][i] = *(const u32x4*)(winT + (size_t)(16 * fs + brow[i]) * D_ + 64 * kt + 8 * c);
  };
  auto writeB = [&](int slot, int buf) {
#pragma unroll
    for (int i = 0; i < 4; ++i) {
      int rr = r + 64 * i;
      if (i < 3 || b3) *(u32x4*)(smem + G1_OFF_B + buf * 28672 + rr * 128 + ((c ^ (rr & 7)) << 4)) = wb[slot][i];
    }
  };
#pragma unroll
  for (int d = 0; d < PD; ++d) loadB(d, d);

  const f32x2 st = ln_stats(s0, s1, 1.0f / D_);
  TS(1);
  float t0 = 0.f, t1 = 0.f;
#pragma unroll
  for (int kt = 0; kt < 10; ++kt) {
    u32x4 o;
#pragma unroll
    for (int i = 0; i < 4; ++i) {
      float a = lo16(xa[kt][i]), b = hi16(xa[kt][i]);
      float ya_ = lo16(ya[kt][i]), yb_ = hi16(ya[kt][i]);
      float da = rbf(rbf(a - st[0]) * st[1]), db = rbf(rbf(b - st[0]) * st[1]);
      float ta = rbf(da * ya_), tb = rbf(db * yb_);
      uint32_t w = pk2(a + ta, b + tb);
      o[i] = w;
      float xa_ = lo16(w), xb_ = hi16(w);
      t0 += xa_ + xb_; t1 += xa_ * xa_ + xb_ * xb_;
    }
    *(u32x4*)(smem + G1_OFF_A + kt * 8192 + r * 128 + ((c ^ (r & 7)) << 4)) = o;
    if (nt == 0) *(u32x4*)(x1out + (size_t)(m0 + r) * D_ + 64 * kt + 8 * c) = o;
  }
  t0 += __shfl_xor(t0, 1); t0 += __shfl_xor(t0, 2); t0 += __shfl_xor(t0, 4);
  t1 += __shfl_xor(t1, 1); t1 += __shfl_xor(t1, 2); t1 += __shfl_xor(t1, 4);
  if (c == 0) {
    f32x2 s = ln_stats(t0, t1, 1.0f / D_);
    *(f32x2*)(smem + G1_OFF_RS + r * 8) = s;
  }
  TS(2);
  writeB(0, 0);
  loadB(0, PD);
  __syncthreads();
  TS(3);

  const int wid = tid >> 6, lane = tid & 63, fr = lane & 15, g = lane >> 4;
  const int wm = wid & 1, wn = wid >> 1;
  f32x4 acc[4][2] = {};
  int wrow[4];
#pragma unroll
  for (int i = 0; i < 4; ++i) { int fi = 4 * wn + i; wrow[i] = 16 * (fi < nf ? fi : nf - 1) + fr; }
#pragma unroll
  for (int kt = 0; kt < 10; ++kt) {
    const int buf = kt & 1;
    if (kt + 1 < 10) {
      writeB((kt + 1) % PD, buf ^ 1);
      if (kt + 1 + PD < 10) loadB((kt + 1) % PD, kt + 1 + PD);
    }
#pragma unroll
    for (int ks = 0; ks < 2; ++ks) {
      bf16x8 X[2], W[4];
#pragma unroll
      for (int i = 0; i < 2; ++i) {
        int rr = 32 * wm + 16 * i + fr;
        X[i] = *(const bf16x8*)(smem + G1_OFF_A + kt * 8192 + rr * 128 + (((4 * ks + g) ^ (rr & 7)) << 4));
      }
#pragma unroll
      for (int i = 0; i < 4; ++i)
        W[i] = *(const bf16x8*)(smem + G1_OFF_B + buf * 28672 + wrow[i] * 128 + (((4 * ks + g) ^ (wrow[i] & 7)) << 4));
#pragma unroll
      for (int i = 0; i < 4; ++i)
#pragma unroll
        for (int j = 0; j < 2; ++j) acc[i][j] = __builtin_amdgcn_mfma_f32_16x16x32_bf16(W[i], X[j], acc[i][j], 0, 0, 0);
    }
    __syncthreads();
  }
  TS(4);
  float zs0[2] = {0.f, 0.f}, zs1[2] = {0.f, 0.f};
#pragma unroll
  for (int i = 0; i < 4; ++i) {
    const int fi = 4 * wn + i;
    if (fi < nf) {
      const int f = 16 * (fs + fi) + 4 * g;
      f32x4 csv = *(const f32x4*)(cs + f);
#pragma unroll
      for (int j = 0; j < 2; ++j) {
        const int rr = 32 * wm + 16 * j + fr;
        f32x2 rs = *(const f32x2*)(smem + G1_OFF_RS + rr * 8);
        float o[4];
#pragma unroll
        for (int e = 0; e < 4; ++e) {
          float pre = rbf((acc[i][j][e] - rs[0] * csv[e]) * rs[1]);
          o[e] = gelu_tanh(pre);
        }
        u32x2 w = {pk2(o[0], o[1]), pk2(o[2], o[3])};
        *(u32x2*)(z + (size_t)(m0 + rr) * F_ + f) = w;
        if (nt >= 4) {
          float a = lo16(w[0]), b = hi16(w[0]), cc = lo16(w[1]), d = hi16(w[1]);
          zs0[j] += a + b + cc + d; zs1[j] += a * a + b * b + cc * cc + d * d;
        }
      }
    }
  }
  if (nt >= 4) {
#pragma unroll
    for (int j = 0; j < 2; ++j) {
      zs0[j] += __shfl_xor(zs0[j], 16); zs0[j] += __shfl_xor(zs0[j], 32);
      zs1[j] += __shfl_xor(zs1[j], 16); zs1[j] += __shfl_xor(zs1[j], 32);
      if (g == 0) {
        f32x2 o = {zs0[j], zs1[j]};
        *(f32x2*)(smem + G1_OFF_ZS + (wn * 64 + 32 * wm + 16 * j + fr) * 8) = o;
      }
    }
    __syncthreads();
    if (tid < 64) {
      float a = 0.f, b = 0.f;
#pragma unroll
      for (int w = 0; w < 4; ++w) { f32x2 v = *(const f32x2*)(smem + G1_OFF_ZS + (w * 64 + tid) * 8); a += v[0]; b += v[1]; }
      f32x2 o = {a, b};
      *(f32x2*)(zst + ((size_t)(m0 + tid) * 4 + (nt - 4)) * 2) = o;
    }
  }
  TS(5);
}

__global__ __launch_bounds__(256) void zn_kernel(const bf16_t* __restrict__ z, bf16_t* __restrict__ zn) {
  const int lane = threadIdx.x & 63;
  const int row = 4 * blockIdx.x + (threadIdx.x >> 6);
  const bf16_t* src = z + (size_t)row * F_ + FH_;
  u32x4 zz = {0u, 0u, 0u, 0u};
  u32x4 v0 = *(const u32x4*)(src + 8 * lane);
  u32x4 v1 = lane < 44 ? *(const u32x4*)(src + 8 * (64 + lane)) : zz;
  float s0 = 0.f, s1 = 0.f;
#pragma unroll
  for (int i = 0; i < 4; ++i) {
    float a = lo16(v0[i]), b = hi16(v0[i]), c = lo16(v1[i]), d = hi16(v1[i]);
    s0 += (a + b) + (c + d);
    s1 += (a * a + b * b) + (c * c + d * d);
  }
#pragma unroll
  for (int o = 1; o < 64; o <<= 1) { s0 += __shfl_xor(s0, o); s1 += __shfl_xor(s1, o); }
  const f32x2 st = ln_stats(s0, s1, 1.0f / FH_);
  u32x4 o0, o1;
#pragma unroll
  for (int i = 0; i < 4; ++i) {
    o0[i] = pk2(rbf(lo16(v0[i]) - st[0]) * st[1], rbf(hi16(v0[i]) - st[0]) * st[1]);
    o1[i] = lane < 44 ? pk2(rbf(lo16(v1[i]) - st[0]) * st[1], rbf(hi16(v1[i]) - st[0]) * st[1]) : 0u;
  }
  bf16_t* dst = zn + (size_t)row * FHP_;
  *(u32x4*)(dst + 8 * lane) = o0;
  if (lane < 48) *(u32x4*)(dst + 8 * (64 + lane)) = o1;
}

constexpr int G2_NS = 6;
constexpr int G2_STG = 24576;
constexpr int G2_LDS = G2_NS * G2_STG;

__device__ __forceinline__ void glds16(const void* gsrc, uint32_t lds_dst) {
  uint32_t keep;
  asm volatile("s_mov_b32 %0, m0\n\ts_mov_b32 m0, %2\n\ts_nop 0\n\tglobal_load_lds_dwordx4 %1, off\n\ts_mov_b32 m0, %0"
               : "=&s"(keep) : "v"(gsrc), "s"(lds_dst) : "memory");
}
__device__ __forceinline__ uint32_t lds_addr(const void* p) {
  return (uint32_t)(uintptr_t)(const __attribute__((address_space(3))) char*)p;
}
#define WAIT_VM(n) asm volatile("s_waitcnt vmcnt(" #n ")" ::: "memory")
#define WAIT_LGKM0() asm volatile("s_waitcnt lgkmcnt(0)" ::: "memory")
#define RAW_BAR() __builtin_amdgcn_s_barrier()

__global__ __launch_bounds__(512, 1) void g2_kernel(
    const bf16_t* __restrict__ wsp, const bf16_t* __restrict__ z, const bf16_t* __restrict__ zn,
    const bf16_t* __restrict__ bsp, bf16_t* __restrict__ u TSP) {
  extern __shared__ __attribute__((aligned(16))) char smem[];
  const int tid = threadIdx.x;
  TS(0);
  const int xcd = blockIdx.x & 7, idx = blockIdx.x >> 3;
  const int mt = 4 * (xcd >> 1) + (idx & 3);
  const int nt = 7 * (xcd & 1) + (idx >> 2);
  const int t0 = 128 * mt, f0 = 64 * nt;
  const int wid = __builtin_amdgcn_readfirstlane(tid >> 6), lane = tid & 63;
  const int rot = idx;
  const int lr = lane >> 3, lc = (lane & 7) ^ lr;
  const bf16_t* ap0 = wsp + (size_t)(t0 + 16 * wid + lr) * S_ + 8 * lc;
  const bf16_t* ap1 = ap0 + (size_t)8 * S_;
  const int brow = 8 * wid + lr;
  const int bsw = (((brow >> 1) & 1) | (((brow >> 3) & 1) << 1)) << 1;
  const bf16_t* bp = zn + (size_t)brow * FHP_ + f0 + 8 * ((lane & 7) ^ bsw);
  const uint32_t lbase = lds_addr(smem) + wid * 2048;
  const uint32_t lbaseB = lds_addr(smem) + 16384 + wid * 1024;
  auto issue = [&](int kt, int slot) {
    const int ke = (kt + rot) & 31;
    glds16(ap0 + 64 * ke, lbase + slot * G2_STG);
    glds16(ap1 + 64 * ke, lbase + slot * G2_STG + 1024);
    glds16(bp + (size_t)(64 * ke) * FHP_, lbaseB + slot * G2_STG);
  };
#pragma unroll
  for (int s = 0; s < G2_NS - 1; ++s) issue(s, s);
  TS(1);
  const int fr = lane & 15, g = lane >> 4;
  const int wm = wid & 3, wn = wid >> 2;
  f32x4 acc[2][2] = {};
  const int q4 = (lane & 15) >> 2, p4 = lane & 3;
  int aoff[2][2], boff[2][2][2];
#pragma unroll
  for (int ks = 0; ks < 2; ++ks) {
#pragma unroll
    for (int i = 0; i < 2; ++i) {
      int rr = 32 * wm + 16 * i + fr;
      aoff[ks][i] = rr * 128 + (((4 * ks + g) ^ (rr & 7)) << 4);
#pragma unroll
      for (int t = 0; t < 2; ++t) {
        int row = 32 * ks + 8 * g + 4 * t + q4;
        int ch = 2 * (2 * wn + i) + (p4 >> 1);
        int sw = (((row >> 1) & 1) | (((row >> 3) & 1) << 1)) << 1;
        boff[ks][i][t] = 16384 + row * 128 + ((ch ^ sw) << 4) + 8 * (p4 & 1);
      }
    }
  }
  auto readF = [&](bf16x8* T, bf16x8* Z, int ks, int slot) {
#pragma unroll
    for (int i = 0; i < 2; ++i) T[i] = *(const bf16x8*)(smem + slot * G2_STG + aoff[ks][i]);
#pragma unroll
    for (int i = 0; i < 2; ++i) {
      typedef __attribute__((address_space(3))) s16x4 lds_s16x4;
      s16x4 h0 = __builtin_amdgcn_ds_read_tr16_b64_v4i16((lds_s16x4*)(smem + slot * G2_STG + boff[ks][i][0]));
      s16x4 h1 = __builtin_amdgcn_ds_read_tr16_b64_v4i16((lds_s16x4*)(smem + slot * G2_STG + boff[ks][i][1]));
      Z[i] = __builtin_shufflevector(h0, h1, 0, 1, 2, 3, 4, 5, 6, 7);
    }
  };
  auto mm = [&](const bf16x8* T, const bf16x8* Z) {
#pragma unroll
    for (int i = 0; i < 2; ++i)
#pragma unroll
      for (int j = 0; j < 2; ++j) acc[i][j] = __builtin_amdgcn_mfma_f32_16x16x32_bf16(Z[i], T[j], acc[i][j], 0, 0, 0);
  };
  bf16x8 TA[2][2], ZA[2][2], TB[2][2], ZB[2][2];
  WAIT_VM(12);
  RAW_BAR();
  readF(TA[0], ZA[0], 0, 0);
  readF(TA[1], ZA[1], 1, 0);
  int slot = 0;
  auto step = [&](int kt, bf16x8 (*Tc)[2], bf16x8 (*Zc)[2], bf16x8 (*Tn)[2], bf16x8 (*Zn)[2]) {
    WAIT_VM(9);
    RAW_BAR();
    {
      int kn = kt + G2_NS - 1; kn = kn < 32 ? kn : 31;
      int sn = slot == 0 ? G2_NS - 1 : slot - 1;
      issue(kn, sn);
    }
    const int slot1 = slot + 1 == G2_NS ? 0 : slot + 1;
    if (wn == 0) {
      readF(Tn[0], Zn[0], 0, slot1);
      readF(Tn[1], Zn[1], 1, slot1);
      __builtin_amdgcn_sched_barrier(0);
      mm(Tc[0], Zc[0]);
      mm(Tc[1], Zc[1]);
    } else {
      mm(Tc[0], Zc[0]);
      mm(Tc[1], Zc[1]);
      __builtin_amdgcn_sched_barrier(0);
      readF(Tn[0], Zn[0], 0, slot1);
      readF(Tn[1], Zn[1], 1, slot1);
    }
    __builtin_amdgcn_sched_barrier(0);
    slot = slot1;
  };
#pragma unroll 1
  for (int kt = 0; kt < 32; kt += 2) {
    step(kt, TA, ZA, TB, ZB);
    step(kt + 1, TB, ZB, TA, ZA);
  }
  WAIT_VM(0);
  TS(3);
#pragma unroll
  for (int j = 0; j < 2; ++j) {
    const int t = t0 + 32 * wm + 16 * j + fr;
    const float bb = bf2f(bsp[t]);
#pragma unroll
    for (int i = 0; i < 2; ++i) {
      const int f = f0 + 32 * wn + 16 * i + 4 * g;
      u32x2 w = {0u, 0u};
      if (f < FH_) {
        u32x2 z1 = *(const u32x2*)(z + (size_t)t * F_ + f);
        float g0 = rbf(rbf(acc[i][j][0]) + bb), g1 = rbf(rbf(acc[i][j][1]) + bb);
        float g2 = rbf(rbf(acc[i][j][2]) + bb), g3 = rbf(rbf(acc[i][j][3]) + bb);
        w[0] = pk2(lo16(z1[0]) * g0, hi16(z1[0]) * g1);
        w[1] = pk2(lo16(z1[1]) * g2, hi16(z1[1]) * g3);
      }
      *(u32x2*)(u + (size_t)t * FHP_ + f) = w;
    }
  }
  TS(4);
}

constexpr int G3_OFF_A = 0;
constexpr int G3_OFF_B = 16384;
constexpr int G3_LDS = 36864;

__global__ __launch_bounds__(256, 1) void g3_kernel(
    const bf16_t* __restrict__ u, const bf16_t* __restrict__ woutT, const bf16_t* __restrict__ x1,
    bf16_t* __restrict__ xout, float* __restrict__ xst TSP) {
  extern __shared__ __attribute__((aligned(16))) char smem[];
  const int tid = threadIdx.x;
  TS(0);
  const int xcd = blockIdx.x & 7, idx = blockIdx.x >> 3;
  const int mt = 8 * (xcd >> 1) + (idx & 7);
  const int nt = 4 * (xcd & 1) + (idx >> 3);
  const int t0 = 64 * mt, d0 = 80 * nt;
  const int r = tid >> 3, c = tid & 7;
  constexpr int PD = 4;
  u32x4 ra[PD][2], rb[PD][3];
  const bool b2 = tid < 128;
  auto loadS = [&](int slot, int kt) {
    ra[slot][0] = *(const u32x4*)(u + (size_t)(t0 + r) * FHP_ + 64 * kt + 8 * c);
    ra[slot][1] = *(const u32x4*)(u + (size_t)(t0 + r + 32) * FHP_ + 64 * kt + 8 * c);
    rb[slot][0] = *(const u32x4*)(woutT + (size_t)(d0 + r) * FHP_ + 64 * kt + 8 * c);
    rb[slot][1] = *(const u32x4*)(woutT + (size_t)(d0 + r + 32) * FHP_ + 64 * kt + 8 * c);
    if (b2) rb[slot][2] = *(const u32x4*)(woutT + (size_t)(d0 + r + 64) * FHP_ + 64 * kt + 8 * c);
  };
  auto writeS = [&](int slot, int buf) {
    const int sw = (c ^ (r & 7)) << 4;
    *(u32x4*)(smem + G3_OFF_A + buf * 8192 + r * 128 + sw) = ra[slot][0];
    *(u32x4*)(smem + G3_OFF_A + buf * 8192 + (r + 32) * 128 + sw) = ra[slot][1];
    *(u32x4*)(smem + G3_OFF_B + buf * 10240 + r * 128 + sw) = rb[slot][0];
    *(u32x4*)(smem + G3_OFF_B + buf * 10240 + (r + 32) * 128 + sw) = rb[slot][1];
    if (b2) *(u32x4*)(smem + G3_OFF_B + buf * 10240 + (r + 64) * 128 + sw) = rb[slot][2];
  };
#pragma unroll
  for (int d = 0; d < PD; ++d) loadS(d, d);
  writeS(0, 0);
  loadS(0, PD);
  __syncthreads();
  TS(1);
  const int wid = tid >> 6, lane = tid & 63, fr = lane & 15, g = lane >> 4;
  f32x4 acc[5] = {};
#pragma unroll
  for (int kt = 0; kt < 14; ++kt) {
    const int buf = kt & 1;
    if (kt + 1 < 14) {
      writeS((kt + 1) % PD, buf ^ 1);
      if (kt + 1 + PD < 14) loadS((kt + 1) % PD, kt + 1 + PD);
    }
#pragma unroll
    for (int ks = 0; ks < 2; ++ks) {
      const int rr = 16 * wid + fr;
      bf16x8 T = *(const bf16x8*)(smem + G3_OFF_A + buf * 8192 + rr * 128 + (((4 * ks + g) ^ (rr & 7)) << 4));
#pragma unroll
      for (int i = 0; i < 5; ++i) {
        const int wr = 16 * i + fr;
        bf16x8 W = *(const bf16x8*)(smem + G3_OFF_B + buf * 10240 + wr * 128 + (((4 * ks + g) ^ (wr & 7)) << 4));
        acc[i] = __builtin_amdgcn_mfma_f32_16x16x32_bf16(W, T, acc[i], 0, 0, 0);
      }
    }
    __syncthreads();
  }
  TS(2);
  const int t = t0 + 16 * wid + fr;
  float s0 = 0.f, s1 = 0.f;
#pragma unroll
  for (int i = 0; i < 5; ++i) {
    const int d = d0 + 16 * i + 4 * g;
    u32x2 xr = *(const u32x2*)(x1 + (size_t)t * D_ + d);
    u32x2 w = {pk2(lo16(xr[0]) + rbf(acc[i][0]), hi16(xr[0]) + rbf(acc[i][1])),
               pk2(lo16(xr[1]) + rbf(acc[i][2]), hi16(xr[1]) + rbf(acc[i][3]))};
    *(u32x2*)(xout + (size_t)t * D_ + d) = w;
    float a = lo16(w[0]), b = hi16(w[0]), cc = lo16(w[1]), dd = hi16(w[1]);
    s0 += a + b + cc + dd; s1 += a * a + b * b + cc * cc + dd * dd;
  }
  s0 += __shfl_xor(s0, 16); s0 += __shfl_xor(s0, 32);
  s1 += __shfl_xor(s1, 16); s1 += __shfl_xor(s1, 32);
  if (g == 0) {
    f32x2 o = {s0, s1};
    *(f32x2*)(xst + ((size_t)t * 8 + nt) * 2) = o;
  }
  TS(3);
}

extern "C" void kernel_launch(void* const* d_in, const int* in_sizes, int n_in,
                              void* d_out, int out_size, void* d_ws, size_t ws_size,
                              hipStream_t stream) {
  const bf16_t* q = (const bf16_t*)d_in[0];
  const bf16_t* k = (const bf16_t*)d_in[1];
  const bf16_t* v = (const bf16_t*)d_in[2];
  const bf16_t* shift_k = (const bf16_t*)d_in[3];
  const bf16_t* ssm_k = (const bf16_t*)d_in[4];
  const bf16_t* ssm_D = (const bf16_t*)d_in[5];
  const bf16_t* w_in = (const bf16_t*)d_in[7];
  const bf16_t* w_sp = (const bf16_t*)d_in[8];
  const bf16_t* b_sp = (const bf16_t*)d_in[9];
  const bf16_t* w_out = (const bf16_t*)d_in[10];
  bf16_t* out = (bf16_t*)d_out;

  char* ws = (char*)d_ws;
  size_t off = 0;
  auto alloc = [&](size_t bytes) { char* p = ws + off; off += (bytes + 255) & ~(size_t)255; return p; };
  bf16_t* winT = (bf16_t*)alloc((size_t)F_ * D_ * 2);
  float* cs = (float*)alloc(F_ * 4);
  bf16_t* woutT = (bf16_t*)alloc((size_t)D_ * FHP_ * 2);
  bf16_t* Y = (bf16_t*)alloc((size_t)S_ * D_ * 2);
  bf16_t* kvT = (bf16_t*)alloc((size_t)S_ * D_ * 2);
  bf16_t* YT = (bf16_t*)alloc((size_t)S_ * D_ * 2);
  float* xst0 = (float*)alloc((size_t)S_ * 16 * 4);
  float* xst1 = (float*)alloc((size_t)S_ * 16 * 4);
  float* xst2 = (float*)alloc((size_t)S_ * 16 * 4);
  bf16_t* x1 = (bf16_t*)alloc((size_t)S_ * D_ * 2);
  bf16_t* x2 = (bf16_t*)alloc((size_t)S_ * D_ * 2);
  bf16_t* x3 = (bf16_t*)alloc((size_t)S_ * D_ * 2);
  bf16_t* z = (bf16_t*)alloc((size_t)S_ * F_ * 2 + 4096);
  float* zst = (float*)alloc((size_t)S_ * 8 * 4);
  bf16_t* u = (bf16_t*)alloc((size_t)S_ * FHP_ * 2);
  bf16_t* zn = (bf16_t*)alloc((size_t)S_ * FHP_ * 2);

  (void)hipFuncSetAttribute((const void*)kconv_kernel, hipFuncAttributeMaxDynamicSharedMemorySize, CV_LDS);
  (void)hipFuncSetAttribute((const void*)g1_kernel, hipFuncAttributeMaxDynamicSharedMemorySize, G1_LDS);
  (void)hipFuncSetAttribute((const void*)g2_kernel, hipFuncAttributeMaxDynamicSharedMemorySize, G2_LDS);
  (void)hipFuncSetAttribute((const void*)g3_kernel, hipFuncAttributeMaxDynamicSharedMemorySize, G3_LDS);

  kpre_kernel<<<320, 256, 0, stream>>>(k, v, shift_k, kvT);
  kconv_kernel<<<320 + 270 + 140 + 27 + 64, 256, CV_LDS, stream>>>(kvT, ssm_k, ssm_D, YT, w_in, w_out, q, winT, cs, woutT, xst0 TSA(0));
  kt_kernel<<<320, 256, 0, stream>>>(YT, Y);
  g1_kernel<<<256, 512, G1_LDS, stream>>>(q, Y, xst0, winT, cs, x1, z, zst TSA(1));
  zn_kernel<<<512, 256, 0, stream>>>(z, zn);
  g2_kernel<<<224, 512, G2_LDS, stream>>>(w_sp, z, zn, b_sp, u TSA(2));
  g3_kernel<<<256, 256, G3_LDS, stream>>>(u, woutT, x1, x2, xst1 TSA(3));
  g1_kernel<<<256, 512, G1_LDS, stream>>>(x2, Y, xst1, winT, cs, x3, z, zst TSA(4));
  zn_kernel<<<512, 256, 0, stream>>>(z, zn);
  g2_kernel<<<224, 512, G2_LDS, stream>>>(w_sp, z, zn, b_sp, u TSA(5));
  g3_kernel<<<256, 256, G3_LDS, stream>>>(u, woutT, x3, out, xst2 TSA(6));
}
```

```cpp
#include <hip/hip_runtime.h>
#include <hip/hip_bf16.h>
#include <stdint.h>

typedef __attribute__((ext_vector_type(8))) short bf16x8;
typedef __attribute__((ext_vector_type(4))) short s16x4;
typedef __attribute__((ext_vector_type(4))) float f32x4;
typedef __attribute__((ext_vector_type(16))) float f32x16;
typedef __attribute__((ext_vector_type(2))) float f32x2;
typedef __attribute__((ext_vector_type(2))) __bf16 bf16x2_t;
typedef __attribute__((ext_vector_type(4))) uint32_t u32x4;
typedef __attribute__((ext_vector_type(2))) uint32_t u32x2;
typedef uint16_t bf16_t;

#define TSP
#define TSA(k)
#define TS(i)

constexpr int S_ = 2048, D_ = 640, F_ = 1728, FH_ = 864, FHP_ = 896;

__device__ __forceinline__ uint32_t pk2(float lo, float hi) {
  f32x2 f = {lo, hi};
  bf16x2_t b = __builtin_convertvector(f, bf16x2_t);
  return __builtin_bit_cast(uint32_t, b);
}
__device__ __forceinline__ float lo16(uint32_t v) { return __uint_as_float(v << 16); }
__device__ __forceinline__ float hi16(uint32_t v) { return __uint_as_float(v & 0xffff0000u); }
__device__ __forceinline__ float rbf(float x) { return lo16(pk2(x, x)); }
__device__ __forceinline__ float bf2f(bf16_t v) { return __uint_as_float(((uint32_t)v) << 16); }

__device__ __forceinline__ f32x2 ln_stats(float sum, float sumsq, float inv_n) {
  float mean = sum * inv_n;
  float var = fmaxf(sumsq * inv_n - mean * mean, 0.f);
  float mb = rbf(mean);
  float vb = rbf(var);
  float sq = rbf(sqrtf(rbf(vb + 1e-5f)));
  f32x2 r = {mb, 1.0f / sq};
  return r;
}

constexpr int TP = 144;
__device__ __forceinline__ void tile_transpose_out(const char* T, bf16_t* __restrict__ dst, size_t dstride, int tid) {
  const int w = tid >> 6, lane = tid & 63, gq = lane >> 4, i = lane & 15;
  typedef __attribute__((address_space(3))) s16x4 lds_s16x4;
#pragma unroll
  for (int h = 0; h < 2; ++h) {
    const int R0 = 8 * gq + 32 * h;
    const char* p = T + (R0 + (i >> 2)) * TP + (16 * w + 4 * (i & 3)) * 2;
    s16x4 a = __builtin_amdgcn_ds_read_tr16_b64_v4i16((lds_s16x4*)p);
    s16x4 b = __builtin_amdgcn_ds_read_tr16_b64_v4i16((lds_s16x4*)(p + 4 * TP));
    bf16x8 o = __builtin_shufflevector(a, b, 0, 1, 2, 3, 4, 5, 6, 7);
    *(bf16x8*)(dst + (size_t)(16 * w + i) * dstride + R0) = o;
  }
}

__global__ __launch_bounds__(256) void kpre_kernel(
    const bf16_t* __restrict__ kin, const bf16_t* __restrict__ vin, const bf16_t* __restrict__ shift_k,
    bf16_t* __restrict__ kvT) {
  __shared__ __attribute__((aligned(16))) char T[64 * TP];
  const int tid = threadIdx.x;
  int j = blockIdx.x;
  const int r = tid >> 2, cq = tid & 3;
  {
    const int t0 = 64 * (j & 31), d0 = 64 * (j >> 5);
    const int t = t0 + r;
    u32x4 kr[2][4], vr[2], skr[2][4];
#pragma unroll
    for (int h = 0; h < 2; ++h) {
      const int d = d0 + 8 * (cq + 4 * h);
#pragma unroll
      for (int i = 0; i < 4; ++i) {
        u32x4 zz = {0u, 0u, 0u, 0u};
        kr[h][i] = (t - 3 + i >= 0) ? *(const u32x4*)(kin + (size_t)(t - 3 + i) * D_ + d) : zz;
      }
      vr[h] = *(const u32x4*)(vin + (size_t)t * D_ + d);
#pragma unroll
      for (int e2 = 0; e2 < 4; ++e2) skr[h][e2] = *(const u32x4*)(shift_k + (size_t)(d + 2 * e2) * 4);
    }
#pragma unroll
    for (int h = 0; h < 2; ++h) {
      u32x4 o;
#pragma unroll
      for (int e2 = 0; e2 < 4; ++e2) {
        const u32x4 sk = skr[h][e2];
        float res[2];
#pragma unroll
        for (int e = 0; e < 2; ++e) {
          float w0 = lo16(sk[2 * e]), w1 = hi16(sk[2 * e]), w2 = lo16(sk[2 * e + 1]), w3 = hi16(sk[2 * e + 1]);
          float k0 = e ? hi16(kr[h][0][e2]) : lo16(kr[h][0][e2]);
          float k1 = e ? hi16(kr[h][1][e2]) : lo16(kr[h][1][e2]);
          float k2 = e ? hi16(kr[h][2][e2]) : lo16(kr[h][2][e2]);
          float k3 = e ? hi16(kr[h][3][e2]) : lo16(kr[h][3][e2]);
          float ks = rbf(k0 * w3);
          ks = rbf(ks + rbf(k1 * w2));
          ks = rbf(ks + rbf(k2 * w1));
          ks = rbf(ks + rbf(k3 * w0));
          float vv = e ? hi16(vr[h][e2]) : lo16(vr[h][e2]);
          res[e] = ks * vv;
        }
        o[e2] = pk2(res[0], res[1]);
      }
      *(u32x4*)(T + r * TP + (cq + 4 * h) * 16) = o;
    }
    __syncthreads();
    tile_transpose_out(T, kvT + (size_t)d0 * S_ + t0, S_, tid);
  }
}

__device__ __forceinline__ void prep_job(int j, int tid, char* T, const bf16_t* __restrict__ w_in, const bf16_t* __restrict__ w_out,
                                         const bf16_t* __restrict__ q, bf16_t* __restrict__ winT, float* __restrict__ cs,
                                         bf16_t* __restrict__ woutT, float* __restrict__ xst) {
  const int r = tid >> 2, cq = tid & 3;
  if (j < 270) {
    const int d0 = 64 * (j % 10), f0 = 64 * (j / 10);
#pragma unroll
    for (int h = 0; h < 2; ++h)
      *(u32x4*)(T + r * TP + (cq + 4 * h) * 16) = *(const u32x4*)(w_in + (size_t)(d0 + r) * F_ + f0 + 8 * (cq + 4 * h));
    __syncthreads();
    tile_transpose_out(T, winT + (size_t)f0 * D_ + d0, D_, tid);
    return;
  }
  j -= 270;
  if (j < 140) {
    const int d0 = 64 * (j % 10), f0 = 64 * (j / 10);
#pragma unroll
    for (int h = 0; h < 2; ++h) {
      u32x4 zz = {0u, 0u, 0u, 0u};
      *(u32x4*)(T + r * TP + (cq + 4 * h) * 16) =
          (f0 + r < FH_) ? *(const u32x4*)(w_out + (size_t)(f0 + r) * D_ + d0 + 8 * (cq + 4 * h)) : zz;
    }
    __syncthreads();
    tile_transpose_out(T, woutT + (size_t)d0 * FHP_ + f0, FHP_, tid);
    return;
  }
  j -= 140;
  if (j < 27) {
    const int f0 = 64 * j, c8 = tid & 7, rr = tid >> 3;
    float s[8] = {0.f, 0.f, 0.f, 0.f, 0.f, 0.f, 0.f, 0.f};
    u32x4 w[20];
#pragma unroll
    for (int it = 0; it < 20; ++it) w[it] = *(const u32x4*)(w_in + (size_t)(32 * it + rr) * F_ + f0 + 8 * c8);
#pragma unroll
    for (int it = 0; it < 20; ++it) {
#pragma unroll
      for (int i = 0; i < 4; ++i) { s[2 * i] += lo16(w[it][i]); s[2 * i + 1] += hi16(w[it][i]); }
    }
    float* R = (float*)T;
#pragma unroll
    for (int i = 0; i < 8; ++i) R[rr * 64 + 8 * c8 + i] = s[i];
    __syncthreads();
    if (tid < 64) {
      float a = 0.f;
#pragma unroll
      for (int i = 0; i < 32; ++i) a += R[i * 64 + tid];
      cs[f0 + tid] = a;
    }
    return;
  }
  j -= 27;
  {
    const int r8 = tid >> 3, c = tid & 7;
    const int row = 32 * j + r8;
    float sum = 0.f, sq = 0.f;
#pragma unroll
    for (int it = 0; it < 10; ++it) {
      u32x4 w = *(const u32x4*)(q + (size_t)row * D_ + 64 * it + 8 * c);
#pragma unroll
      for (int i = 0; i < 4; ++i) {
        float a = lo16(w[i]), b = hi16(w[i]);
        sum += a + b; sq += a * a + b * b;
      }
    }
    sum += __shfl_xor(sum, 1); sum += __shfl_xor(sum, 2); sum += __shfl_xor(sum, 4);
    sq += __shfl_xor(sq, 1); sq += __shfl_xor(sq, 2); sq += __shfl_xor(sq, 4);
    f32x2 o = {c == 0 ? sum : 0.f, c == 0 ? sq : 0.f};
    *(f32x2*)(xst + ((size_t)row * 8 + c) * 2) = o;
  }
}

__global__ __launch_bounds__(256) void kt_kernel(const bf16_t* __restrict__ YT, bf16_t* __restrict__ Y) {
  __shared__ __attribute__((aligned(16))) char T[64 * TP];
  const int tid = threadIdx.x;
  const int j = blockIdx.x;
  const int r = tid >> 2, cq = tid & 3;
  const int t0 = 64 * (j & 31), d0 = 64 * (j >> 5);
#pragma unroll
  for (int h = 0; h < 2; ++h)
    *(u32x4*)(T + r * TP + (cq + 4 * h) * 16) = *(const u32x4*)(YT + (size_t)(d0 + r) * S_ + t0 + 8 * (cq + 4 * h));
  __syncthreads();
  tile_transpose_out(T, Y + (size_t)t0 * D_ + d0, D_, tid);
}

constexpr int CV_HP = 4416;
constexpr int CV_HCH = 4 * CV_HP;
constexpr int CV_UCH = 96 * 80;
constexpr int CV_OFF_H = 0;
constexpr int CV_OFF_U = CV_OFF_H + 2 * CV_HCH;
constexpr int CV_OFF_R = CV_OFF_U + 2 * CV_UCH;
constexpr int CV_OFF_Y = CV_OFF_R + 2 * 8192;
constexpr int CV_LDS = CV_OFF_Y + 2 * 4096;

__global__ __launch_bounds__(256, 2) void kconv_kernel(
    const bf16_t* __restrict__ kvT, const bf16_t* __restrict__ ssm_k, const bf16_t* __restrict__ ssm_D,
    bf16_t* __restrict__ YT, const bf16_t* __restrict__ w_in, const bf16_t* __restrict__ w_out, const bf16_t* __restrict__ q,
    bf16_t* __restrict__ winT, float* __restrict__ cs, bf16_t* __restrict__ woutT, float* __restrict__ xst TSP) {
  extern __shared__ __attribute__((aligned(16))) char smem[];
  const int tid = threadIdx.x;
  if (blockIdx.x >= 320) {
    prep_job(blockIdx.x - 320, tid, smem, w_in, w_out, q, winT, cs, woutT, xst);
    return;
  }
  TS(0);
  const int c0 = 2 * blockIdx.x;
  u32x4 ur[2];
#pragma unroll
  for (int i = 0; i < 2; ++i) ur[i] = *(const u32x4*)(kvT + (size_t)c0 * S_ + 8 * (tid + 256 * i));
  u32x2 hr[3][3];
#pragma unroll
  for (int rd = 0; rd < 3; ++rd) {
    int task = tid + 256 * rd;
    int ch = task / 262, c = task % 262 - 6;
#pragma unroll
    for (int i = 0; i < 3; ++i) {
      int idx = 8 * c + 4 * i;
      u32x2 z = {0u, 0u};
      hr[rd][i] = (task < 524 && idx >= 0 && idx < 2048) ? *(const u32x2*)(ssm_k + (size_t)(c0 + ch) * S_ + idx) : z;
    }
  }
  for (int i = tid; i < 2 * 160; i += 256) {
    int ch = i / 160, o = i % 160;
    u32x4 z = {0u, 0u, 0u, 0u};
    *(u32x4*)(smem + CV_OFF_U + ch * CV_UCH + o * 16) = z;
  }
  TS(1);
#pragma unroll
  for (int i = 0; i < 2; ++i) {
    const int e = 8 * (tid + 256 * i), ch = e >> 11, s = e & 2047;
    *(u32x4*)(smem + CV_OFF_U + ch * CV_UCH + ((s >> 5) + 32) * 80 + (s & 31) * 2) = ur[i];
  }
#pragma unroll
  for (int rd = 0; rd < 3; ++rd) {
    int task = tid + 256 * rd;
    if (task < 524) {
      int ch = task / 262, c = task % 262 - 6;
      uint32_t w[6] = {hr[rd][0][0], hr[rd][0][1], hr[rd][1][0], hr[rd][1][1], hr[rd][2][0], hr[rd][2][1]};
#pragma unroll
      for (int p = 0; p < 4; ++p) {
#pragma unroll
        for (int gi = 0; gi < 2; ++gi) {
          const int ao = ((1 - p) & 3) + 4 * gi;
          int a = 8 * c + ao;
          int qx = 2045 - p - a;
          auto el = [&](int e) -> uint32_t { return (e & 1) ? (w[e >> 1] >> 16) : (w[e >> 1] & 0xffff); };
          u32x2 o = {el(ao + 3) | (el(ao + 2) << 16), el(ao + 1) | (el(ao) << 16)};
          if (qx >= 0 && qx < 2088) *(u32x2*)(smem + CV_OFF_H + ch * CV_HCH + p * CV_HP + qx * 2) = o;
        }
      }
    }
  }
  TS(2);
  __syncthreads();
  TS(3);
  const int wid = tid >> 6, lane = tid & 63;
  const int ch = wid & 1, kb = wid >> 1;
  const int m = lane & 31, hh = lane >> 5;
  {
    const int p = (4 - (m & 3)) & 3;
    const char* abase = smem + CV_OFF_H + ch * CV_HCH + p * CV_HP + (2048 - m + 8 * hh - p + 16 * kb) * 2;
    const char* bbase = smem + CV_OFF_U + ch * CV_UCH + (m + 32) * 80 + 32 * kb + 16 * hh;
    f32x16 acc0 = {}, acc1 = {};
    u32x2 ra[3][4];
    bf16x8 rbB[3][2];
    auto ld = [&](int sl, int dl) {
      typedef const volatile __attribute__((address_space(3))) u32x2* lds_v2;
      ra[sl][0] = *(lds_v2)(abase - 64 * dl);
      ra[sl][1] = *(lds_v2)(abase - 64 * dl + 8);
      ra[sl][2] = *(lds_v2)(abase - 64 * (dl + 32));
      ra[sl][3] = *(lds_v2)(abase - 64 * (dl + 32) + 8);
      rbB[sl][0] = *(const bf16x8*)(bbase - 80 * dl);
      rbB[sl][1] = *(const bf16x8*)(bbase + 80 * (32 - dl));
    };
    ld(0, 0);
    ld(1, 1);
#pragma unroll
    for (int dl = 0; dl < 32; ++dl) {
      if (dl + 2 < 32) ld((dl + 2) % 3, dl + 2);
      __builtin_amdgcn_sched_barrier(0);
      const int sl = dl % 3;
      u32x4 A0u = {ra[sl][0][0], ra[sl][0][1], ra[sl][1][0], ra[sl][1][1]};
      u32x4 A1u = {ra[sl][2][0], ra[sl][2][1], ra[sl][3][0], ra[sl][3][1]};
      bf16x8 A0 = __builtin_bit_cast(bf16x8, A0u), A1 = __builtin_bit_cast(bf16x8, A1u);
      acc0 = __builtin_amdgcn_mfma_f32_32x32x16_bf16(A0, rbB[sl][0], acc0, 0, 0, 0);
      acc1 = __builtin_amdgcn_mfma_f32_32x32x16_bf16(A0, rbB[sl][1], acc1, 0, 0, 0);
      acc1 = __builtin_amdgcn_mfma_f32_32x32x16_bf16(A1, rbB[sl][0], acc1, 0, 0, 0);
      __builtin_amdgcn_sched_barrier(0);
    }
    TS(4);
    float* red = (float*)(smem + CV_OFF_R + ch * 8192);
    if (kb == 1) {
#pragma unroll
      for (int r = 0; r < 16; ++r) { red[r * 64 + lane] = acc0[r]; red[(16 + r) * 64 + lane] = acc1[r]; }
    }
    __syncthreads();
    if (kb == 0) {
      const float Dv = bf2f(ssm_D[c0 + ch]);
      bf16_t* yb = (bf16_t*)(smem + CV_OFF_Y + ch * 4096);
#pragma unroll
      for (int half = 0; half < 2; ++half) {
        const int a = m + 32 * half;
#pragma unroll
        for (int rq = 0; rq < 4; ++rq) {
          const int b0 = 8 * rq + 4 * hh;
          u32x2 kvw = *(const u32x2*)(smem + CV_OFF_U + ch * CV_UCH + (a + 32) * 80 + b0 * 2);
          float kvv[4] = {lo16(kvw[0]), hi16(kvw[0]), lo16(kvw[1]), hi16(kvw[1])};
          float o[4];
#pragma unroll
          for (int i = 0; i < 4; ++i) {
            const int r = 4 * rq + i;
            float y = (half ? acc1[r] : acc0[r]) + red[(16 * half + r) * 64 + lane];
            o[i] = rbf(y) + rbf(kvv[i] * Dv);
          }
          u32x2 ow = {pk2(o[0], o[1]), pk2(o[2], o[3])};
          *(u32x2*)(yb + 32 * a + b0) = ow;
        }
      }
    }
    __syncthreads();
    TS(5);
#pragma unroll
    for (int i = 0; i < 2; ++i) {
      const int e = tid + 256 * i;
      *(u32x4*)(YT + (size_t)c0 * S_ + 8 * e) = *(const u32x4*)(smem + CV_OFF_Y + 16 * e);
    }
    TS(6);
  }
}

template <int I> struct IC { static constexpr int value = I; };
template <int N, int I = 0, typename F>
__device__ __forceinline__ void static_for(F&& f) {
  if constexpr (I < N) { f(IC<I>{}); static_for<N, I + 1>(f); }
}

__device__ __forceinline__ float dpp_add(float v, int ctrl_sel) {
  int x = __float_as_int(v), y;
  switch (ctrl_sel) {
    case 0: y = __builtin_amdgcn_update_dpp(0, x, 0xB1, 0xf, 0xf, false); break;
    case 1: y = __builtin_amdgcn_update_dpp(0, x, 0x4E, 0xf, 0xf, false); break;
    case 2: y = __builtin_amdgcn_update_dpp(0, x, 0x141, 0xf, 0xf, false); break;
    default: y = __builtin_amdgcn_update_dpp(0, x, 0x140, 0xf, 0xf, false); break;
  }
  return v + __int_as_float(y);
}
__device__ __forceinline__ float wave_sum(float v) {
  v = dpp_add(v, 0); v = dpp_add(v, 1); v = dpp_add(v, 2); v = dpp_add(v, 3);
  int x = __float_as_int(v);
  return (__int_as_float(__builtin_amdgcn_readlane(x, 0)) + __int_as_float(__builtin_amdgcn_readlane(x, 16))) +
         (__int_as_float(__builtin_amdgcn_readlane(x, 32)) + __int_as_float(__builtin_amdgcn_readlane(x, 48)));
}

__global__ __launch_bounds__(256) void xln_kernel(const bf16_t* __restrict__ x0, const bf16_t* __restrict__ Yb,
                                                  bf16_t* __restrict__ x1, bf16_t* __restrict__ a1) {
  const int lane = threadIdx.x & 63;
  const int row = 4 * blockIdx.x + (threadIdx.x >> 6);
  const uint32_t* px = (const uint32_t*)(x0 + (size_t)row * D_);
  const uint32_t* py = (const uint32_t*)(Yb + (size_t)row * D_);
  uint32_t xv[5], yv[5];
#pragma unroll
  for (int i = 0; i < 5; ++i) { xv[i] = px[lane + 64 * i]; yv[i] = py[lane + 64 * i]; }
  float s0 = 0.f, s1 = 0.f;
#pragma unroll
  for (int i = 0; i < 5; ++i) { float a = lo16(xv[i]), b = hi16(xv[i]); s0 += a + b; s1 += a * a + b * b; }
  const f32x2 st = ln_stats(wave_sum(s0), wave_sum(s1), 1.0f / D_);
  float t0 = 0.f, t1 = 0.f;
  uint32_t* p1 = (uint32_t*)(x1 + (size_t)row * D_);
#pragma unroll
  for (int i = 0; i < 5; ++i) {
    float a = lo16(xv[i]), b = hi16(xv[i]);
    float da = rbf(rbf(a - st[0]) * st[1]), db = rbf(rbf(b - st[0]) * st[1]);
    float ta = rbf(da * lo16(yv[i])), tb = rbf(db * hi16(yv[i]));
    uint32_t w = pk2(a + ta, b + tb);
    xv[i] = w;
    p1[lane + 64 * i] = w;
    float xa = lo16(w), xb = hi16(w);
    t0 += xa + xb; t1 += xa * xa + xb * xb;
  }
  const f32x2 su = ln_stats(wave_sum(t0), wave_sum(t1), 1.0f / D_);
  uint32_t* pa = (uint32_t*)(a1 + (size_t)row * D_);
#pragma unroll
  for (int i = 0; i < 5; ++i)
    pa[lane + 64 * i] = pk2(rbf(lo16(xv[i]) - su[0]) * su[1], rbf(hi16(xv[i]) - su[0]) * su[1]);
}

__device__ __forceinline__ void glds16(const void* gsrc, uint32_t lds_dst) {
  uint32_t keep;
  asm volatile("s_mov_b32 %0, m0\n\ts_mov_b32 m0, %2\n\ts_nop 0\n\tglobal_load_lds_dwordx4 %1, off\n\ts_mov_b32 m0, %0"
               : "=&s"(keep) : "v"(gsrc), "s"(lds_dst) : "memory");
}
__device__ __forceinline__ uint32_t lds_addr(const void* p) {
  return (uint32_t)(uintptr_t)(const __attribute__((address_space(3))) char*)p;
}
__device__ __forceinline__ void gload16(bf16x8& dst, const void* p) {
  asm volatile("global_load_dwordx4 %0, %1, off" : "=v"(dst) : "v"(p) : "memory");
}
template <int N>
__device__ __forceinline__ void wait_vm2(bf16x8& a, bf16x8& b) {
  asm volatile("s_waitcnt vmcnt(%2)" : "+v"(a), "+v"(b) : "n"(N) : "memory");
}
#define WAIT_VM(n) asm volatile("s_waitcnt vmcnt(" #n ")" ::: "memory")
#define WAIT_LGKM0() asm volatile("s_waitcnt lgkmcnt(0)" ::: "memory")
#define RAW_BAR() __builtin_amdgcn_s_barrier()

constexpr int G1_LDS = 81920;
constexpr int G1_OP = 464;

__device__ __forceinline__ float gelu_tanh(float x) {
  float y = 0.7978845608028654f * (x + 0.044715f * x * x * x);
  float e = __expf(2.f * y);
  return x - x * __frcp_rn(1.f + e);
}

__global__ __launch_bounds__(512, 1) void g1_kernel(
    const bf16_t* __restrict__ a1, const bf16_t* __restrict__ winT, bf16_t* __restrict__ z TSP) {
  extern __shared__ __attribute__((aligned(16))) char smem[];
  const int tid = threadIdx.x;
  TS(0);
  const int xcd = blockIdx.x & 7, idx = blockIdx.x >> 3;
  const int mt = 8 * (xcd >> 1) + (idx & 7);
  const int nt = 4 * (xcd & 1) + (idx >> 3);
  const int fs = (27 * nt + 1) >> 1;
  const int nf = ((27 * (nt + 1) + 1) >> 1) - fs;
  const int m0 = 64 * mt;
  const int wid = __builtin_amdgcn_readfirstlane(tid >> 6), lane = tid & 63;
  {
    const int lr = lane >> 3, lc = (lane & 7) ^ lr;
    const bf16_t* ap = a1 + (size_t)(m0 + 8 * wid + lr) * D_ + 8 * lc;
    const uint32_t lbase = lds_addr(smem) + wid * 1024;
#pragma unroll
    for (int kt = 0; kt < 10; ++kt) glds16(ap + 64 * kt, lbase + kt * 8192);
  }
  const int fr = lane & 15, g = lane >> 4;
  constexpr int P = 10;
  const bf16_t* wp[2];
#pragma unroll
  for (int i = 0; i < 2; ++i) {
    int fi = 2 * wid + i; fi = fi < nf ? fi : nf - 1;
    wp[i] = winT + (size_t)(16 * (fs + fi) + fr) * D_ + 8 * g;
  }
  bf16x8 wv[P][2];
#pragma unroll
  for (int s = 0; s < P; ++s)
#pragma unroll
    for (int i = 0; i < 2; ++i) gload16(wv[s][i], wp[i] + 32 * s);
  TS(1);
  WAIT_VM(20);
  RAW_BAR();
  TS(2);
  f32x4 acc[2][4] = {};
  if (wid < 7) {
    int xoff[4];
#pragma unroll
    for (int j = 0; j < 4; ++j) xoff[j] = (16 * j + fr) * 128;
    auto step = [&](auto KS) {
      constexpr int ks = decltype(KS)::value;
      bf16x8 X[4];
#pragma unroll
      for (int j = 0; j < 4; ++j)
        X[j] = *(const bf16x8*)(smem + (ks >> 1) * 8192 + xoff[j] + (((4 * (ks & 1) + g) ^ (fr & 7)) << 4));
      constexpr int newest = (ks + P - 1 < 19) ? ks + P - 1 : 19;
      wait_vm2<2 * (newest - ks)>(wv[ks % P][0], wv[ks % P][1]);
#pragma unroll
      for (int j = 0; j < 4; ++j) {
        acc[0][j] = __builtin_amdgcn_mfma_f32_16x16x32_bf16(wv[ks % P][0], X[j], acc[0][j], 0, 0, 0);
        acc[1][j] = __builtin_amdgcn_mfma_f32_16x16x32_bf16(wv[ks % P][1], X[j], acc[1][j], 0, 0, 0);
      }
      if constexpr (ks + P < 20) {
        gload16(wv[ks % P][0], wp[0] + 32 * (ks + P));
        gload16(wv[ks % P][1], wp[1] + 32 * (ks + P));
      }
    };
    static_for<20>(step);
  } else {
    WAIT_VM(0);
  }
  TS(3);
  __syncthreads();
  if (wid < 7) {
#pragma unroll
    for (int i = 0; i < 2; ++i)
#pragma unroll
      for (int j = 0; j < 4; ++j) {
        u32x2 w = {pk2(gelu_tanh(rbf(acc[i][j][0])), gelu_tanh(rbf(acc[i][j][1]))),
                   pk2(gelu_tanh(rbf(acc[i][j][2])), gelu_tanh(rbf(acc[i][j][3])))};
        *(u32x2*)(smem + (16 * j + fr) * G1_OP + (32 * wid + 16 * i + 4 * g) * 2) = w;
      }
  }
  __syncthreads();
  {
    const int r = tid >> 3, c0 = tid & 7;
    bf16_t* zr = z + (size_t)(m0 + r) * F_ + 16 * fs;
#pragma unroll
    for (int i = 0; i < 4; ++i) {
      const int c = c0 + 8 * i;
      if (c < 2 * nf) *(u32x4*)(zr + 8 * c) = *(const u32x4*)(smem + r * G1_OP + 16 * c);
    }
  }
  TS(4);
}

__global__ __launch_bounds__(256) void zn_kernel(const bf16_t* __restrict__ z, bf16_t* __restrict__ zn) {
  const int lane = threadIdx.x & 63;
  const int row = 4 * blockIdx.x + (threadIdx.x >> 6);
  const bf16_t* src = z + (size_t)row * F_ + FH_;
  u32x4 zz = {0u, 0u, 0u, 0u};
  u32x4 v0 = *(const u32x4*)(src + 8 * lane);
  u32x4 v1 = lane < 44 ? *(const u32x4*)(src + 8 * (64 + lane)) : zz;
  float s0 = 0.f, s1 = 0.f;
#pragma unroll
  for (int i = 0; i < 4; ++i) {
    float a = lo16(v0[i]), b = hi16(v0[i]), c = lo16(v1[i]), d = hi16(v1[i]);
    s0 += (a + b) + (c + d);
    s1 += (a * a + b * b) + (c * c + d * d);
  }
#pragma unroll
  for (int o = 1; o < 64; o <<= 1) { s0 += __shfl_xor(s0, o); s1 += __shfl_xor(s1, o); }
  const f32x2 st = ln_stats(s0, s1, 1.0f / FH_);
  u32x4 o0, o1;
#pragma unroll
  for (int i = 0; i < 4; ++i) {
    o0[i] = pk2(rbf(lo16(v0[i]) - st[0]) * st[1], rbf(hi16(v0[i]) - st[0]) * st[1]);
    o1[i] = lane < 44 ? pk2(rbf(lo16(v1[i]) - st[0]) * st[1], rbf(hi16(v1[i]) - st[0]) * st[1]) : 0u;
  }
  bf16_t* dst = zn + (size_t)row * FHP_;
  *(u32x4*)(dst + 8 * lane) = o0;
  if (lane < 48) *(u32x4*)(dst + 8 * (64 + lane)) = o1;
}

constexpr int G2_NS = 6;
constexpr int G2_STG = 24576;
constexpr int G2_LDS = G2_NS * G2_STG;

__global__ __launch_bounds__(512, 1) void g2_kernel(
    const bf16_t* __restrict__ wsp, const bf16_t* __restrict__ z, const bf16_t* __restrict__ zn,
    const bf16_t* __restrict__ bsp, bf16_t* __restrict__ u TSP) {
  extern __shared__ __attribute__((aligned(16))) char smem[];
  const int tid = threadIdx.x;
  TS(0);
  const int xcd = blockIdx.x & 7, idx = blockIdx.x >> 3;
  const int mt = 4 * (xcd >> 1) + (idx & 3);
  const int nt = 7 * (xcd & 1) + (idx >> 2);
  const int t0 = 128 * mt, f0 = 64 * nt;
  const int wid = __builtin_amdgcn_readfirstlane(tid >> 6), lane = tid & 63;
  const int rot = idx;
  const int lr = lane >> 3, lc = (lane & 7) ^ lr;
  const bf16_t* ap0 = wsp + (size_t)(t0 + 16 * wid + lr) * S_ + 8 * lc;
  const bf16_t* ap1 = ap0 + (size_t)8 * S_;
  const int brow = 8 * wid + lr;
  const int bsw = (((brow >> 1) & 1) | (((brow >> 3) & 1) << 1)) << 1;
  const bf16_t* bp = zn + (size_t)brow * FHP_ + f0 + 8 * ((lane & 7) ^ bsw);
  const uint32_t lbase = lds_addr(smem) + wid * 2048;
  const uint32_t lbaseB = lds_addr(smem) + 16384 + wid * 1024;
  auto issue = [&](int kt, int slot) {
    const int ke = (kt + rot) & 31;
    glds16(ap0 + 64 * ke, lbase + slot * G2_STG);
    glds16(ap1 + 64 * ke, lbase + slot * G2_STG + 1024);
    glds16(bp + (size_t)(64 * ke) * FHP_, lbaseB + slot * G2_STG);
  };
#pragma unroll
  for (int s = 0; s < G2_NS - 1; ++s) issue(s, s);
  TS(1);
  const int fr = lane & 15, g = lane >> 4;
  const int wm = wid & 3, wn = wid >> 2;
  f32x4 acc[2][2] = {};
  const int q4 = (lane & 15) >> 2, p4 = lane & 3;
  int aoff[2][2], boff[2][2][2];
#pragma unroll
  for (int ks = 0; ks < 2; ++ks) {
#pragma unroll
    for (int i = 0; i < 2; ++i) {
      int rr = 32 * wm + 16 * i + fr;
      aoff[ks][i] = rr * 128 + (((4 * ks + g) ^ (rr & 7)) << 4);
#pragma unroll
      for (int t = 0; t < 2; ++t) {
        int row = 32 * ks + 8 * g + 4 * t + q4;
        int ch = 2 * (2 * wn + i) + (p4 >> 1);
        int sw = (((row >> 1) & 1) | (((row >> 3) & 1) << 1)) << 1;
        boff[ks][i][t] = 16384 + row * 128 + ((ch ^ sw) << 4) + 8 * (p4 & 1);
      }
    }
  }
  auto readF = [&](bf16x8* T, bf16x8* Z, int ks, int slot) {
#pragma unroll
    for (int i = 0; i < 2; ++i) T[i] = *(const bf16x8*)(smem + slot * G2_STG + aoff[ks][i]);
#pragma unroll
    for (int i = 0; i < 2; ++i) {
      typedef __attribute__((address_space(3))) s16x4 lds_s16x4;
      s16x4 h0 = __builtin_amdgcn_ds_read_tr16_b64_v4i16((lds_s16x4*)(smem + slot * G2_STG + boff[ks][i][0]));
      s16x4 h1 = __builtin_amdgcn_ds_read_tr16_b64_v4i16((lds_s16x4*)(smem + slot * G2_STG + boff[ks][i][1]));
      Z[i] = __builtin_shufflevector(h0, h1, 0, 1, 2, 3, 4, 5, 6, 7);
    }
  };
  auto mm = [&](const bf16x8* T, const bf16x8* Z) {
#pragma unroll
    for (int i = 0; i < 2; ++i)
#pragma unroll
      for (int j = 0; j < 2; ++j) acc[i][j] = __builtin_amdgcn_mfma_f32_16x16x32_bf16(Z[i], T[j], acc[i][j], 0, 0, 0);
  };
  bf16x8 TA[2][2], ZA[2][2], TB[2][2], ZB[2][2];
  WAIT_VM(12);
  RAW_BAR();
  readF(TA[0], ZA[0], 0, 0);
  readF(TA[1], ZA[1], 1, 0);
  int slot = 0;
  auto step = [&](int kt, bf16x8 (*Tc)[2], bf16x8 (*Zc)[2], bf16x8 (*Tn)[2], bf16x8 (*Zn)[2]) {
    WAIT_VM(9);
    RAW_BAR();
    {
      int kn = kt + G2_NS - 1; kn = kn < 32 ? kn : 31;
      int sn = slot == 0 ? G2_NS - 1 : slot - 1;
      issue(kn, sn);
    }
    const int slot1 = slot + 1 == G2_NS ? 0 : slot + 1;
    if (wn == 0) {
      readF(Tn[0], Zn[0], 0, slot1);
      readF(Tn[1], Zn[1], 1, slot1);
      __builtin_amdgcn_sched_barrier(0);
      mm(Tc[0], Zc[0]);
      mm(Tc[1], Zc[1]);
    } else {
      mm(Tc[0], Zc[0]);
      mm(Tc[1], Zc[1]);
      __builtin_amdgcn_sched_barrier(0);
      readF(Tn[0], Zn[0], 0, slot1);
      readF(Tn[1], Zn[1], 1, slot1);
    }
    __builtin_amdgcn_sched_barrier(0);
    slot = slot1;
  };
#pragma unroll 1
  for (int kt = 0; kt < 32; kt += 2) {
    step(kt, TA, ZA, TB, ZB);
    step(kt + 1, TB, ZB, TA, ZA);
  }
  WAIT_VM(0);
  TS(3);
#pragma unroll
  for (int j = 0; j < 2; ++j) {
    const int t = t0 + 32 * wm + 16 * j + fr;
    const float bb = bf2f(bsp[t]);
#pragma unroll
    for (int i = 0; i < 2; ++i) {
      const int f = f0 + 32 * wn + 16 * i + 4 * g;
      u32x2 w = {0u, 0u};
      if (f < FH_) {
        u32x2 z1 = *(const u32x2*)(z + (size_t)t * F_ + f);
        float g0 = rbf(rbf(acc[i][j][0]) + bb), g1 = rbf(rbf(acc[i][j][1]) + bb);
        float g2 = rbf(rbf(acc[i][j][2]) + bb), g3 = rbf(rbf(acc[i][j][3]) + bb);
        w[0] = pk2(lo16(z1[0]) * g0, hi16(z1[0]) * g1);
        w[1] = pk2(lo16(z1[1]) * g2, hi16(z1[1]) * g3);
      }
      *(u32x2*)(u + (size_t)t * FHP_ + f) = w;
    }
  }
  TS(4);
}

constexpr int G3_OFF_A = 0;
constexpr int G3_OFF_B = 16384;
constexpr int G3_LDS = 36864;

__global__ __launch_bounds__(256, 1) void g3_kernel(
    const bf16_t* __restrict__ u, const bf16_t* __restrict__ woutT, const bf16_t* __restrict__ x1,
    bf16_t* __restrict__ xout, float* __restrict__ xst TSP) {
  extern __shared__ __attribute__((aligned(16))) char smem[];
  const int tid = threadIdx.x;
  TS(0);
  const int xcd = blockIdx.x & 7, idx = blockIdx.x >> 3;
  const int mt = 8 * (xcd >> 1) + (idx & 7);
  const int nt = 4 * (xcd & 1) + (idx >> 3);
  const int t0 = 64 * mt, d0 = 80 * nt;
  const int r = tid >> 3, c = tid & 7;
  constexpr int PD = 4;
  u32x4 ra[PD][2], rb[PD][3];
  const bool b2 = tid < 128;
  auto loadS = [&](int slot, int kt) {
    ra[slot][0] = *(const u32x4*)(u + (size_t)(t0 + r) * FHP_ + 64 * kt + 8 * c);
    ra[slot][1] = *(const u32x4*)(u + (size_t)(t0 + r + 32) * FHP_ + 64 * kt + 8 * c);
    rb[slot][0] = *(const u32x4*)(woutT + (size_t)(d0 + r) * FHP_ + 64 * kt + 8 * c);
    rb[slot][1] = *(const u32x4*)(woutT + (size_t)(d0 + r + 32) * FHP_ + 64 * kt + 8 * c);
    if (b2) rb[slot][2] = *(const u32x4*)(woutT + (size_t)(d0 + r + 64) * FHP_ + 64 * kt + 8 * c);
  };
  auto writeS = [&](int slot, int buf) {
    const int sw = (c ^ (r & 7)) << 4;
    *(u32x4*)(smem + G3_OFF_A + buf * 8192 + r * 128 + sw) = ra[slot][0];
    *(u32x4*)(smem + G3_OFF_A + buf * 8192 + (r + 32) * 128 + sw) = ra[slot][1];
    *(u32x4*)(smem + G3_OFF_B + buf * 10240 + r * 128 + sw) = rb[slot][0];
    *(u32x4*)(smem + G3_OFF_B + buf * 10240 + (r + 32) * 128 + sw) = rb[slot][1];
    if (b2) *(u32x4*)(smem + G3_OFF_B + buf * 10240 + (r + 64) * 128 + sw) = rb[slot][2];
  };
#pragma unroll
  for (int d = 0; d < PD; ++d) loadS(d, d);
  writeS(0, 0);
  loadS(0, PD);
  __syncthreads();
  TS(1);
  const int wid = tid >> 6, lane = tid & 63, fr = lane & 15, g = lane >> 4;
  f32x4 acc[5] = {};
#pragma unroll
  for (int kt = 0; kt < 14; ++kt) {
    const int buf = kt & 1;
    if (kt + 1 < 14) {
      writeS((kt + 1) % PD, buf ^ 1);
      if (kt + 1 + PD < 14) loadS((kt + 1) % PD, kt + 1 + PD);
    }
#pragma unroll
    for (int ks = 0; ks < 2; ++ks) {
      const int rr = 16 * wid + fr;
      bf16x8 T = *(const bf16x8*)(smem + G3_OFF_A + buf * 8192 + rr * 128 + (((4 * ks + g) ^ (rr & 7)) << 4));
#pragma unroll
      for (int i = 0; i < 5; ++i) {
        const int wr = 16 * i + fr;
        bf16x8 W = *(const bf16x8*)(smem + G3_OFF_B + buf * 10240 + wr * 128 + (((4 * ks + g) ^ (wr & 7)) << 4));
        acc[i] = __builtin_amdgcn_mfma_f32_16x16x32_bf16(W, T, acc[i], 0, 0, 0);
      }
    }
    __syncthreads();
  }
  TS(2);
  const int t = t0 + 16 * wid + fr;
  float s0 = 0.f, s1 = 0.f;
#pragma unroll
  for (int i = 0; i < 5; ++i) {
    const int d = d0 + 16 * i + 4 * g;
    u32x2 xr = *(const u32x2*)(x1 + (size_t)t * D_ + d);
    u32x2 w = {pk2(lo16(xr[0]) + rbf(acc[i][0]), hi16(xr[0]) + rbf(acc[i][1])),
               pk2(lo16(xr[1]) + rbf(acc[i][2]), hi16(xr[1]) + rbf(acc[i][3]))};
    *(u32x2*)(xout + (size_t)t * D_ + d) = w;
    float a = lo16(w[0]), b = hi16(w[0]), cc = lo16(w[1]), dd = hi16(w[1]);
    s0 += a + b + cc + dd; s1 += a * a + b * b + cc * cc + dd * dd;
  }
  s0 += __shfl_xor(s0, 16); s0 += __shfl_xor(s0, 32);
  s1 += __shfl_xor(s1, 16); s1 += __shfl_xor(s1, 32);
  if (g == 0) {
    f32x2 o = {s0, s1};
    *(f32x2*)(xst + ((size_t)t * 8 + nt) * 2) = o;
  }
  TS(3);
}

extern "C" void kernel_launch(void* const* d_in, const int* in_sizes, int n_in,
                              void* d_out, int out_size, void* d_ws, size_t ws_size,
                              hipStream_t stream) {
  const bf16_t* q = (const bf16_t*)d_in[0];
  const bf16_t* k = (const bf16_t*)d_in[1];
  const bf16_t* v = (const bf16_t*)d_in[2];
  const bf16_t* shift_k = (const bf16_t*)d_in[3];
  const bf16_t* ssm_k = (const bf16_t*)d_in[4];
  const bf16_t* ssm_D = (const bf16_t*)d_in[5];
  const bf16_t* w_in = (const bf16_t*)d_in[7];
  const bf16_t* w_sp = (const bf16_t*)d_in[8];
  const bf16_t* b_sp = (const bf16_t*)d_in[9];
  const bf16_t* w_out = (const bf16_t*)d_in[10];
  bf16_t* out = (bf16_t*)d_out;

  char* ws = (char*)d_ws;
  size_t off = 0;
  auto alloc = [&](size_t bytes) { char* p = ws + off; off += (bytes + 255) & ~(size_t)255; return p; };
  bf16_t* winT = (bf16_t*)alloc((size_t)F_ * D_ * 2);
  float* cs = (float*)alloc(F_ * 4);
  bf16_t* woutT = (bf16_t*)alloc((size_t)D_ * FHP_ * 2);
  bf16_t* Y = (bf16_t*)alloc((size_t)S_ * D_ * 2);
  bf16_t* kvT = (bf16_t*)alloc((size_t)S_ * D_ * 2);
  bf16_t* YT = (bf16_t*)alloc((size_t)S_ * D_ * 2);
  float* xst0 = (float*)alloc((size_t)S_ * 16 * 4);
  float* xst1 = (float*)alloc((size_t)S_ * 16 * 4);
  float* xst2 = (float*)alloc((size_t)S_ * 16 * 4);
  bf16_t* x1 = (bf16_t*)alloc((size_t)S_ * D_ * 2);
  bf16_t* x2 = (bf16_t*)alloc((size_t)S_ * D_ * 2);
  bf16_t* x3 = (bf16_t*)alloc((size_t)S_ * D_ * 2);
  bf16_t* z = (bf16_t*)alloc((size_t)S_ * F_ * 2 + 4096);
  float* zst = (float*)alloc((size_t)S_ * 8 * 4);
  bf16_t* u = (bf16_t*)alloc((size_t)S_ * FHP_ * 2);
  bf16_t* zn = (bf16_t*)alloc((size_t)S_ * FHP_ * 2);
  bf16_t* a1 = (bf16_t*)alloc((size_t)S_ * D_ * 2);

  (void)hipFuncSetAttribute((const void*)kconv_kernel, hipFuncAttributeMaxDynamicSharedMemorySize, CV_LDS);
  (void)hipFuncSetAttribute((const void*)g1_kernel, hipFuncAttributeMaxDynamicSharedMemorySize, G1_LDS);
  (void)hipFuncSetAttribute((const void*)g2_kernel, hipFuncAttributeMaxDynamicSharedMemorySize, G2_LDS);
  (void)hipFuncSetAttribute((const void*)g3_kernel, hipFuncAttributeMaxDynamicSharedMemorySize, G3_LDS);

  kpre_kernel<<<320, 256, 0, stream>>>(k, v, shift_k, kvT);
  kconv_kernel<<<320 + 270 + 140 + 27 + 64, 256, CV_LDS, stream>>>(kvT, ssm_k, ssm_D, YT, w_in, w_out, q, winT, cs, woutT, xst0 TSA(0));
  kt_kernel<<<320, 256, 0, stream>>>(YT, Y);
  xln_kernel<<<512, 256, 0, stream>>>(q, Y, x1, a1);
  g1_kernel<<<256, 512, G1_LDS, stream>>>(a1, winT, z TSA(1));
  zn_kernel<<<512, 256, 0, stream>>>(z, zn);
  g2_kernel<<<224, 512, G2_LDS, stream>>>(w_sp, z, zn, b_sp, u TSA(2));
  g3_kernel<<<256, 256, G3_LDS, stream>>>(u, woutT, x1, x2, xst1 TSA(3));
  xln_kernel<<<512, 256, 0, stream>>>(x2, Y, x3, a1);
  g1_kernel<<<256, 512, G1_LDS, stream>>>(a1, winT, z TSA(4));
  zn_kernel<<<512, 256, 0, stream>>>(z, zn);
  g2_kernel<<<224, 512, G2_LDS, stream>>>(w_sp, z, zn, b_sp, u TSA(5));
  g3_kernel<<<256, 256, G3_LDS, stream>>>(u, woutT, x3, out, xst2 TSA(6));
}
```

```cpp
#include <hip/hip_runtime.h>
#include <hip/hip_bf16.h>
#include <stdint.h>

typedef __attribute__((ext_vector_type(8))) short bf16x8;
typedef __attribute__((ext_vector_type(4))) short s16x4;
typedef __attribute__((ext_vector_type(4))) float f32x4;
typedef __attribute__((ext_vector_type(16))) float f32x16;
typedef __attribute__((ext_vector_type(2))) float f32x2;
typedef __attribute__((ext_vector_type(2))) __bf16 bf16x2_t;
typedef __attribute__((ext_vector_type(4))) uint32_t u32x4;
typedef __attribute__((ext_vector_type(2))) uint32_t u32x2;
typedef uint16_t bf16_t;

#define TSP
#define TSA(k)
#define TS(i)

constexpr int S_ = 2048, D_ = 640, F_ = 1728, FH_ = 864, FHP_ = 896;

__device__ __forceinline__ uint32_t pk2(float lo, float hi) {
  f32x2 f = {lo, hi};
  bf16x2_t b = __builtin_convertvector(f, bf16x2_t);
  return __builtin_bit_cast(uint32_t, b);
}
__device__ __forceinline__ float lo16(uint32_t v) { return __uint_as_float(v << 16); }
__device__ __forceinline__ float hi16(uint32_t v) { return __uint_as_float(v & 0xffff0000u); }
__device__ __forceinline__ float rbf(float x) { return lo16(pk2(x, x)); }
__device__ __forceinline__ float bf2f(bf16_t v) { return __uint_as_float(((uint32_t)v) << 16); }

__device__ __forceinline__ f32x2 ln_stats(float sum, float sumsq, float inv_n) {
  float mean = sum * inv_n;
  float var = fmaxf(sumsq * inv_n - mean * mean, 0.f);
  float mb = rbf(mean);
  float vb = rbf(var);
  float sq = rbf(sqrtf(rbf(vb + 1e-5f)));
  f32x2 r = {mb, 1.0f / sq};
  return r;
}

constexpr int TP = 144;
__device__ __forceinline__ void tile_transpose_out(const char* T, bf16_t* __restrict__ dst, size_t dstride, int tid) {
  const int w = tid >> 6, lane = tid & 63, gq = lane >> 4, i = lane & 15;
  typedef __attribute__((address_space(3))) s16x4 lds_s16x4;
#pragma unroll
  for (int h = 0; h < 2; ++h) {
    const int R0 = 8 * gq + 32 * h;
    const char* p = T + (R0 + (i >> 2)) * TP + (16 * w + 4 * (i & 3)) * 2;
    s16x4 a = __builtin_amdgcn_ds_read_tr16_b64_v4i16((lds_s16x4*)p);
    s16x4 b = __builtin_amdgcn_ds_read_tr16_b64_v4i16((lds_s16x4*)(p + 4 * TP));
    bf16x8 o = __builtin_shufflevector(a, b, 0, 1, 2, 3, 4, 5, 6, 7);
    *(bf16x8*)(dst + (size_t)(16 * w + i) * dstride + R0) = o;
  }
}

__device__ __forceinline__ void tile_transpose_out_packed(const char* T, bf16_t* __restrict__ dst, int nks, int col0, int row0, int tid) {
  const int w = tid >> 6, lane = tid & 63, gq = lane >> 4, i = lane & 15;
  typedef __attribute__((address_space(3))) s16x4 lds_s16x4;
#pragma unroll
  for (int h = 0; h < 2; ++h) {
    const int R0 = 8 * gq + 32 * h;
    const char* p = T + (R0 + (i >> 2)) * TP + (32 * (w >> 1) + 8 * (i & 3) + 4 * (w & 1)) * 2;
    s16x4 a = __builtin_amdgcn_ds_read_tr16_b64_v4i16((lds_s16x4*)p);
    s16x4 b = __builtin_amdgcn_ds_read_tr16_b64_v4i16((lds_s16x4*)(p + 4 * TP));
    bf16x8 o = __builtin_shufflevector(a, b, 0, 1, 2, 3, 4, 5, 6, 7);
    *(bf16x8*)(dst + ((size_t)((col0 >> 4) + w) * nks + (row0 >> 5) + h) * 512 + lane * 8) = o;
  }
}

__global__ __launch_bounds__(256) void kpre_kernel(
    const bf16_t* __restrict__ kin, const bf16_t* __restrict__ vin, const bf16_t* __restrict__ shift_k,
    bf16_t* __restrict__ kvT) {
  __shared__ __attribute__((aligned(16))) char T[64 * TP];
  const int tid = threadIdx.x;
  int j = blockIdx.x;
  const int r = tid >> 2, cq = tid & 3;
  {
    const int t0 = 64 * (j & 31), d0 = 64 * (j >> 5);
    const int t = t0 + r;
    u32x4 kr[2][4], vr[2], skr[2][4];
#pragma unroll
    for (int h = 0; h < 2; ++h) {
      const int d = d0 + 8 * (cq + 4 * h);
#pragma unroll
      for (int i = 0; i < 4; ++i) {
        u32x4 zz = {0u, 0u, 0u, 0u};
        kr[h][i] = (t - 3 + i >= 0) ? *(const u32x4*)(kin + (size_t)(t - 3 + i) * D_ + d) : zz;
      }
      vr[h] = *(const u32x4*)(vin + (size_t)t * D_ + d);
#pragma unroll
      for (int e2 = 0; e2 < 4; ++e2) skr[h][e2] = *(const u32x4*)(shift_k + (size_t)(d + 2 * e2) * 4);
    }
#pragma unroll
    for (int h = 0; h < 2; ++h) {
      u32x4 o;
#pragma unroll
      for (int e2 = 0; e2 < 4; ++e2) {
        const u32x4 sk = skr[h][e2];
        float res[2];
#pragma unroll
        for (int e = 0; e < 2; ++e) {
          float w0 = lo16(sk[2 * e]), w1 = hi16(sk[2 * e]), w2 = lo16(sk[2 * e + 1]), w3 = hi16(sk[2 * e + 1]);
          float k0 = e ? hi16(kr[h][0][e2]) : lo16(kr[h][0][e2]);
          float k1 = e ? hi16(kr[h][1][e2]) : lo16(kr[h][1][e2]);
          float k2 = e ? hi16(kr[h][2][e2]) : lo16(kr[h][2][e2]);
          float k3 = e ? hi16(kr[h][3][e2]) : lo16(kr[h][3][e2]);
          float ks = rbf(k0 * w3);
          ks = rbf(ks + rbf(k1 * w2));
          ks = rbf(ks + rbf(k2 * w1));
          ks = rbf(ks + rbf(k3 * w0));
          float vv = e ? hi16(vr[h][e2]) : lo16(vr[h][e2]);
          res[e] = ks * vv;
        }
        o[e2] = pk2(res[0], res[1]);
      }
      *(u32x4*)(T + r * TP + (cq + 4 * h) * 16) = o;
    }
    __syncthreads();
    tile_transpose_out(T, kvT + (size_t)d0 * S_ + t0, S_, tid);
  }
}

__device__ __forceinline__ void prep_job(int j, int tid, char* T, const bf16_t* __restrict__ w_in, const bf16_t* __restrict__ w_out,
                                         const bf16_t* __restrict__ q, bf16_t* __restrict__ winT, float* __restrict__ cs,
                                         bf16_t* __restrict__ woutT, float* __restrict__ xst) {
  const int r = tid >> 2, cq = tid & 3;
  if (j < 270) {
    const int d0 = 64 * (j % 10), f0 = 64 * (j / 10);
#pragma unroll
    for (int h = 0; h < 2; ++h)
      *(u32x4*)(T + r * TP + (cq + 4 * h) * 16) = *(const u32x4*)(w_in + (size_t)(d0 + r) * F_ + f0 + 8 * (cq + 4 * h));
    __syncthreads();
    tile_transpose_out_packed(T, winT, 20, f0, d0, tid);
    return;
  }
  j -= 270;
  if (j < 140) {
    const int d0 = 64 * (j % 10), f0 = 64 * (j / 10);
#pragma unroll
    for (int h = 0; h < 2; ++h) {
      u32x4 zz = {0u, 0u, 0u, 0u};
      *(u32x4*)(T + r * TP + (cq + 4 * h) * 16) =
          (f0 + r < FH_) ? *(const u32x4*)(w_out + (size_t)(f0 + r) * D_ + d0 + 8 * (cq + 4 * h)) : zz;
    }
    __syncthreads();
    tile_transpose_out(T, woutT + (size_t)d0 * FHP_ + f0, FHP_, tid);
    return;
  }
  j -= 140;
  if (j < 27) {
    const int f0 = 64 * j, c8 = tid & 7, rr = tid >> 3;
    float s[8] = {0.f, 0.f, 0.f, 0.f, 0.f, 0.f, 0.f, 0.f};
    u32x4 w[20];
#pragma unroll
    for (int it = 0; it < 20; ++it) w[it] = *(const u32x4*)(w_in + (size_t)(32 * it + rr) * F_ + f0 + 8 * c8);
#pragma unroll
    for (int it = 0; it < 20; ++it) {
#pragma unroll
      for (int i = 0; i < 4; ++i) { s[2 * i] += lo16(w[it][i]); s[2 * i + 1] += hi16(w[it][i]); }
    }
    float* R = (float*)T;
#pragma unroll
    for (int i = 0; i < 8; ++i) R[rr * 64 + 8 * c8 + i] = s[i];
    __syncthreads();
    if (tid < 64) {
      float a = 0.f;
#pragma unroll
      for (int i = 0; i < 32; ++i) a += R[i * 64 + tid];
      cs[f0 + tid] = a;
    }
    return;
  }
  j -= 27;
  {
    const int r8 = tid >> 3, c = tid & 7;
    const int row = 32 * j + r8;
    float sum = 0.f, sq = 0.f;
#pragma unroll
    for (int it = 0; it < 10; ++it) {
      u32x4 w = *(const u32x4*)(q + (size_t)row * D_ + 64 * it + 8 * c);
#pragma unroll
      for (int i = 0; i < 4; ++i) {
        float a = lo16(w[i]), b = hi16(w[i]);
        sum += a + b; sq += a * a + b * b;
      }
    }
    sum += __shfl_xor(sum, 1); sum += __shfl_xor(sum, 2); sum += __shfl_xor(sum, 4);
    sq += __shfl_xor(sq, 1); sq += __shfl_xor(sq, 2); sq += __shfl_xor(sq, 4);
    f32x2 o = {c == 0 ? sum : 0.f, c == 0 ? sq : 0.f};
    *(f32x2*)(xst + ((size_t)row * 8 + c) * 2) = o;
  }
}

__global__ __launch_bounds__(256) void kt_kernel(const bf16_t* __restrict__ YT, bf16_t* __restrict__ Y) {
  __shared__ __attribute__((aligned(16))) char T[64 * TP];
  const int tid = threadIdx.x;
  const int j = blockIdx.x;
  const int r = tid >> 2, cq = tid & 3;
  const int t0 = 64 * (j & 31), d0 = 64 * (j >> 5);
#pragma unroll
  for (int h = 0; h < 2; ++h)
    *(u32x4*)(T + r * TP + (cq + 4 * h) * 16) = *(const u32x4*)(YT + (size_t)(d0 + r) * S_ + t0 + 8 * (cq + 4 * h));
  __syncthreads();
  tile_transpose_out(T, Y + (size_t)t0 * D_ + d0, D_, tid);
}

constexpr int CV_HP = 4416;
constexpr int CV_HCH = 4 * CV_HP;
constexpr int CV_UCH = 96 * 80;
constexpr int CV_OFF_H = 0;
constexpr int CV_OFF_U = CV_OFF_H + 2 * CV_HCH;
constexpr int CV_OFF_R = CV_OFF_U + 2 * CV_UCH;
constexpr int CV_OFF_Y = CV_OFF_R + 2 * 8192;
constexpr int CV_LDS = CV_OFF_Y + 2 * 4096;

__global__ __launch_bounds__(256, 2) void kconv_kernel(
    const bf16_t* __restrict__ kvT, const bf16_t* __restrict__ ssm_k, const bf16_t* __restrict__ ssm_D,
    bf16_t* __restrict__ YT, const bf16_t* __restrict__ w_in, const bf16_t* __restrict__ w_out, const bf16_t* __restrict__ q,
    bf16_t* __restrict__ winT, float* __restrict__ cs, bf16_t* __restrict__ woutT, float* __restrict__ xst TSP) {
  extern __shared__ __attribute__((aligned(16))) char smem[];
  const int tid = threadIdx.x;
  if (blockIdx.x >= 320) {
    prep_job(blockIdx.x - 320, tid, smem, w_in, w_out, q, winT, cs, woutT, xst);
    return;
  }
  TS(0);
  const int c0 = 2 * blockIdx.x;
  u32x4 ur[2];
#pragma unroll
  for (int i = 0; i < 2; ++i) ur[i] = *(const u32x4*)(kvT + (size_t)c0 * S_ + 8 * (tid + 256 * i));
  u32x2 hr[3][3];
#pragma unroll
  for (int rd = 0; rd < 3; ++rd) {
    int task = tid + 256 * rd;
    int ch = task / 262, c = task % 262 - 6;
#pragma unroll
    for (int i = 0; i < 3; ++i) {
      int idx = 8 * c + 4 * i;
      u32x2 z = {0u, 0u};
      hr[rd][i] = (task < 524 && idx >= 0 && idx < 2048) ? *(const u32x2*)(ssm_k + (size_t)(c0 + ch) * S_ + idx) : z;
    }
  }
  for (int i = tid; i < 2 * 160; i += 256) {
    int ch = i / 160, o = i % 160;
    u32x4 z = {0u, 0u, 0u, 0u};
    *(u32x4*)(smem + CV_OFF_U + ch * CV_UCH + o * 16) = z;
  }
  TS(1);
#pragma unroll
  for (int i = 0; i < 2; ++i) {
    const int e = 8 * (tid + 256 * i), ch = e >> 11, s = e & 2047;
    *(u32x4*)(smem + CV_OFF_U + ch * CV_UCH + ((s >> 5) + 32) * 80 + (s & 31) * 2) = ur[i];
  }
#pragma unroll
  for (int rd = 0; rd < 3; ++rd) {
    int task = tid + 256 * rd;
    if (task < 524) {
      int ch = task / 262, c = task % 262 - 6;
      uint32_t w[6] = {hr[rd][0][0], hr[rd][0][1], hr[rd][1][0], hr[rd][1][1], hr[rd][2][0], hr[rd][2][1]};
#pragma unroll
      for (int p = 0; p < 4; ++p) {
#pragma unroll
        for (int gi = 0; gi < 2; ++gi) {
          const int ao = ((1 - p) & 3) + 4 * gi;
          int a = 8 * c + ao;
          int qx = 2045 - p - a;
          auto el = [&](int e) -> uint32_t { return (e & 1) ? (w[e >> 1] >> 16) : (w[e >> 1] & 0xffff); };
          u32x2 o = {el(ao + 3) | (el(ao + 2) << 16), el(ao + 1) | (el(ao) << 16)};
          if (qx >= 0 && qx < 2088) *(u32x2*)(smem + CV_OFF_H + ch * CV_HCH + p * CV_HP + qx * 2) = o;
        }
      }
    }
  }
  TS(2);
  __syncthreads();
  TS(3);
  const int wid = tid >> 6, lane = tid & 63;
  const int ch = wid & 1, kb = wid >> 1;
  const int m = lane & 31, hh = lane >> 5;
  {
    const int p = (4 - (m & 3)) & 3;
    const char* abase = smem + CV_OFF_H + ch * CV_HCH + p * CV_HP + (2048 - m + 8 * hh - p + 16 * kb) * 2;
    const char* bbase = smem + CV_OFF_U + ch * CV_UCH + (m + 32) * 80 + 32 * kb + 16 * hh;
    f32x16 acc0 = {}, acc1 = {};
    u32x2 ra[3][4];
    bf16x8 rbB[3][2];
    auto ld = [&](int sl, int dl) {
      typedef const volatile __attribute__((address_space(3))) u32x2* lds_v2;
      ra[sl][0] = *(lds_v2)(abase - 64 * dl);
      ra[sl][1] = *(lds_v2)(abase - 64 * dl + 8);
      ra[sl][2] = *(lds_v2)(abase - 64 * (dl + 32));
      ra[sl][3] = *(lds_v2)(abase - 64 * (dl + 32) + 8);
      rbB[sl][0] = *(const bf16x8*)(bbase - 80 * dl);
      rbB[sl][1] = *(const bf16x8*)(bbase + 80 * (32 - dl));
    };
    ld(0, 0);
    ld(1, 1);
#pragma unroll
    for (int dl = 0; dl < 32; ++dl) {
      if (dl + 2 < 32) ld((dl + 2) % 3, dl + 2);
      __builtin_amdgcn_sched_barrier(0);
      const int sl = dl % 3;
      u32x4 A0u = {ra[sl][0][0], ra[sl][0][1], ra[sl][1][0], ra[sl][1][1]};
      u32x4 A1u = {ra[sl][2][0], ra[sl][2][1], ra[sl][3][0], ra[sl][3][1]};
      bf16x8 A0 = __builtin_bit_cast(bf16x8, A0u), A1 = __builtin_bit_cast(bf16x8, A1u);
      acc0 = __builtin_amdgcn_mfma_f32_32x32x16_bf16(A0, rbB[sl][0], acc0, 0, 0, 0);
      acc1 = __builtin_amdgcn_mfma_f32_32x32x16_bf16(A0, rbB[sl][1], acc1, 0, 0, 0);
      acc1 = __builtin_amdgcn_mfma_f32_32x32x16_bf16(A1, rbB[sl][0], acc1, 0, 0, 0);
      __builtin_amdgcn_sched_barrier(0);
    }
    TS(4);
    float* red = (float*)(smem + CV_OFF_R + ch * 8192);
    if (kb == 1) {
#pragma unroll
      for (int r = 0; r < 16; ++r) { red[r * 64 + lane] = acc0[r]; red[(16 + r) * 64 + lane] = acc1[r]; }
    }
    __syncthreads();
    if (kb == 0) {
      const float Dv = bf2f(ssm_D[c0 + ch]);
      bf16_t* yb = (bf16_t*)(smem + CV_OFF_Y + ch * 4096);
#pragma unroll
      for (int half = 0; half < 2; ++half) {
        const int a = m + 32 * half;
#pragma unroll
        for (int rq = 0; rq < 4; ++rq) {
          const int b0 = 8 * rq + 4 * hh;
          u32x2 kvw = *(const u32x2*)(smem + CV_OFF_U + ch * CV_UCH + (a + 32) * 80 + b0 * 2);
          float kvv[4] = {lo16(kvw[0]), hi16(kvw[0]), lo16(kvw[1]), hi16(kvw[1])};
          float o[4];
#pragma unroll
          for (int i = 0; i < 4; ++i) {
            const int r = 4 * rq + i;
            float y = (half ? acc1[r] : acc0[r]) + red[(16 * half + r) * 64 + lane];
            o[i] = rbf(y) + rbf(kvv[i] * Dv);
          }
          u32x2 ow = {pk2(o[0], o[1]), pk2(o[2], o[3])};
          *(u32x2*)(yb + 32 * a + b0) = ow;
        }
      }
    }
    __syncthreads();
    TS(5);
#pragma unroll
    for (int i = 0; i < 2; ++i) {
      const int e = tid + 256 * i;
      *(u32x4*)(YT + (size_t)c0 * S_ + 8 * e) = *(const u32x4*)(smem + CV_OFF_Y + 16 * e);
    }
    TS(6);
  }
}

template <int I> struct IC { static constexpr int value = I; };
template <int N, int I = 0, typename F>
__device__ __forceinline__ void static_for(F&& f) {
  if constexpr (I < N) { f(IC<I>{}); static_for<N, I + 1>(f); }
}

__device__ __forceinline__ float dpp_add(float v, int ctrl_sel) {
  int x = __float_as_int(v), y;
  switch (ctrl_sel) {
    case 0: y = __builtin_amdgcn_update_dpp(0, x, 0xB1, 0xf, 0xf, false); break;
    case 1: y = __builtin_amdgcn_update_dpp(0, x, 0x4E, 0xf, 0xf, false); break;
    case 2: y = __builtin_amdgcn_update_dpp(0, x, 0x141, 0xf, 0xf, false); break;
    default: y = __builtin_amdgcn_update_dpp(0, x, 0x140, 0xf, 0xf, false); break;
  }
  return v + __int_as_float(y);
}
__device__ __forceinline__ float wave_sum(float v) {
  v = dpp_add(v, 0); v = dpp_add(v, 1); v = dpp_add(v, 2); v = dpp_add(v, 3);
  int x = __float_as_int(v);
  return (__int_as_float(__builtin_amdgcn_readlane(x, 0)) + __int_as_float(__builtin_amdgcn_readlane(x, 16))) +
         (__int_as_float(__builtin_amdgcn_readlane(x, 32)) + __int_as_float(__builtin_amdgcn_readlane(x, 48)));
}

__global__ __launch_bounds__(256) void xln_kernel(const bf16_t* __restrict__ x0, const bf16_t* __restrict__ Yb,
                                                  bf16_t* __restrict__ x1, bf16_t* __restrict__ a1) {
  const int lane = threadIdx.x & 63;
  const int row = 4 * blockIdx.x + (threadIdx.x >> 6);
  const uint32_t* px = (const uint32_t*)(x0 + (size_t)row * D_);
  const uint32_t* py = (const uint32_t*)(Yb + (size_t)row * D_);
  uint32_t xv[5], yv[5];
#pragma unroll
  for (int i = 0; i < 5; ++i) { xv[i] = px[lane + 64 * i]; yv[i] = py[lane + 64 * i]; }
  float s0 = 0.f, s1 = 0.f;
#pragma unroll
  for (int i = 0; i < 5; ++i) { float a = lo16(xv[i]), b = hi16(xv[i]); s0 += a + b; s1 += a * a + b * b; }
  const f32x2 st = ln_stats(wave_sum(s0), wave_sum(s1), 1.0f / D_);
  float t0 = 0.f, t1 = 0.f;
  uint32_t* p1 = (uint32_t*)(x1 + (size_t)row * D_);
#pragma unroll
  for (int i = 0; i < 5; ++i) {
    float a = lo16(xv[i]), b = hi16(xv[i]);
    float da = rbf(rbf(a - st[0]) * st[1]), db = rbf(rbf(b - st[0]) * st[1]);
    float ta = rbf(da * lo16(yv[i])), tb = rbf(db * hi16(yv[i]));
    uint32_t w = pk2(a + ta, b + tb);
    xv[i] = w;
    p1[lane + 64 * i] = w;
    float xa = lo16(w), xb = hi16(w);
    t0 += xa + xb; t1 += xa * xa + xb * xb;
  }
  const f32x2 su = ln_stats(wave_sum(t0), wave_sum(t1), 1.0f / D_);
  uint32_t* pa = (uint32_t*)(a1 + (size_t)row * D_);
#pragma unroll
  for (int i = 0; i < 5; ++i)
    pa[lane + 64 * i] = pk2(rbf(lo16(xv[i]) - su[0]) * su[1], rbf(hi16(xv[i]) - su[0]) * su[1]);
}

__device__ __forceinline__ void glds16(const void* gsrc, uint32_t lds_dst) {
  uint32_t keep;
  asm volatile("s_mov_b32 %0, m0\n\ts_mov_b32 m0, %2\n\ts_nop 0\n\tglobal_load_lds_dwordx4 %1, off\n\ts_mov_b32 m0, %0"
               : "=&s"(keep) : "v"(gsrc), "s"(lds_dst) : "memory");
}
__device__ __forceinline__ uint32_t lds_addr(const void* p) {
  return (uint32_t)(uintptr_t)(const __attribute__((address_space(3))) char*)p;
}
__device__ __forceinline__ void gload16(bf16x8& dst, const void* p) {
  asm volatile("global_load_dwordx4 %0, %1, off" : "=v"(dst) : "v"(p) : "memory");
}
template <int N>
__device__ __forceinline__ void wait_vm2(bf16x8& a, bf16x8& b) {
  asm volatile("s_waitcnt vmcnt(%2)" : "+v"(a), "+v"(b) : "n"(N) : "memory");
}
#define WAIT_VM(n) asm volatile("s_waitcnt vmcnt(" #n ")" ::: "memory")
#define WAIT_LGKM0() asm volatile("s_waitcnt lgkmcnt(0)" ::: "memory")
#define RAW_BAR() __builtin_amdgcn_s_barrier()

constexpr int G1_LDS = 81920;

__device__ __forceinline__ f32x2 gelu2(f32x2 x) {
  const float c1 = -2.f * 0.7978845608028654f * 1.4426950408889634f, c2 = c1 * 0.044715f;
  f32x2 t = x * x;
  f32x2 a = x * (t * c2 + c1);
  f32x2 d = {1.f + __builtin_amdgcn_exp2f(a[0]), 1.f + __builtin_amdgcn_exp2f(a[1])};
  f32x2 r = {__builtin_amdgcn_rcpf(d[0]), __builtin_amdgcn_rcpf(d[1])};
  return x * r;
}
__device__ __forceinline__ f32x2 rbf2(f32x2 v) {
  uint32_t w = pk2(v[0], v[1]);
  f32x2 r = {lo16(w), hi16(w)};
  return r;
}

__global__ __launch_bounds__(512, 1) void g1_kernel(
    const bf16_t* __restrict__ a1, const bf16_t* __restrict__ winT, bf16_t* __restrict__ z TSP) {
  extern __shared__ __attribute__((aligned(16))) char smem[];
  const int tid = threadIdx.x;
  TS(0);
  const int xcd = blockIdx.x & 7, idx = blockIdx.x >> 3;
  const int mt = 8 * (xcd >> 1) + (idx & 7);
  const int nt = 4 * (xcd & 1) + (idx >> 3);
  const int ps = nt < 6 ? 7 * nt : 42 + 6 * (nt - 6);
  const int np = nt < 6 ? 7 : 6;
  const int m0 = 64 * mt;
  const int wid = __builtin_amdgcn_readfirstlane(tid >> 6), lane = tid & 63;
  {
    const int lr = lane >> 3, lc = (lane & 7) ^ lr;
    const bf16_t* ap = a1 + (size_t)(m0 + 8 * wid + lr) * D_ + 8 * lc;
    const uint32_t lbase = lds_addr(smem) + wid * 1024;
#pragma unroll
    for (int kt = 0; kt < 10; ++kt) glds16(ap + 64 * kt, lbase + kt * 8192);
  }
  if (wid >= np) {
    WAIT_VM(0);
    RAW_BAR();
    return;
  }
  const int fr = lane & 15, g = lane >> 4;
  constexpr int P = 10;
  const bf16_t* wp[2];
#pragma unroll
  for (int i = 0; i < 2; ++i) wp[i] = winT + (size_t)(2 * (ps + wid) + i) * (20 * 512) + lane * 8;
  bf16x8 wv[P][2];
#pragma unroll
  for (int s = 0; s < P; ++s)
#pragma unroll
    for (int i = 0; i < 2; ++i) gload16(wv[s][i], wp[i] + 512 * s);
  TS(1);
  WAIT_VM(20);
  RAW_BAR();
  TS(2);
  f32x4 acc[2][4] = {};
  {
    int xoff[4];
#pragma unroll
    for (int j = 0; j < 4; ++j) xoff[j] = (16 * j + fr) * 128;
    auto step = [&](auto KS) {
      constexpr int ks = decltype(KS)::value;
      bf16x8 X[4];
#pragma unroll
      for (int j = 0; j < 4; ++j)
        X[j] = *(const bf16x8*)(smem + (ks >> 1) * 8192 + xoff[j] + (((4 * (ks & 1) + g) ^ (fr & 7)) << 4));
      constexpr int newest = (ks + P - 1 < 19) ? ks + P - 1 : 19;
      wait_vm2<2 * (newest - ks)>(wv[ks % P][0], wv[ks % P][1]);
#pragma unroll
      for (int j = 0; j < 4; ++j) {
        acc[0][j] = __builtin_amdgcn_mfma_f32_16x16x32_bf16(wv[ks % P][0], X[j], acc[0][j], 0, 0, 0);
        acc[1][j] = __builtin_amdgcn_mfma_f32_16x16x32_bf16(wv[ks % P][1], X[j], acc[1][j], 0, 0, 0);
      }
      if constexpr (ks + P < 20) {
        gload16(wv[ks % P][0], wp[0] + 512 * (ks + P));
        gload16(wv[ks % P][1], wp[1] + 512 * (ks + P));
      }
    };
    static_for<20>(step);
  }
  TS(3);
  bf16_t* zp = z + (size_t)(m0 + fr) * F_ + 32 * (ps + wid) + 8 * g;
#pragma unroll
  for (int j = 0; j < 4; ++j) {
    u32x4 o;
#pragma unroll
    for (int i = 0; i < 2; ++i) {
      f32x2 lo = {acc[i][j][0], acc[i][j][1]}, hi = {acc[i][j][2], acc[i][j][3]};
      lo = gelu2(rbf2(lo)); hi = gelu2(rbf2(hi));
      o[2 * i] = pk2(lo[0], lo[1]); o[2 * i + 1] = pk2(hi[0], hi[1]);
    }
    *(u32x4*)(zp + (size_t)(16 * j) * F_) = o;
  }
  TS(4);
}

__global__ __launch_bounds__(256) void zn_kernel(const bf16_t* __restrict__ z, bf16_t* __restrict__ zn) {
  const int lane = threadIdx.x & 63;
  const int row = 4 * blockIdx.x + (threadIdx.x >> 6);
  const bf16_t* src = z + (size_t)row * F_ + FH_;
  u32x4 zz = {0u, 0u, 0u, 0u};
  u32x4 v0 = *(const u32x4*)(src + 8 * lane);
  u32x4 v1 = lane < 44 ? *(const u32x4*)(src + 8 * (64 + lane)) : zz;
  float s0 = 0.f, s1 = 0.f;
#pragma unroll
  for (int i = 0; i < 4; ++i) {
    float a = lo16(v0[i]), b = hi16(v0[i]), c = lo16(v1[i]), d = hi16(v1[i]);
    s0 += (a + b) + (c + d);
    s1 += (a * a + b * b) + (c * c + d * d);
  }
#pragma unroll
  for (int o = 1; o < 64; o <<= 1) { s0 += __shfl_xor(s0, o); s1 += __shfl_xor(s1, o); }
  const f32x2 st = ln_stats(s0, s1, 1.0f / FH_);
  u32x4 o0, o1;
#pragma unroll
  for (int i = 0; i < 4; ++i) {
    o0[i] = pk2(rbf(lo16(v0[i]) - st[0]) * st[1], rbf(hi16(v0[i]) - st[0]) * st[1]);
    o1[i] = lane < 44 ? pk2(rbf(lo16(v1[i]) - st[0]) * st[1], rbf(hi16(v1[i]) - st[0]) * st[1]) : 0u;
  }
  bf16_t* dst = zn + (size_t)row * FHP_;
  *(u32x4*)(dst + 8 * lane) = o0;
  if (lane < 48) *(u32x4*)(dst + 8 * (64 + lane)) = o1;
}

constexpr int G2_NS = 6;
constexpr int G2_STG = 24576;
constexpr int G2_LDS = G2_NS * G2_STG;

__global__ __launch_bounds__(512, 1) void g2_kernel(
    const bf16_t* __restrict__ wsp, const bf16_t* __restrict__ z, const bf16_t* __restrict__ zn,
    const bf16_t* __restrict__ bsp, bf16_t* __restrict__ u TSP) {
  extern __shared__ __attribute__((aligned(16))) char smem[];
  const int tid = threadIdx.x;
  TS(0);
  const int xcd = blockIdx.x & 7, idx = blockIdx.x >> 3;
  const int mt = 4 * (xcd >> 1) + (idx & 3);
  const int nt = 7 * (xcd & 1) + (idx >> 2);
  const int t0 = 128 * mt, f0 = 64 * nt;
  const int wid = __builtin_amdgcn_readfirstlane(tid >> 6), lane = tid & 63;
  const int rot = idx;
  const int lr = lane >> 3, lc = (lane & 7) ^ lr;
  const bf16_t* ap0 = wsp + (size_t)(t0 + 16 * wid + lr) * S_ + 8 * lc;
  const bf16_t* ap1 = ap0 + (size_t)8 * S_;
  const int brow = 8 * wid + lr;
  const int bsw = (((brow >> 1) & 1) | (((brow >> 3) & 1) << 1)) << 1;
  const bf16_t* bp = zn + (size_t)brow * FHP_ + f0 + 8 * ((lane & 7) ^ bsw);
  const uint32_t lbase = lds_addr(smem) + wid * 2048;
  const uint32_t lbaseB = lds_addr(smem) + 16384 + wid * 1024;
  auto issue = [&](int kt, int slot) {
    const int ke = (kt + rot) & 31;
    glds16(ap0 + 64 * ke, lbase + slot * G2_STG);
    glds16(ap1 + 64 * ke, lbase + slot * G2_STG + 1024);
    glds16(bp + (size_t)(64 * ke) * FHP_, lbaseB + slot * G2_STG);
  };
#pragma unroll
  for (int s = 0; s < G2_NS - 1; ++s) issue(s, s);
  TS(1);
  const int fr = lane & 15, g = lane >> 4;
  const int wm = wid & 3, wn = wid >> 2;
  f32x4 acc[2][2] = {};
  const int q4 = (lane & 15) >> 2, p4 = lane & 3;
  int aoff[2][2], boff[2][2][2];
#pragma unroll
  for (int ks = 0; ks < 2; ++ks) {
#pragma unroll
    for (int i = 0; i < 2; ++i) {
      int rr = 32 * wm + 16 * i + fr;
      aoff[ks][i] = rr * 128 + (((4 * ks + g) ^ (rr & 7)) << 4);
#pragma unroll
      for (int t = 0; t < 2; ++t) {
        int row = 32 * ks + 8 * g + 4 * t + q4;
        int ch = 2 * (2 * wn + i) + (p4 >> 1);
        int sw = (((row >> 1) & 1) | (((row >> 3) & 1) << 1)) << 1;
        boff[ks][i][t] = 16384 + row * 128 + ((ch ^ sw) << 4) + 8 * (p4 & 1);
      }
    }
  }
  auto readF = [&](bf16x8* T, bf16x8* Z, int ks, int slot) {
#pragma unroll
    for (int i = 0; i < 2; ++i) T[i] = *(const bf16x8*)(smem + slot * G2_STG + aoff[ks][i]);
#pragma unroll
    for (int i = 0; i < 2; ++i) {
      typedef __attribute__((address_space(3))) s16x4 lds_s16x4;
      s16x4 h0 = __builtin_amdgcn_ds_read_tr16_b64_v4i16((lds_s16x4*)(smem + slot * G2_STG + boff[ks][i][0]));
      s16x4 h1 = __builtin_amdgcn_ds_read_tr16_b64_v4i16((lds_s16x4*)(smem + slot * G2_STG + boff[ks][i][1]));
      Z[i] = __builtin_shufflevector(h0, h1, 0, 1, 2, 3, 4, 5, 6, 7);
    }
  };
  auto mm = [&](const bf16x8* T, const bf16x8* Z) {
#pragma unroll
    for (int i = 0; i < 2; ++i)
#pragma unroll
      for (int j = 0; j < 2; ++j) acc[i][j] = __builtin_amdgcn_mfma_f32_16x16x32_bf16(Z[i], T[j], acc[i][j], 0, 0, 0);
  };
  bf16x8 TA[2][2], ZA[2][2], TB[2][2], ZB[2][2];
  WAIT_VM(12);
  RAW_BAR();
  readF(TA[0], ZA[0], 0, 0);
  readF(TA[1], ZA[1], 1, 0);
  int slot = 0;
  auto step = [&](int kt, bf16x8 (*Tc)[2], bf16x8 (*Zc)[2], bf16x8 (*Tn)[2], bf16x8 (*Zn)[2]) {
    WAIT_VM(9);
    RAW_BAR();
    {
      int kn = kt + G2_NS - 1; kn = kn < 32 ? kn : 31;
      int sn = slot == 0 ? G2_NS - 1 : slot - 1;
      issue(kn, sn);
    }
    const int slot1 = slot + 1 == G2_NS ? 0 : slot + 1;
    if (wn == 0) {
      readF(Tn[0], Zn[0], 0, slot1);
      readF(Tn[1], Zn[1], 1, slot1);
      __builtin_amdgcn_sched_barrier(0);
      mm(Tc[0], Zc[0]);
      mm(Tc[1], Zc[1]);
    } else {
      mm(Tc[0], Zc[0]);
      mm(Tc[1], Zc[1]);
      __builtin_amdgcn_sched_barrier(0);
      readF(Tn[0], Zn[0], 0, slot1);
      readF(Tn[1], Zn[1], 1, slot1);
    }
    __builtin_amdgcn_sched_barrier(0);
    slot = slot1;
  };
#pragma unroll 1
  for (int kt = 0; kt < 32; kt += 2) {
    step(kt, TA, ZA, TB, ZB);
    step(kt + 1, TB, ZB, TA, ZA);
  }
  WAIT_VM(0);
  TS(3);
#pragma unroll
  for (int j = 0; j < 2; ++j) {
    const int t = t0 + 32 * wm + 16 * j + fr;
    const float bb = bf2f(bsp[t]);
#pragma unroll
    for (int i = 0; i < 2; ++i) {
      const int f = f0 + 32 * wn + 16 * i + 4 * g;
      u32x2 w = {0u, 0u};
      if (f < FH_) {
        u32x2 z1 = *(const u32x2*)(z + (size_t)t * F_ + f);
        float g0 = rbf(rbf(acc[i][j][0]) + bb), g1 = rbf(rbf(acc[i][j][1]) + bb);
        float g2 = rbf(rbf(acc[i][j][2]) + bb), g3 = rbf(rbf(acc[i][j][3]) + bb);
        w[0] = pk2(lo16(z1[0]) * g0, hi16(z1[0]) * g1);
        w[1] = pk2(lo16(z1[1]) * g2, hi16(z1[1]) * g3);
      }
      *(u32x2*)(u + (size_t)t * FHP_ + f) = w;
    }
  }
  TS(4);
}

constexpr int G3_OFF_A = 0;
constexpr int G3_OFF_B = 16384;
constexpr int G3_LDS = 36864;

__global__ __launch_bounds__(256, 1) void g3_kernel(
    const bf16_t* __restrict__ u, const bf16_t* __restrict__ woutT, const bf16_t* __restrict__ x1,
    bf16_t* __restrict__ xout, float* __restrict__ xst TSP) {
  extern __shared__ __attribute__((aligned(16))) char smem[];
  const int tid = threadIdx.x;
  TS(0);
  const int xcd = blockIdx.x & 7, idx = blockIdx.x >> 3;
  const int mt = 8 * (xcd >> 1) + (idx & 7);
  const int nt = 4 * (xcd & 1) + (idx >> 3);
  const int t0 = 64 * mt, d0 = 80 * nt;
  const int r = tid >> 3, c = tid & 7;
  constexpr int PD = 4;
  u32x4 ra[PD][2], rb[PD][3];
  const bool b2 = tid < 128;
  auto loadS = [&](int slot, int kt) {
    ra[slot][0] = *(const u32x4*)(u + (size_t)(t0 + r) * FHP_ + 64 * kt + 8 * c);
    ra[slot][1] = *(const u32x4*)(u + (size_t)(t0 + r + 32) * FHP_ + 64 * kt + 8 * c);
    rb[slot][0] = *(const u32x4*)(woutT + (size_t)(d0 + r) * FHP_ + 64 * kt + 8 * c);
    rb[slot][1] = *(const u32x4*)(woutT + (size_t)(d0 + r + 32) * FHP_ + 64 * kt + 8 * c);
    if (b2) rb[slot][2] = *(const u32x4*)(woutT + (size_t)(d0 + r + 64) * FHP_ + 64 * kt + 8 * c);
  };
  auto writeS = [&](int slot, int buf) {
    const int sw = (c ^ (r & 7)) << 4;
    *(u32x4*)(smem + G3_OFF_A + buf * 8192 + r * 128 + sw) = ra[slot][0];
    *(u32x4*)(smem + G3_OFF_A + buf * 8192 + (r + 32) * 128 + sw) = ra[slot][1];
    *(u32x4*)(smem + G3_OFF_B + buf * 10240 + r * 128 + sw) = rb[slot][0];
    *(u32x4*)(smem + G3_OFF_B + buf * 10240 + (r + 32) * 128 + sw) = rb[slot][1];
    if (b2) *(u32x4*)(smem + G3_OFF_B + buf * 10240 + (r + 64) * 128 + sw) = rb[slot][2];
  };
#pragma unroll
  for (int d = 0; d < PD; ++d) loadS(d, d);
  writeS(0, 0);
  loadS(0, PD);
  __syncthreads();
  TS(1);
  const int wid = tid >> 6, lane = tid & 63, fr = lane & 15, g = lane >> 4;
  f32x4 acc[5] = {};
#pragma unroll
  for (int kt = 0; kt < 14; ++kt) {
    const int buf = kt & 1;
    if (kt + 1 < 14) {
      writeS((kt + 1) % PD, buf ^ 1);
      if (kt + 1 + PD < 14) loadS((kt + 1) % PD, kt + 1 + PD);
    }
#pragma unroll
    for (int ks = 0; ks < 2; ++ks) {
      const int rr = 16 * wid + fr;
      bf16x8 T = *(const bf16x8*)(smem + G3_OFF_A + buf * 8192 + rr * 128 + (((4 * ks + g) ^ (rr & 7)) << 4));
#pragma unroll
      for (int i = 0; i < 5; ++i) {
        const int wr = 16 * i + fr;
        bf16x8 W = *(const bf16x8*)(smem + G3_OFF_B + buf * 10240 + wr * 128 + (((4 * ks + g) ^ (wr & 7)) << 4));
        acc[i] = __builtin_amdgcn_mfma_f32_16x16x32_bf16(W, T, acc[i], 0, 0, 0);
      }
    }
    __syncthreads();
  }
  TS(2);
  const int t = t0 + 16 * wid + fr;
  float s0 = 0.f, s1 = 0.f;
#pragma unroll
  for (int i = 0; i < 5; ++i) {
    const int d = d0 + 16 * i + 4 * g;
    u32x2 xr = *(const u32x2*)(x1 + (size_t)t * D_ + d);
    u32x2 w = {pk2(lo16(xr[0]) + rbf(acc[i][0]), hi16(xr[0]) + rbf(acc[i][1])),
               pk2(lo16(xr[1]) + rbf(acc[i][2]), hi16(xr[1]) + rbf(acc[i][3]))};
    *(u32x2*)(xout + (size_t)t * D_ + d) = w;
    float a = lo16(w[0]), b = hi16(w[0]), cc = lo16(w[1]), dd = hi16(w[1]);
    s0 += a + b + cc + dd; s1 += a * a + b * b + cc * cc + dd * dd;
  }
  s0 += __shfl_xor(s0, 16); s0 += __shfl_xor(s0, 32);
  s1 += __shfl_xor(s1, 16); s1 += __shfl_xor(s1, 32);
  if (g == 0) {
    f32x2 o = {s0, s1};
    *(f32x2*)(xst + ((size_t)t * 8 + nt) * 2) = o;
  }
  TS(3);
}

extern "C" void kernel_launch(void* const* d_in, const int* in_sizes, int n_in,
                              void* d_out, int out_size, void* d_ws, size_t ws_size,
                              hipStream_t stream) {
  const bf16_t* q = (const bf16_t*)d_in[0];
  const bf16_t* k = (const bf16_t*)d_in[1];
  const bf16_t* v = (const bf16_t*)d_in[2];
  const bf16_t* shift_k = (const bf16_t*)d_in[3];
  const bf16_t* ssm_k = (const bf16_t*)d_in[4];
  const bf16_t* ssm_D = (const bf16_t*)d_in[5];
  const bf16_t* w_in = (const bf16_t*)d_in[7];
  const bf16_t* w_sp = (const bf16_t*)d_in[8];
  const bf16_t* b_sp = (const bf16_t*)d_in[9];
  const bf16_t* w_out = (const bf16_t*)d_in[10];
  bf16_t* out = (bf16_t*)d_out;

  char* ws = (char*)d_ws;
  size_t off = 0;
  auto alloc = [&](size_t bytes) { char* p = ws + off; off += (bytes + 255) & ~(size_t)255; return p; };
  bf16_t* winT = (bf16_t*)alloc((size_t)F_ * D_ * 2);
  float* cs = (float*)alloc(F_ * 4);
  bf16_t* woutT = (bf16_t*)alloc((size_t)D_ * FHP_ * 2);
  bf16_t* Y = (bf16_t*)alloc((size_t)S_ * D_ * 2);
  bf16_t* kvT = (bf16_t*)alloc((size_t)S_ * D_ * 2);
  bf16_t* YT = (bf16_t*)alloc((size_t)S_ * D_ * 2);
  float* xst0 = (float*)alloc((size_t)S_ * 16 * 4);
  float* xst1 = (float*)alloc((size_t)S_ * 16 * 4);
  float* xst2 = (float*)alloc((size_t)S_ * 16 * 4);
  bf16_t* x1 = (bf16_t*)alloc((size_t)S_ * D_ * 2);
  bf16_t* x2 = (bf16_t*)alloc((size_t)S_ * D_ * 2);
  bf16_t* x3 = (bf16_t*)alloc((size_t)S_ * D_ * 2);
  bf16_t* z = (bf16_t*)alloc((size_t)S_ * F_ * 2 + 4096);
  float* zst = (float*)alloc((size_t)S_ * 8 * 4);
  bf16_t* u = (bf16_t*)alloc((size_t)S_ * FHP_ * 2);
  bf16_t* zn = (bf16_t*)alloc((size_t)S_ * FHP_ * 2);
  bf16_t* a1 = (bf16_t*)alloc((size_t)S_ * D_ * 2);

  (void)hipFuncSetAttribute((const void*)kconv_kernel, hipFuncAttributeMaxDynamicSharedMemorySize, CV_LDS);
  (void)hipFuncSetAttribute((const void*)g1_kernel, hipFuncAttributeMaxDynamicSharedMemorySize, G1_LDS);
  (void)hipFuncSetAttribute((const void*)g2_kernel, hipFuncAttributeMaxDynamicSharedMemorySize, G2_LDS);
  (void)hipFuncSetAttribute((const void*)g3_kernel, hipFuncAttributeMaxDynamicSharedMemorySize, G3_LDS);

  kpre_kernel<<<320, 256, 0, stream>>>(k, v, shift_k, kvT);
  kconv_kernel<<<320 + 270 + 140 + 27 + 64, 256, CV_LDS, stream>>>(kvT, ssm_k, ssm_D, YT, w_in, w_out, q, winT, cs, woutT, xst0 TSA(0));
  kt_kernel<<<320, 256, 0, stream>>>(YT, Y);
  xln_kernel<<<512, 256, 0, stream>>>(q, Y, x1, a1);
  g1_kernel<<<256, 512, G1_LDS, stream>>>(a1, winT, z TSA(1));
  zn_kernel<<<512, 256, 0, stream>>>(z, zn);
  g2_kernel<<<224, 512, G2_LDS, stream>>>(w_sp, z, zn, b_sp, u TSA(2));
  g3_kernel<<<256, 256, G3_LDS, stream>>>(u, woutT, x1, x2, xst1 TSA(3));
  xln_kernel<<<512, 256, 0, stream>>>(x2, Y, x3, a1);
  g1_kernel<<<256, 512, G1_LDS, stream>>>(a1, winT, z TSA(4));
  zn_kernel<<<512, 256, 0, stream>>>(z, zn);
  g2_kernel<<<224, 512, G2_LDS, stream>>>(w_sp, z, zn, b_sp, u TSA(5));
  g3_kernel<<<256, 256, G3_LDS, stream>>>(u, woutT, x3, out, xst2 TSA(6));
}
```

```cpp
#include <hip/hip_runtime.h>
#include <hip/hip_bf16.h>
#include <stdint.h>

typedef __attribute__((ext_vector_type(8))) short bf16x8;
typedef __attribute__((ext_vector_type(4))) short s16x4;
typedef __attribute__((ext_vector_type(4))) float f32x4;
typedef __attribute__((ext_vector_type(16))) float f32x16;
typedef __attribute__((ext_vector_type(2))) float f32x2;
typedef __attribute__((ext_vector_type(2))) __bf16 bf16x2_t;
typedef __attribute__((ext_vector_type(4))) uint32_t u32x4;
typedef __attribute__((ext_vector_type(2))) uint32_t u32x2;
typedef uint16_t bf16_t;

#define TSP
#define TSA(k)
#define TS(i)

constexpr int S_ = 2048, D_ = 640, F_ = 1728, FH_ = 864, FHP_ = 896;

__device__ __forceinline__ uint32_t pk2(float lo, float hi) {
  f32x2 f = {lo, hi};
  bf16x2_t b = __builtin_convertvector(f, bf16x2_t);
  return __builtin_bit_cast(uint32_t, b);
}
__device__ __forceinline__ float lo16(uint32_t v) { return __uint_as_float(v << 16); }
__device__ __forceinline__ float hi16(uint32_t v) { return __uint_as_float(v & 0xffff0000u); }
__device__ __forceinline__ float rbf(float x) { return lo16(pk2(x, x)); }
__device__ __forceinline__ float bf2f(bf16_t v) { return __uint_as_float(((uint32_t)v) << 16); }

__device__ __forceinline__ f32x2 ln_stats(float sum, float sumsq, float inv_n) {
  float mean = sum * inv_n;
  float var = fmaxf(sumsq * inv_n - mean * mean, 0.f);
  float mb = rbf(mean);
  float vb = rbf(var);
  float sq = rbf(sqrtf(rbf(vb + 1e-5f)));
  f32x2 r = {mb, 1.0f / sq};
  return r;
}

constexpr int TP = 144;
__device__ __forceinline__ void tile_transpose_out(const char* T, bf16_t* __restrict__ dst, size_t dstride, int tid) {
  const int w = tid >> 6, lane = tid & 63, gq = lane >> 4, i = lane & 15;
  typedef __attribute__((address_space(3))) s16x4 lds_s16x4;
#pragma unroll
  for (int h = 0; h < 2; ++h) {
    const int R0 = 8 * gq + 32 * h;
    const char* p = T + (R0 + (i >> 2)) * TP + (16 * w + 4 * (i & 3)) * 2;
    s16x4 a = __builtin_amdgcn_ds_read_tr16_b64_v4i16((lds_s16x4*)p);
    s16x4 b = __builtin_amdgcn_ds_read_tr16_b64_v4i16((lds_s16x4*)(p + 4 * TP));
    bf16x8 o = __builtin_shufflevector(a, b, 0, 1, 2, 3, 4, 5, 6, 7);
    *(bf16x8*)(dst + (size_t)(16 * w + i) * dstride + R0) = o;
  }
}

template <bool PERM>
__device__ __forceinline__ void tile_transpose_out_packed(const char* T, bf16_t* __restrict__ dst, int nks, int col0, int row0, int nh, int tid) {
  const int w = tid >> 6, lane = tid & 63, gq = lane >> 4, i = lane & 15;
  typedef __attribute__((address_space(3))) s16x4 lds_s16x4;
#pragma unroll
  for (int h = 0; h < 2; ++h) {
    const int R0 = 8 * gq + 32 * h;
    const char* p = T + (R0 + (i >> 2)) * TP + (PERM ? 32 * (w >> 1) + 8 * (i & 3) + 4 * (w & 1) : 16 * w + 4 * (i & 3)) * 2;
    s16x4 a = __builtin_amdgcn_ds_read_tr16_b64_v4i16((lds_s16x4*)p);
    s16x4 b = __builtin_amdgcn_ds_read_tr16_b64_v4i16((lds_s16x4*)(p + 4 * TP));
    bf16x8 o = __builtin_shufflevector(a, b, 0, 1, 2, 3, 4, 5, 6, 7);
    if (h < nh) *(bf16x8*)(dst + ((size_t)((col0 >> 4) + w) * nks + (row0 >> 5) + h) * 512 + lane * 8) = o;
  }
}

__global__ __launch_bounds__(256) void kpre_kernel(
    const bf16_t* __restrict__ kin, const bf16_t* __restrict__ vin, const bf16_t* __restrict__ shift_k,
    bf16_t* __restrict__ kvT) {
  __shared__ __attribute__((aligned(16))) char T[64 * TP];
  const int tid = threadIdx.x;
  int j = blockIdx.x;
  const int r = tid >> 2, cq = tid & 3;
  {
    const int t0 = 64 * (j & 31), d0 = 64 * (j >> 5);
    const int t = t0 + r;
    u32x4 kr[2][4], vr[2], skr[2][4];
#pragma unroll
    for (int h = 0; h < 2; ++h) {
      const int d = d0 + 8 * (cq + 4 * h);
#pragma unroll
      for (int i = 0; i < 4; ++i) {
        u32x4 zz = {0u, 0u, 0u, 0u};
        kr[h][i] = (t - 3 + i >= 0) ? *(const u32x4*)(kin + (size_t)(t - 3 + i) * D_ + d) : zz;
      }
      vr[h] = *(const u32x4*)(vin + (size_t)t * D_ + d);
#pragma unroll
      for (int e2 = 0; e2 < 4; ++e2) skr[h][e2] = *(const u32x4*)(shift_k + (size_t)(d + 2 * e2) * 4);
    }
#pragma unroll
    for (int h = 0; h < 2; ++h) {
      u32x4 o;
#pragma unroll
      for (int e2 = 0; e2 < 4; ++e2) {
        const u32x4 sk = skr[h][e2];
        float res[2];
#pragma unroll
        for (int e = 0; e < 2; ++e) {
          float w0 = lo16(sk[2 * e]), w1 = hi16(sk[2 * e]), w2 = lo16(sk[2 * e + 1]), w3 = hi16(sk[2 * e + 1]);
          float k0 = e ? hi16(kr[h][0][e2]) : lo16(kr[h][0][e2]);
          float k1 = e ? hi16(kr[h][1][e2]) : lo16(kr[h][1][e2]);
          float k2 = e ? hi16(kr[h][2][e2]) : lo16(kr[h][2][e2]);
          float k3 = e ? hi16(kr[h][3][e2]) : lo16(kr[h][3][e2]);
          float ks = rbf(k0 * w3);
          ks = rbf(ks + rbf(k1 * w2));
          ks = rbf(ks + rbf(k2 * w1));
          ks = rbf(ks + rbf(k3 * w0));
          float vv = e ? hi16(vr[h][e2]) : lo16(vr[h][e2]);
          res[e] = ks * vv;
        }
        o[e2] = pk2(res[0], res[1]);
      }
      *(u32x4*)(T + r * TP + (cq + 4 * h) * 16) = o;
    }
    __syncthreads();
    tile_transpose_out(T, kvT + (size_t)d0 * S_ + t0, S_, tid);
  }
}

__device__ __forceinline__ int wsp_skew(int a) { return a & 3; }
__device__ __forceinline__ void prep_job(int j, int tid, char* T, const bf16_t* __restrict__ w_in, const bf16_t* __restrict__ w_out,
                                         const bf16_t* __restrict__ w_sp, bf16_t* __restrict__ winP,
                                         bf16_t* __restrict__ woutP, bf16_t* __restrict__ wspP) {
  const int r = tid >> 2, cq = tid & 3;
  if (j < 270) {
    const int d0 = 64 * (j % 10), f0 = 64 * (j / 10);
#pragma unroll
    for (int h = 0; h < 2; ++h)
      *(u32x4*)(T + r * TP + (cq + 4 * h) * 16) = *(const u32x4*)(w_in + (size_t)(d0 + r) * F_ + f0 + 8 * (cq + 4 * h));
    __syncthreads();
    tile_transpose_out_packed<true>(T, winP, 20, f0, d0, 2, tid);
    return;
  }
  j -= 270;
  if (j < 140) {
    const int d0 = 64 * (j % 10), f0 = 64 * (j / 10);
#pragma unroll
    for (int h = 0; h < 2; ++h) {
      u32x4 zz = {0u, 0u, 0u, 0u};
      *(u32x4*)(T + r * TP + (cq + 4 * h) * 16) =
          (f0 + r < FH_) ? *(const u32x4*)(w_out + (size_t)(f0 + r) * D_ + d0 + 8 * (cq + 4 * h)) : zz;
    }
    __syncthreads();
    tile_transpose_out_packed<false>(T, woutP, 27, d0, f0, f0 + 32 < FH_ ? 2 : 1, tid);
    return;
  }
  j -= 140;
  {
    const int wv = 4 * j + (tid >> 6), lane = tid & 63, fr = lane & 15, g = lane >> 4;
    const int a = wv >> 3, k0 = 8 * (wv & 7);
    const bf16_t* src = w_sp + (size_t)(16 * a + fr) * S_ + 32 * k0 + 8 * g;
    u32x4 v[8];
#pragma unroll
    for (int i = 0; i < 8; ++i) v[i] = *(const u32x4*)(src + 32 * i);
#pragma unroll
    for (int i = 0; i < 8; ++i)
      *(u32x4*)(wspP + ((size_t)a * 64 + ((k0 + i + wsp_skew(a)) & 63)) * 512 + lane * 8) = v[i];
  }
}

__global__ __launch_bounds__(256) void kt_kernel(const bf16_t* __restrict__ YT, bf16_t* __restrict__ Y) {
  __shared__ __attribute__((aligned(16))) char T[64 * TP];
  const int tid = threadIdx.x;
  const int j = blockIdx.x;
  const int r = tid >> 2, cq = tid & 3;
  const int t0 = 64 * (j & 31), d0 = 64 * (j >> 5);
#pragma unroll
  for (int h = 0; h < 2; ++h)
    *(u32x4*)(T + r * TP + (cq + 4 * h) * 16) = *(const u32x4*)(YT + (size_t)(d0 + r) * S_ + t0 + 8 * (cq + 4 * h));
  __syncthreads();
  tile_transpose_out(T, Y + (size_t)t0 * D_ + d0, D_, tid);
}

constexpr int CV_HP = 4416;
constexpr int CV_HCH = 4 * CV_HP;
constexpr int CV_UCH = 96 * 80;
constexpr int CV_OFF_H = 0;
constexpr int CV_OFF_U = CV_OFF_H + 2 * CV_HCH;
constexpr int CV_OFF_R = CV_OFF_U + 2 * CV_UCH;
constexpr int CV_OFF_Y = CV_OFF_R + 2 * 8192;
constexpr int CV_LDS = CV_OFF_Y + 2 * 4096;

__global__ __launch_bounds__(256, 2) void kconv_kernel(
    const bf16_t* __restrict__ kvT, const bf16_t* __restrict__ ssm_k, const bf16_t* __restrict__ ssm_D,
    bf16_t* __restrict__ YT, const bf16_t* __restrict__ w_in, const bf16_t* __restrict__ w_out, const bf16_t* __restrict__ w_sp,
    bf16_t* __restrict__ winP, bf16_t* __restrict__ woutP, bf16_t* __restrict__ wspP TSP) {
  extern __shared__ __attribute__((aligned(16))) char smem[];
  const int tid = threadIdx.x;
  if (blockIdx.x >= 320) {
    prep_job(blockIdx.x - 320, tid, smem, w_in, w_out, w_sp, winP, woutP, wspP);
    return;
  }
  TS(0);
  const int c0 = 2 * blockIdx.x;
  u32x4 ur[2];
#pragma unroll
  for (int i = 0; i < 2; ++i) ur[i] = *(const u32x4*)(kvT + (size_t)c0 * S_ + 8 * (tid + 256 * i));
  u32x2 hr[3][3];
#pragma unroll
  for (int rd = 0; rd < 3; ++rd) {
    int task = tid + 256 * rd;
    int ch = task / 262, c = task % 262 - 6;
#pragma unroll
    for (int i = 0; i < 3; ++i) {
      int idx = 8 * c + 4 * i;
      u32x2 z = {0u, 0u};
      hr[rd][i] = (task < 524 && idx >= 0 && idx < 2048) ? *(const u32x2*)(ssm_k + (size_t)(c0 + ch) * S_ + idx) : z;
    }
  }
  for (int i = tid; i < 2 * 160; i += 256) {
    int ch = i / 160, o = i % 160;
    u32x4 z = {0u, 0u, 0u, 0u};
    *(u32x4*)(smem + CV_OFF_U + ch * CV_UCH + o * 16) = z;
  }
  TS(1);
#pragma unroll
  for (int i = 0; i < 2; ++i) {
    const int e = 8 * (tid + 256 * i), ch = e >> 11, s = e & 2047;
    *(u32x4*)(smem + CV_OFF_U + ch * CV_UCH + ((s >> 5) + 32) * 80 + (s & 31) * 2) = ur[i];
  }
#pragma unroll
  for (int rd = 0; rd < 3; ++rd) {
    int task = tid + 256 * rd;
    if (task < 524) {
      int ch = task / 262, c = task % 262 - 6;
      uint32_t w[6] = {hr[rd][0][0], hr[rd][0][1], hr[rd][1][0], hr[rd][1][1], hr[rd][2][0], hr[rd][2][1]};
#pragma unroll
      for (int p = 0; p < 4; ++p) {
#pragma unroll
        for (int gi = 0; gi < 2; ++gi) {
          const int ao = ((1 - p) & 3) + 4 * gi;
          int a = 8 * c + ao;
          int qx = 2045 - p - a;
          auto el = [&](int e) -> uint32_t { return (e & 1) ? (w[e >> 1] >> 16) : (w[e >> 1] & 0xffff); };
          u32x2 o = {el(ao + 3) | (el(ao + 2) << 16), el(ao + 1) | (el(ao) << 16)};
          if (qx >= 0 && qx < 2088) *(u32x2*)(smem + CV_OFF_H + ch * CV_HCH + p * CV_HP + qx * 2) = o;
        }
      }
    }
  }
  TS(2);
  __syncthreads();
  TS(3);
  const int wid = tid >> 6, lane = tid & 63;
  const int ch = wid & 1, kb = wid >> 1;
  const int m = lane & 31, hh = lane >> 5;
  {
    const int p = (4 - (m & 3)) & 3;
    const char* abase = smem + CV_OFF_H + ch * CV_HCH + p * CV_HP + (2048 - m + 8 * hh - p + 16 * kb) * 2;
    const char* bbase = smem + CV_OFF_U + ch * CV_UCH + (m + 32) * 80 + 32 * kb + 16 * hh;
    f32x16 acc0 = {}, acc1 = {};
    u32x2 ra[3][4];
    bf16x8 rbB[3][2];
    auto ld = [&](int sl, int dl) {
      typedef const volatile __attribute__((address_space(3))) u32x2* lds_v2;
      ra[sl][0] = *(lds_v2)(abase - 64 * dl);
      ra[sl][1] = *(lds_v2)(abase - 64 * dl + 8);
      ra[sl][2] = *(lds_v2)(abase - 64 * (dl + 32));
      ra[sl][3] = *(lds_v2)(abase - 64 * (dl + 32) + 8);
      rbB[sl][0] = *(const bf16x8*)(bbase - 80 * dl);
      rbB[sl][1] = *(const bf16x8*)(bbase + 80 * (32 - dl));
    };
    ld(0, 0);
    ld(1, 1);
#pragma unroll
    for (int dl = 0; dl < 32; ++dl) {
      if (dl + 2 < 32) ld((dl + 2) % 3, dl + 2);
      __builtin_amdgcn_sched_barrier(0);
      const int sl = dl % 3;
      u32x4 A0u = {ra[sl][0][0], ra[sl][0][1], ra[sl][1][0], ra[sl][1][1]};
      u32x4 A1u = {ra[sl][2][0], ra[sl][2][1], ra[sl][3][0], ra[sl][3][1]};
      bf16x8 A0 = __builtin_bit_cast(bf16x8, A0u), A1 = __builtin_bit_cast(bf16x8, A1u);
      acc0 = __builtin_amdgcn_mfma_f32_32x32x16_bf16(A0, rbB[sl][0], acc0, 0, 0, 0);
      acc1 = __builtin_amdgcn_mfma_f32_32x32x16_bf16(A0, rbB[sl][1], acc1, 0, 0, 0);
      acc1 = __builtin_amdgcn_mfma_f32_32x32x16_bf16(A1, rbB[sl][0], acc1, 0, 0, 0);
      __builtin_amdgcn_sched_barrier(0);
    }
    TS(4);
    float* red = (float*)(smem + CV_OFF_R + ch * 8192);
    if (kb == 1) {
#pragma unroll
      for (int r = 0; r < 16; ++r) { red[r * 64 + lane] = acc0[r]; red[(16 + r) * 64 + lane] = acc1[r]; }
    }
    __syncthreads();
    if (kb == 0) {
      const float Dv = bf2f(ssm_D[c0 + ch]);
      bf16_t* yb = (bf16_t*)(smem + CV_OFF_Y + ch * 4096);
#pragma unroll
      for (int half = 0; half < 2; ++half) {
        const int a = m + 32 * half;
#pragma unroll
        for (int rq = 0; rq < 4; ++rq) {
          const int b0 = 8 * rq + 4 * hh;
          u32x2 kvw = *(const u32x2*)(smem + CV_OFF_U + ch * CV_UCH + (a + 32) * 80 + b0 * 2);
          float kvv[4] = {lo16(kvw[0]), hi16(kvw[0]), lo16(kvw[1]), hi16(kvw[1])};
          float o[4];
#pragma unroll
          for (int i = 0; i < 4; ++i) {
            const int r = 4 * rq + i;
            float y = (half ? acc1[r] : acc0[r]) + red[(16 * half + r) * 64 + lane];
            o[i] = rbf(y) + rbf(kvv[i] * Dv);
          }
          u32x2 ow = {pk2(o[0], o[1]), pk2(o[2], o[3])};
          *(u32x2*)(yb + 32 * a + b0) = ow;
        }
      }
    }
    __syncthreads();
    TS(5);
#pragma unroll
    for (int i = 0; i < 2; ++i) {
      const int e = tid + 256 * i;
      *(u32x4*)(YT + (size_t)c0 * S_ + 8 * e) = *(const u32x4*)(smem + CV_OFF_Y + 16 * e);
    }
    TS(6);
  }
}

template <int I> struct IC { static constexpr int value = I; };
template <int N, int I = 0, typename F>
__device__ __forceinline__ void static_for(F&& f) {
  if constexpr (I < N) { f(IC<I>{}); static_for<N, I + 1>(f); }
}

__device__ __forceinline__ float dpp_add(float v, int ctrl_sel) {
  int x = __float_as_int(v), y;
  switch (ctrl_sel) {
    case 0: y = __builtin_amdgcn_update_dpp(0, x, 0xB1, 0xf, 0xf, false); break;
    case 1: y = __builtin_amdgcn_update_dpp(0, x, 0x4E, 0xf, 0xf, false); break;
    case 2: y = __builtin_amdgcn_update_dpp(0, x, 0x141, 0xf, 0xf, false); break;
    default: y = __builtin_amdgcn_update_dpp(0, x, 0x140, 0xf, 0xf, false); break;
  }
  return v + __int_as_float(y);
}
__device__ __forceinline__ float wave_sum(float v) {
  v = dpp_add(v, 0); v = dpp_add(v, 1); v = dpp_add(v, 2); v = dpp_add(v, 3);
  int x = __float_as_int(v);
  return (__int_as_float(__builtin_amdgcn_readlane(x, 0)) + __int_as_float(__builtin_amdgcn_readlane(x, 16))) +
         (__int_as_float(__builtin_amdgcn_readlane(x, 32)) + __int_as_float(__builtin_amdgcn_readlane(x, 48)));
}

__global__ __launch_bounds__(256) void xln_kernel(const bf16_t* __restrict__ x0, const bf16_t* __restrict__ Yb,
                                                  bf16_t* __restrict__ x1, bf16_t* __restrict__ a1) {
  const int lane = threadIdx.x & 63;
  const int row = 4 * blockIdx.x + (threadIdx.x >> 6);
  const uint32_t* px = (const uint32_t*)(x0 + (size_t)row * D_);
  const uint32_t* py = (const uint32_t*)(Yb + (size_t)row * D_);
  uint32_t xv[5], yv[5];
#pragma unroll
  for (int i = 0; i < 5; ++i) { xv[i] = px[lane + 64 * i]; yv[i] = py[lane + 64 * i]; }
  float s0 = 0.f, s1 = 0.f;
#pragma unroll
  for (int i = 0; i < 5; ++i) { float a = lo16(xv[i]), b = hi16(xv[i]); s0 += a + b; s1 += a * a + b * b; }
  const f32x2 st = ln_stats(wave_sum(s0), wave_sum(s1), 1.0f / D_);
  float t0 = 0.f, t1 = 0.f;
  uint32_t* p1 = (uint32_t*)(x1 + (size_t)row * D_);
#pragma unroll
  for (int i = 0; i < 5; ++i) {
    float a = lo16(xv[i]), b = hi16(xv[i]);
    float da = rbf(rbf(a - st[0]) * st[1]), db = rbf(rbf(b - st[0]) * st[1]);
    float ta = rbf(da * lo16(yv[i])), tb = rbf(db * hi16(yv[i]));
    uint32_t w = pk2(a + ta, b + tb);
    xv[i] = w;
    p1[lane + 64 * i] = w;
    float xa = lo16(w), xb = hi16(w);
    t0 += xa + xb; t1 += xa * xa + xb * xb;
  }
  const f32x2 su = ln_stats(wave_sum(t0), wave_sum(t1), 1.0f / D_);
  uint32_t* pa = (uint32_t*)(a1 + (size_t)row * D_);
#pragma unroll
  for (int i = 0; i < 5; ++i)
    pa[lane + 64 * i] = pk2(rbf(lo16(xv[i]) - su[0]) * su[1], rbf(hi16(xv[i]) - su[0]) * su[1]);
}

__device__ __forceinline__ void glds16(const void* gsrc, uint32_t lds_dst) {
  uint32_t keep;
  asm volatile("s_mov_b32 %0, m0\n\ts_mov_b32 m0, %2\n\ts_nop 0\n\tglobal_load_lds_dwordx4 %1, off\n\ts_mov_b32 m0, %0"
               : "=&s"(keep) : "v"(gsrc), "s"(lds_dst) : "memory");
}
__device__ __forceinline__ uint32_t lds_addr(const void* p) {
  return (uint32_t)(uintptr_t)(const __attribute__((address_space(3))) char*)p;
}
__device__ __forceinline__ void gload16(bf16x8& dst, const void* p) {
  asm volatile("global_load_dwordx4 %0, %1, off" : "=v"(dst) : "v"(p) : "memory");
}
template <int N>
__device__ __forceinline__ void wait_vm2(bf16x8& a, bf16x8& b) {
  asm volatile("s_waitcnt vmcnt(%2)" : "+v"(a), "+v"(b) : "n"(N) : "memory");
}
#define WAIT_VM(n) asm volatile("s_waitcnt vmcnt(" #n ")" ::: "memory")
#define WAIT_LGKM0() asm volatile("s_waitcnt lgkmcnt(0)" ::: "memory")
#define RAW_BAR() __builtin_amdgcn_s_barrier()

constexpr int G1_LDS = 81920;

__device__ __forceinline__ f32x2 gelu2(f32x2 x) {
  const float c1 = -2.f * 0.7978845608028654f * 1.4426950408889634f, c2 = c1 * 0.044715f;
  f32x2 t = x * x;
  f32x2 a = x * (t * c2 + c1);
  f32x2 d = {1.f + __builtin_amdgcn_exp2f(a[0]), 1.f + __builtin_amdgcn_exp2f(a[1])};
  f32x2 r = {__builtin_amdgcn_rcpf(d[0]), __builtin_amdgcn_rcpf(d[1])};
  return x * r;
}
__device__ __forceinline__ f32x2 rbf2(f32x2 v) {
  uint32_t w = pk2(v[0], v[1]);
  f32x2 r = {lo16(w), hi16(w)};
  return r;
}

__global__ __launch_bounds__(512, 1) void g1_kernel(
    const bf16_t* __restrict__ a1, const bf16_t* __restrict__ winT, bf16_t* __restrict__ z TSP) {
  extern __shared__ __attribute__((aligned(16))) char smem[];
  const int tid = threadIdx.x;
  TS(0);
  const int xcd = blockIdx.x & 7, idx = blockIdx.x >> 3;
  const int mt = 8 * (xcd >> 1) + (idx & 7);
  const int nt = 4 * (xcd & 1) + (idx >> 3);
  const int ps = nt < 6 ? 7 * nt : 42 + 6 * (nt - 6);
  const int np = nt < 6 ? 7 : 6;
  const int m0 = 64 * mt;
  const int wid = __builtin_amdgcn_readfirstlane(tid >> 6), lane = tid & 63;
  {
    const int lr = lane >> 3, lc = (lane & 7) ^ lr;
    const bf16_t* ap = a1 + (size_t)(m0 + 8 * wid + lr) * D_ + 8 * lc;
    const uint32_t lbase = lds_addr(smem) + wid * 1024;
#pragma unroll
    for (int kt = 0; kt < 10; ++kt) glds16(ap + 64 * kt, lbase + kt * 8192);
  }
  if (wid >= np) {
    WAIT_VM(0);
    RAW_BAR();
    return;
  }
  const int fr = lane & 15, g = lane >> 4;
  constexpr int P = 10;
  const bf16_t* wp[2];
#pragma unroll
  for (int i = 0; i < 2; ++i) wp[i] = winT + (size_t)(2 * (ps + wid) + i) * (20 * 512) + lane * 8;
  bf16x8 wv[P][2];
#pragma unroll
  for (int s = 0; s < P; ++s)
#pragma unroll
    for (int i = 0; i < 2; ++i) gload16(wv[s][i], wp[i] + 512 * s);
  TS(1);
  WAIT_VM(20);
  RAW_BAR();
  TS(2);
  f32x4 acc[2][4] = {};
  {
    int xoff[4];
#pragma unroll
    for (int j = 0; j < 4; ++j) xoff[j] = (16 * j + fr) * 128;
    auto step = [&](auto KS) {
      constexpr int ks = decltype(KS)::value;
      bf16x8 X[4];
#pragma unroll
      for (int j = 0; j < 4; ++j)
        X[j] = *(const bf16x8*)(smem + (ks >> 1) * 8192 + xoff[j] + (((4 * (ks & 1) + g) ^ (fr & 7)) << 4));
      constexpr int newest = (ks + P - 1 < 19) ? ks + P - 1 : 19;
      wait_vm2<2 * (newest - ks)>(wv[ks % P][0], wv[ks % P][1]);
#pragma unroll
      for (int j = 0; j < 4; ++j) {
        acc[0][j] = __builtin_amdgcn_mfma_f32_16x16x32_bf16(wv[ks % P][0], X[j], acc[0][j], 0, 0, 0);
        acc[1][j] = __builtin_amdgcn_mfma_f32_16x16x32_bf16(wv[ks % P][1], X[j], acc[1][j], 0, 0, 0);
      }
      if constexpr (ks + P < 20) {
        gload16(wv[ks % P][0], wp[0] + 512 * (ks + P));
        gload16(wv[ks % P][1], wp[1] + 512 * (ks + P));
      }
    };
    static_for<20>(step);
  }
  TS(3);
  bf16_t* zp = z + ((size_t)(4 * mt) * 54 + ps + wid) * 512 + lane * 8;
#pragma unroll
  for (int j = 0; j < 4; ++j) {
    u32x4 o;
#pragma unroll
    for (int i = 0; i < 2; ++i) {
      f32x2 lo = {acc[i][j][0], acc[i][j][1]}, hi = {acc[i][j][2], acc[i][j][3]};
      lo = gelu2(rbf2(lo)); hi = gelu2(rbf2(hi));
      o[2 * i] = pk2(lo[0], lo[1]); o[2 * i + 1] = pk2(hi[0], hi[1]);
    }
    *(u32x4*)(zp + (size_t)j * (54 * 512)) = o;
  }
  TS(4);
}

constexpr int ZT_P = 1744;
__global__ __launch_bounds__(256) void znt_kernel(const bf16_t* __restrict__ zP, bf16_t* __restrict__ znP) {
  __shared__ __attribute__((aligned(16))) char T[16 * ZT_P];
  __shared__ float R[4][16][2];
  const int tid = threadIdx.x, w = tid >> 6, lane = tid & 63, fr = lane & 15, g = lane >> 4;
  const int a = blockIdx.x;
  const bf16_t* src = zP + ((size_t)a * 54 + 27) * 512 + lane * 8;
  u32x4 v[7];
#pragma unroll
  for (int i = 0; i < 7; ++i) {
    u32x4 zz = {0u, 0u, 0u, 0u};
    v[i] = (w + 4 * i < 27) ? *(const u32x4*)(src + (size_t)(w + 4 * i) * 512) : zz;
  }
  float s0 = 0.f, s1 = 0.f;
#pragma unroll
  for (int i = 0; i < 7; ++i)
#pragma unroll
    for (int e = 0; e < 4; ++e) { float x = lo16(v[i][e]), y = hi16(v[i][e]); s0 += x + y; s1 += x * x + y * y; }
  s0 += __shfl_xor(s0, 16); s0 += __shfl_xor(s0, 32);
  s1 += __shfl_xor(s1, 16); s1 += __shfl_xor(s1, 32);
  if (g == 0) { R[w][fr][0] = s0; R[w][fr][1] = s1; }
  __syncthreads();
  const f32x2 st = ln_stats((R[0][fr][0] + R[1][fr][0]) + (R[2][fr][0] + R[3][fr][0]),
                            (R[0][fr][1] + R[1][fr][1]) + (R[2][fr][1] + R[3][fr][1]), 1.0f / FH_);
#pragma unroll
  for (int i = 0; i < 7; ++i) {
    if (w + 4 * i < 27) {
      u32x4 o;
#pragma unroll
      for (int e = 0; e < 4; ++e) o[e] = pk2(rbf(lo16(v[i][e]) - st[0]) * st[1], rbf(hi16(v[i][e]) - st[0]) * st[1]);
      *(u32x4*)(T + fr * ZT_P + (32 * (w + 4 * i) + 8 * g) * 2) = o;
    }
  }
  __syncthreads();
  typedef __attribute__((address_space(3))) s16x4 lds_s16x4;
  const int h2 = g >> 1, gl = g & 1;
#pragma unroll
  for (int i = 0; i < 7; ++i) {
    const int p = w + 4 * i;
    if (p < 27) {
      const char* q = T + (8 * gl + (fr >> 2)) * ZT_P + (32 * p + 8 * (fr & 3) + 4 * h2) * 2;
      s16x4 x = __builtin_amdgcn_ds_read_tr16_b64_v4i16((lds_s16x4*)q);
      s16x4 y = __builtin_amdgcn_ds_read_tr16_b64_v4i16((lds_s16x4*)(q + 4 * ZT_P));
      bf16x8 o = __builtin_shufflevector(x, y, 0, 1, 2, 3, 4, 5, 6, 7);
      *(bf16x8*)(znP + ((size_t)(2 * p + h2) * 64 + (a >> 1)) * 512 + (16 * (2 * (a & 1) + gl) + fr) * 8) = o;
    }
  }
}

constexpr int G2_LDS = 4 * 32768;
constexpr int g2_wait(int kind, int idx) {
  int n = 0, endD[8] = {}, endW[32] = {};
  n += 4; endD[0] = n;
  for (int s = 0; s < 4; ++s) { n += 2; endW[s] = n; }
  n += 4; endD[1] = n;
  for (int s = 4; s < 8; ++s) { n += 2; endW[s] = n; }
  n += 4; endD[2] = n;
  for (int c = 0; c < 8; ++c) {
    if (kind == 0 && idx == c) return n - endD[c];
    if (c + 3 < 8) { n += 4; endD[c + 3] = n; }
    for (int q = 0; q < 4; ++q) {
      const int s = 4 * c + q;
      if (kind == 1 && idx == s) return n - endW[s];
      if (s + 8 < 32) { n += 2; endW[s + 8] = n; }
    }
  }
  return 0;
}

template <int N>
__device__ __forceinline__ void wait_vm() { asm volatile("s_waitcnt vmcnt(%0)" ::"n"(N) : "memory"); }

__global__ __launch_bounds__(512, 1) void g2_kernel(
    const bf16_t* __restrict__ wspP, const bf16_t* __restrict__ zP, const bf16_t* __restrict__ znP,
    const bf16_t* __restrict__ bsp, bf16_t* __restrict__ uP TSP) {
  extern __shared__ __attribute__((aligned(16))) char smem[];
  const int tid = threadIdx.x;
  TS(0);
  const int xcd = blockIdx.x & 7, idx = blockIdx.x >> 3;
  const int mt = 4 * (xcd >> 1) + (idx & 3);
  const int nt = 7 * (xcd & 1) + (idx >> 2);
  const int wid = __builtin_amdgcn_readfirstlane(tid >> 6), lane = tid & 63;
  const int tg = wid & 3, h = wid >> 2;
  const int rot = (2 * idx + 9 * xcd) & 63;
  const bf16_t* zsrc[4];
#pragma unroll
  for (int j = 0; j < 4; ++j) { int ff = 4 * nt + j; ff = ff < 54 ? ff : ff - 2; zsrc[j] = znP + (size_t)ff * (64 * 512) + lane * 8; }
  const uint32_t lbase = lds_addr(smem) + wid * 4096;
  auto issueD = [&](int c) {
    const int ke = (8 * c + wid + rot) & 63;
#pragma unroll
    for (int j = 0; j < 4; ++j) glds16(zsrc[j] + 512 * ke, lbase + (c & 3) * 32768 + j * 1024);
  };
  const bf16_t* wsrc[2];
  int wrot[2];
#pragma unroll
  for (int i = 0; i < 2; ++i) {
    const int a = 8 * mt + 2 * tg + i;
    wsrc[i] = wspP + (size_t)a * (64 * 512) + lane * 8;
    wrot[i] = h + rot + wsp_skew(a);
  }
  bf16x8 wv[8][2];
  auto issueW = [&](int s) {
#pragma unroll
    for (int i = 0; i < 2; ++i) gload16(wv[s & 7][i], wsrc[i] + 512 * ((2 * s + wrot[i]) & 63));
  };
  issueD(0);
#pragma unroll
  for (int s = 0; s < 4; ++s) issueW(s);
  issueD(1);
#pragma unroll
  for (int s = 4; s < 8; ++s) issueW(s);
  issueD(2);
  TS(1);
  f32x4 acc[2][4] = {};
  const char* lrd = smem + h * 4096 + lane * 16;
  auto stage = [&](auto C) {
    constexpr int c = decltype(C)::value;
    wait_vm<g2_wait(0, c)>();
    RAW_BAR();
    if constexpr (c + 3 < 8) issueD(c + 3);
    auto step = [&](auto Q) {
      constexpr int q = decltype(Q)::value, s = 4 * c + q;
      bf16x8 Z[4];
#pragma unroll
      for (int j = 0; j < 4; ++j) Z[j] = *(const bf16x8*)(lrd + (c & 3) * 32768 + q * 8192 + j * 1024);
      wait_vm2<g2_wait(1, s)>(wv[s & 7][0], wv[s & 7][1]);
#pragma unroll
      for (int j = 0; j < 4; ++j) {
        acc[0][j] = __builtin_amdgcn_mfma_f32_16x16x32_bf16(Z[j], wv[s & 7][0], acc[0][j], 0, 0, 0);
        acc[1][j] = __builtin_amdgcn_mfma_f32_16x16x32_bf16(Z[j], wv[s & 7][1], acc[1][j], 0, 0, 0);
      }
      if constexpr (s + 8 < 32) issueW(s + 8);
    };
    static_for<4>(step);
  };
  static_for<8>(stage);
  TS(3);
  __syncthreads();
  {
    f32x4* X = (f32x4*)smem + wid * 256 + lane;
#pragma unroll
    for (int i = 0; i < 2; ++i)
#pragma unroll
      for (int jj = 0; jj < 2; ++jj) X[(2 * i + jj) * 64] = h ? acc[i][jj] : acc[i][2 + jj];
  }
  __syncthreads();
  const int pair = 2 * nt + h;
  if (pair < 27) {
    const f32x4* X = (const f32x4*)smem + (wid ^ 4) * 256 + lane;
    const int fr = lane & 15;
#pragma unroll
    for (int i = 0; i < 2; ++i) {
      const int a = 8 * mt + 2 * tg + i;
      const float bb = bf2f(bsp[16 * a + fr]);
      const u32x4 z1 = *(const u32x4*)(zP + ((size_t)a * 54 + pair) * 512 + lane * 8);
      u32x4 o;
#pragma unroll
      for (int jj = 0; jj < 2; ++jj) {
        f32x4 v = (h ? acc[i][2 + jj] : acc[i][jj]) + X[(2 * i + jj) * 64];
        float g0 = rbf(rbf(v[0]) + bb), g1 = rbf(rbf(v[1]) + bb), g2 = rbf(rbf(v[2]) + bb), g3 = rbf(rbf(v[3]) + bb);
        o[2 * jj] = pk2(lo16(z1[2 * jj]) * g0, hi16(z1[2 * jj]) * g1);
        o[2 * jj + 1] = pk2(lo16(z1[2 * jj + 1]) * g2, hi16(z1[2 * jj + 1]) * g3);
      }
      *(u32x4*)(uP + ((size_t)a * 27 + pair) * 512 + lane * 8) = o;
    }
  }
  TS(4);
}

constexpr int G3_LDS = 28 * 4096;
constexpr int G3_P = 10;
constexpr int g3_nd(int c) { return c < 3 ? 4 : 2; }
constexpr int g3_wait(int kind, int idx) {
  int n = 0, endD[4] = {}, endW[27] = {};
  n += g3_nd(0); endD[0] = n;
  for (int s = 0; s < 5; ++s) { n += 1; endW[s] = n; }
  n += g3_nd(1); endD[1] = n;
  for (int s = 5; s < G3_P; ++s) { n += 1; endW[s] = n; }
  n += g3_nd(2); endD[2] = n;
  n += g3_nd(3); endD[3] = n;
  for (int s = 0; s < 27; ++s) {
    if (kind == 0 && 8 * idx == s) return n - endD[idx];
    if (kind == 1 && idx == s) return n - endW[s];
    if (s + G3_P < 27) { n += 1; endW[s + G3_P] = n; }
  }
  return 0;
}

__global__ __launch_bounds__(512, 1) void g3_kernel(
    const bf16_t* __restrict__ uP, const bf16_t* __restrict__ woutP, const bf16_t* __restrict__ x1,
    bf16_t* __restrict__ xout TSP) {
  extern __shared__ __attribute__((aligned(16))) char smem[];
  const int tid = threadIdx.x;
  TS(0);
  const int xcd = blockIdx.x & 7, idx = blockIdx.x >> 3;
  const int mt = 8 * (xcd >> 1) + (idx & 7);
  const int nt = 4 * (xcd & 1) + (idx >> 3);
  const int wid = __builtin_amdgcn_readfirstlane(tid >> 6), lane = tid & 63;
  const bf16_t* usrc = uP + (size_t)(4 * mt) * (27 * 512) + lane * 8;
  const uint32_t lb = lds_addr(smem);
  auto issueD = [&](int c) {
    if (c < 3) {
      const int ks = 8 * c + wid;
#pragma unroll
      for (int tf = 0; tf < 4; ++tf) glds16(usrc + (size_t)(tf * 27 + ks) * 512, lb + (4 * ks + tf) * 1024);
    } else {
      const int ks = 24 + (wid % 3), t2 = 2 * ((wid / 3) & 1);
#pragma unroll
      for (int tf = 0; tf < 2; ++tf) glds16(usrc + (size_t)((t2 + tf) * 27 + ks) * 512, lb + (4 * ks + t2 + tf) * 1024);
    }
  };
  if (wid >= 5) {
    issueD(0); issueD(1); issueD(2); issueD(3);
    WAIT_VM(10); RAW_BAR();
    WAIT_VM(6); RAW_BAR();
    WAIT_VM(2); RAW_BAR();
    WAIT_VM(0); RAW_BAR();
    return;
  }
  const bf16_t* wsrc = woutP + (size_t)(5 * nt + wid) * (27 * 512) + lane * 8;
  bf16x8 wv[G3_P];
  issueD(0);
#pragma unroll
  for (int s = 0; s < 5; ++s) gload16(wv[s], wsrc + 512 * s);
  issueD(1);
#pragma unroll
  for (int s = 5; s < G3_P; ++s) gload16(wv[s], wsrc + 512 * s);
  issueD(2);
  issueD(3);
  const int fr = lane & 15, g = lane >> 4;
  u32x2 xr[4];
  TS(1);
  f32x4 acc[4] = {};
  auto step = [&](auto S) {
    constexpr int s = decltype(S)::value;
    if constexpr ((s & 7) == 0) { wait_vm<g3_wait(0, s >> 3)>(); RAW_BAR(); }
    bf16x8 U[4];
#pragma unroll
    for (int tf = 0; tf < 4; ++tf) U[tf] = *(const bf16x8*)(smem + (4 * s + tf) * 1024 + lane * 16);
    asm volatile("s_waitcnt vmcnt(%1)" : "+v"(wv[s % G3_P]) : "n"(g3_wait(1, s)) : "memory");
#pragma unroll
    for (int tf = 0; tf < 4; ++tf) acc[tf] = __builtin_amdgcn_mfma_f32_16x16x32_bf16(wv[s % G3_P], U[tf], acc[tf], 0, 0, 0);
    if constexpr (s + G3_P < 27) gload16(wv[s % G3_P], wsrc + 512 * (s + G3_P));
  };
  static_for<27>(step);
  TS(2);
  const int d = 80 * nt + 16 * wid + 4 * g;
#pragma unroll
  for (int tf = 0; tf < 4; ++tf) xr[tf] = *(const u32x2*)(x1 + (size_t)(64 * mt + 16 * tf + fr) * D_ + d);
#pragma unroll
  for (int tf = 0; tf < 4; ++tf) {
    u32x2 w = {pk2(lo16(xr[tf][0]) + rbf(acc[tf][0]), hi16(xr[tf][0]) + rbf(acc[tf][1])),
               pk2(lo16(xr[tf][1]) + rbf(acc[tf][2]), hi16(xr[tf][1]) + rbf(acc[tf][3]))};
    *(u32x2*)(xout + (size_t)(64 * mt + 16 * tf + fr) * D_ + d) = w;
  }
  TS(3);
}

extern "C" void kernel_launch(void* const* d_in, const int* in_sizes, int n_in,
                              void* d_out, int out_size, void* d_ws, size_t ws_size,
                              hipStream_t stream) {
  const bf16_t* q = (const bf16_t*)d_in[0];
  const bf16_t* k = (const bf16_t*)d_in[1];
  const bf16_t* v = (const bf16_t*)d_in[2];
  const bf16_t* shift_k = (const bf16_t*)d_in[3];
  const bf16_t* ssm_k = (const bf16_t*)d_in[4];
  const bf16_t* ssm_D = (const bf16_t*)d_in[5];
  const bf16_t* w_in = (const bf16_t*)d_in[7];
  const bf16_t* w_sp = (const bf16_t*)d_in[8];
  const bf16_t* b_sp = (const bf16_t*)d_in[9];
  const bf16_t* w_out = (const bf16_t*)d_in[10];
  bf16_t* out = (bf16_t*)d_out;

  char* ws = (char*)d_ws;
  size_t off = 0;
  auto alloc = [&](size_t bytes) { char* p = ws + off; off += (bytes + 255) & ~(size_t)255; return p; };
  bf16_t* winP = (bf16_t*)alloc((size_t)F_ * D_ * 2);
  bf16_t* woutP = (bf16_t*)alloc((size_t)D_ * FH_ * 2);
  bf16_t* wspP = (bf16_t*)alloc((size_t)S_ * S_ * 2);
  bf16_t* Y = (bf16_t*)alloc((size_t)S_ * D_ * 2);
  bf16_t* kvT = (bf16_t*)alloc((size_t)S_ * D_ * 2);
  bf16_t* YT = (bf16_t*)alloc((size_t)S_ * D_ * 2);
  bf16_t* x1 = (bf16_t*)alloc((size_t)S_ * D_ * 2);
  bf16_t* x2 = (bf16_t*)alloc((size_t)S_ * D_ * 2);
  bf16_t* x3 = (bf16_t*)alloc((size_t)S_ * D_ * 2);
  bf16_t* zP = (bf16_t*)alloc((size_t)S_ * F_ * 2);
  bf16_t* uP = (bf16_t*)alloc((size_t)S_ * FH_ * 2);
  bf16_t* znP = (bf16_t*)alloc((size_t)S_ * FH_ * 2);
  bf16_t* a1 = (bf16_t*)alloc((size_t)S_ * D_ * 2);

  (void)hipFuncSetAttribute((const void*)kconv_kernel, hipFuncAttributeMaxDynamicSharedMemorySize, CV_LDS);
  (void)hipFuncSetAttribute((const void*)g1_kernel, hipFuncAttributeMaxDynamicSharedMemorySize, G1_LDS);
  (void)hipFuncSetAttribute((const void*)g2_kernel, hipFuncAttributeMaxDynamicSharedMemorySize, G2_LDS);
  (void)hipFuncSetAttribute((const void*)g3_kernel, hipFuncAttributeMaxDynamicSharedMemorySize, G3_LDS);

  kpre_kernel<<<320, 256, 0, stream>>>(k, v, shift_k, kvT);
  kconv_kernel<<<320 + 270 + 140 + 256, 256, CV_LDS, stream>>>(kvT, ssm_k, ssm_D, YT, w_in, w_out, w_sp, winP, woutP, wspP TSA(0));
  kt_kernel<<<320, 256, 0, stream>>>(YT, Y);
  xln_kernel<<<512, 256, 0, stream>>>(q, Y, x1, a1);
  g1_kernel<<<256, 512, G1_LDS, stream>>>(a1, winP, zP TSA(1));
  znt_kernel<<<128, 256, 0, stream>>>(zP, znP);
  g2_kernel<<<224, 512, G2_LDS, stream>>>(wspP, zP, znP, b_sp, uP TSA(2));
  g3_kernel<<<256, 512, G3_LDS, stream>>>(uP, woutP, x1, x2 TSA(3));
  xln_kernel<<<512, 256, 0, stream>>>(x2, Y, x3, a1);
  g1_kernel<<<256, 512, G1_LDS, stream>>>(a1, winP, zP TSA(4));
  znt_kernel<<<128, 256, 0, stream>>>(zP, znP);
  g2_kernel<<<224, 512, G2_LDS, stream>>>(wspP, zP, znP, b_sp, uP TSA(5));
  g3_kernel<<<256, 512, G3_LDS, stream>>>(uP, woutP, x3, out TSA(6));
}
```

```cpp
#include <hip/hip_runtime.h>
#include <hip/hip_bf16.h>
#include <stdint.h>

typedef __attribute__((ext_vector_type(8))) short bf16x8;
typedef __attribute__((ext_vector_type(4))) short s16x4;
typedef __attribute__((ext_vector_type(4))) float f32x4;
typedef __attribute__((ext_vector_type(16))) float f32x16;
typedef __attribute__((ext_vector_type(2))) float f32x2;
typedef __attribute__((ext_vector_type(2))) __bf16 bf16x2_t;
typedef __attribute__((ext_vector_type(4))) uint32_t u32x4;
typedef __attribute__((ext_vector_type(2))) uint32_t u32x2;
typedef uint16_t bf16_t;

#define TSP
#define TSA(k)
#define TS(i)

constexpr int S_ = 2048, D_ = 640, F_ = 1728, FH_ = 864, FHP_ = 896;

__device__ __forceinline__ uint32_t pk2(float lo, float hi) {
  f32x2 f = {lo, hi};
  bf16x2_t b = __builtin_convertvector(f, bf16x2_t);
  return __builtin_bit_cast(uint32_t, b);
}
__device__ __forceinline__ float lo16(uint32_t v) { return __uint_as_float(v << 16); }
__device__ __forceinline__ float hi16(uint32_t v) { return __uint_as_float(v & 0xffff0000u); }
__device__ __forceinline__ float rbf(float x) { return lo16(pk2(x, x)); }
__device__ __forceinline__ float bf2f(bf16_t v) { return __uint_as_float(((uint32_t)v) << 16); }

__device__ __forceinline__ f32x2 ln_stats(float sum, float sumsq, float inv_n) {
  float mean = sum * inv_n;
  float var = fmaxf(sumsq * inv_n - mean * mean, 0.f);
  float mb = rbf(mean);
  float vb = rbf(var);
  float sq = rbf(sqrtf(rbf(vb + 1e-5f)));
  f32x2 r = {mb, 1.0f / sq};
  return r;
}

constexpr int TP = 144;
__device__ __forceinline__ void tile_transpose_out(const char* T, bf16_t* __restrict__ dst, size_t dstride, int tid) {
  const int w = tid >> 6, lane = tid & 63, gq = lane >> 4, i = lane & 15;
  typedef __attribute__((address_space(3))) s16x4 lds_s16x4;
#pragma unroll
  for (int h = 0; h < 2; ++h) {
    const int R0 = 8 * gq + 32 * h;
    const char* p = T + (R0 + (i >> 2)) * TP + (16 * w + 4 * (i & 3)) * 2;
    s16x4 a = __builtin_amdgcn_ds_read_tr16_b64_v4i16((lds_s16x4*)p);
    s16x4 b = __builtin_amdgcn_ds_read_tr16_b64_v4i16((lds_s16x4*)(p + 4 * TP));
    bf16x8 o = __builtin_shufflevector(a, b, 0, 1, 2, 3, 4, 5, 6, 7);
    *(bf16x8*)(dst + (size_t)(16 * w + i) * dstride + R0) = o;
  }
}

template <bool PERM>
__device__ __forceinline__ void tile_transpose_out_packed(const char* T, bf16_t* __restrict__ dst, int nks, int col0, int row0, int nh, int tid) {
  const int w = tid >> 6, lane = tid & 63, gq = lane >> 4, i = lane & 15;
  typedef __attribute__((address_space(3))) s16x4 lds_s16x4;
#pragma unroll
  for (int h = 0; h < 2; ++h) {
    const int R0 = 8 * gq + 32 * h;
    const char* p = T + (R0 + (i >> 2)) * TP + (PERM ? 32 * (w >> 1) + 8 * (i & 3) + 4 * (w & 1) : 16 * w + 4 * (i & 3)) * 2;
    s16x4 a = __builtin_amdgcn_ds_read_tr16_b64_v4i16((lds_s16x4*)p);
    s16x4 b = __builtin_amdgcn_ds_read_tr16_b64_v4i16((lds_s16x4*)(p + 4 * TP));
    bf16x8 o = __builtin_shufflevector(a, b, 0, 1, 2, 3, 4, 5, 6, 7);
    if (h < nh) *(bf16x8*)(dst + ((size_t)((col0 >> 4) + w) * nks + (row0 >> 5) + h) * 512 + lane * 8) = o;
  }
}

__global__ __launch_bounds__(256) void kpre_kernel(
    const bf16_t* __restrict__ kin, const bf16_t* __restrict__ vin, const bf16_t* __restrict__ shift_k,
    bf16_t* __restrict__ kvT) {
  __shared__ __attribute__((aligned(16))) char T[64 * TP];
  const int tid = threadIdx.x;
  int j = blockIdx.x;
  const int r = tid >> 2, cq = tid & 3;
  {
    const int t0 = 64 * (j & 31), d0 = 64 * (j >> 5);
    const int t = t0 + r;
    u32x4 kr[2][4], vr[2], skr[2][4];
#pragma unroll
    for (int h = 0; h < 2; ++h) {
      const int d = d0 + 8 * (cq + 4 * h);
#pragma unroll
      for (int i = 0; i < 4; ++i) {
        u32x4 zz = {0u, 0u, 0u, 0u};
        kr[h][i] = (t - 3 + i >= 0) ? *(const u32x4*)(kin + (size_t)(t - 3 + i) * D_ + d) : zz;
      }
      vr[h] = *(const u32x4*)(vin + (size_t)t * D_ + d);
#pragma unroll
      for (int e2 = 0; e2 < 4; ++e2) skr[h][e2] = *(const u32x4*)(shift_k + (size_t)(d + 2 * e2) * 4);
    }
#pragma unroll
    for (int h = 0; h < 2; ++h) {
      u32x4 o;
#pragma unroll
      for (int e2 = 0; e2 < 4; ++e2) {
        const u32x4 sk = skr[h][e2];
        float res[2];
#pragma unroll
        for (int e = 0; e < 2; ++e) {
          float w0 = lo16(sk[2 * e]), w1 = hi16(sk[2 * e]), w2 = lo16(sk[2 * e + 1]), w3 = hi16(sk[2 * e + 1]);
          float k0 = e ? hi16(kr[h][0][e2]) : lo16(kr[h][0][e2]);
          float k1 = e ? hi16(kr[h][1][e2]) : lo16(kr[h][1][e2]);
          float k2 = e ? hi16(kr[h][2][e2]) : lo16(kr[h][2][e2]);
          float k3 = e ? hi16(kr[h][3][e2]) : lo16(kr[h][3][e2]);
          float ks = rbf(k0 * w3);
          ks = rbf(ks + rbf(k1 * w2));
          ks = rbf(ks + rbf(k2 * w1));
          ks = rbf(ks + rbf(k3 * w0));
          float vv = e ? hi16(vr[h][e2]) : lo16(vr[h][e2]);
          res[e] = ks * vv;
        }
        o[e2] = pk2(res[0], res[1]);
      }
      *(u32x4*)(T + r * TP + (cq + 4 * h) * 16) = o;
    }
    __syncthreads();
    tile_transpose_out(T, kvT + (size_t)d0 * S_ + t0, S_, tid);
  }
}

__device__ __forceinline__ int wsp_skew(int a) { return a & 3; }
__device__ __forceinline__ void prep_job(int j, int tid, char* T, const bf16_t* __restrict__ w_in, const bf16_t* __restrict__ w_out,
                                         const bf16_t* __restrict__ w_sp, bf16_t* __restrict__ winP,
                                         bf16_t* __restrict__ woutP, bf16_t* __restrict__ wspP) {
  const int r = tid >> 2, cq = tid & 3;
  if (j < 270) {
    const int d0 = 64 * (j % 10), f0 = 64 * (j / 10);
#pragma unroll
    for (int h = 0; h < 2; ++h)
      *(u32x4*)(T + r * TP + (cq + 4 * h) * 16) = *(const u32x4*)(w_in + (size_t)(d0 + r) * F_ + f0 + 8 * (cq + 4 * h));
    __syncthreads();
    tile_transpose_out_packed<true>(T, winP, 20, f0, d0, 2, tid);
    return;
  }
  j -= 270;
  if (j < 140) {
    const int d0 = 64 * (j % 10), f0 = 64 * (j / 10);
#pragma unroll
    for (int h = 0; h < 2; ++h) {
      u32x4 zz = {0u, 0u, 0u, 0u};
      *(u32x4*)(T + r * TP + (cq + 4 * h) * 16) =
          (f0 + r < FH_) ? *(const u32x4*)(w_out + (size_t)(f0 + r) * D_ + d0 + 8 * (cq + 4 * h)) : zz;
    }
    __syncthreads();
    tile_transpose_out_packed<false>(T, woutP, 27, d0, f0, f0 + 32 < FH_ ? 2 : 1, tid);
    return;
  }
  j -= 140;
  {
    const int wv = 4 * j + (tid >> 6), lane = tid & 63, fr = lane & 15, g = lane >> 4;
    const int a = wv >> 3, k0 = 8 * (wv & 7);
    const bf16_t* src = w_sp + (size_t)(16 * a + fr) * S_ + 32 * k0 + 8 * g;
    u32x4 v[8];
#pragma unroll
    for (int i = 0; i < 8; ++i) v[i] = *(const u32x4*)(src + 32 * i);
#pragma unroll
    for (int i = 0; i < 8; ++i)
      *(u32x4*)(wspP + ((size_t)a * 64 + ((k0 + i + wsp_skew(a)) & 63)) * 512 + lane * 8) = v[i];
  }
}

constexpr int CV_HP = 4416;
constexpr int CV_HCH = 4 * CV_HP;
constexpr int CV_UCH = 96 * 80;
constexpr int CV_OFF_H = 0;
constexpr int CV_OFF_U = CV_OFF_H + 2 * CV_HCH;
constexpr int CV_OFF_R = CV_OFF_U + 2 * CV_UCH;
constexpr int CV_OFF_Y = CV_OFF_R + 2 * 8192;
constexpr int CV_LDS = CV_OFF_Y + 2 * 4096;

__global__ __launch_bounds__(256, 2) void kconv_kernel(
    const bf16_t* __restrict__ kvT, const bf16_t* __restrict__ ssm_k, const bf16_t* __restrict__ ssm_D,
    bf16_t* __restrict__ YT, const bf16_t* __restrict__ w_in, const bf16_t* __restrict__ w_out, const bf16_t* __restrict__ w_sp,
    bf16_t* __restrict__ winP, bf16_t* __restrict__ woutP, bf16_t* __restrict__ wspP TSP) {
  extern __shared__ __attribute__((aligned(16))) char smem[];
  const int tid = threadIdx.x;
  if (blockIdx.x >= 320) {
    prep_job(blockIdx.x - 320, tid, smem, w_in, w_out, w_sp, winP, woutP, wspP);
    return;
  }
  TS(0);
  const int c0 = 2 * blockIdx.x;
  u32x4 ur[2];
#pragma unroll
  for (int i = 0; i < 2; ++i) ur[i] = *(const u32x4*)(kvT + (size_t)c0 * S_ + 8 * (tid + 256 * i));
  u32x2 hr[3][3];
#pragma unroll
  for (int rd = 0; rd < 3; ++rd) {
    int task = tid + 256 * rd;
    int ch = task / 262, c = task % 262 - 6;
#pragma unroll
    for (int i = 0; i < 3; ++i) {
      int idx = 8 * c + 4 * i;
      u32x2 z = {0u, 0u};
      hr[rd][i] = (task < 524 && idx >= 0 && idx < 2048) ? *(const u32x2*)(ssm_k + (size_t)(c0 + ch) * S_ + idx) : z;
    }
  }
  for (int i = tid; i < 2 * 160; i += 256) {
    int ch = i / 160, o = i % 160;
    u32x4 z = {0u, 0u, 0u, 0u};
    *(u32x4*)(smem + CV_OFF_U + ch * CV_UCH + o * 16) = z;
  }
  TS(1);
#pragma unroll
  for (int i = 0; i < 2; ++i) {
    const int e = 8 * (tid + 256 * i), ch = e >> 11, s = e & 2047;
    *(u32x4*)(smem + CV_OFF_U + ch * CV_UCH + ((s >> 5) + 32) * 80 + (s & 31) * 2) = ur[i];
  }
#pragma unroll
  for (int rd = 0; rd < 3; ++rd) {
    int task = tid + 256 * rd;
    if (task < 524) {
      int ch = task / 262, c = task % 262 - 6;
      uint32_t w[6] = {hr[rd][0][0], hr[rd][0][1], hr[rd][1][0], hr[rd][1][1], hr[rd][2][0], hr[rd][2][1]};
#pragma unroll
      for (int p = 0; p < 4; ++p) {
#pragma unroll
        for (int gi = 0; gi < 2; ++gi) {
          const int ao = ((1 - p) & 3) + 4 * gi;
          int a = 8 * c + ao;
          int qx = 2045 - p - a;
          auto el = [&](int e) -> uint32_t { return (e & 1) ? (w[e >> 1] >> 16) : (w[e >> 1] & 0xffff); };
          u32x2 o = {el(ao + 3) | (el(ao + 2) << 16), el(ao + 1) | (el(ao) << 16)};
          if (qx >= 0 && qx < 2088) *(u32x2*)(smem + CV_OFF_H + ch * CV_HCH + p * CV_HP + qx * 2) = o;
        }
      }
    }
  }
  TS(2);
  __syncthreads();
  TS(3);
  const int wid = tid >> 6, lane = tid & 63;
  const int ch = wid & 1, kb = wid >> 1;
  const int m = lane & 31, hh = lane >> 5;
  {
    const int p = (4 - (m & 3)) & 3;
    const char* abase = smem + CV_OFF_H + ch * CV_HCH + p * CV_HP + (2048 - m + 8 * hh - p + 16 * kb) * 2;
    const char* bbase = smem + CV_OFF_U + ch * CV_UCH + (m + 32) * 80 + 32 * kb + 16 * hh;
    f32x16 acc0 = {}, acc1 = {};
    u32x2 ra[3][4];
    bf16x8 rbB[3][2];
    auto ld = [&](int sl, int dl) {
      typedef const volatile __attribute__((address_space(3))) u32x2* lds_v2;
      ra[sl][0] = *(lds_v2)(abase - 64 * dl);
      ra[sl][1] = *(lds_v2)(abase - 64 * dl + 8);
      ra[sl][2] = *(lds_v2)(abase - 64 * (dl + 32));
      ra[sl][3] = *(lds_v2)(abase - 64 * (dl + 32) + 8);
      rbB[sl][0] = *(const bf16x8*)(bbase - 80 * dl);
      rbB[sl][1] = *(const bf16x8*)(bbase + 80 * (32 - dl));
    };
    ld(0, 0);
    ld(1, 1);
#pragma unroll
    for (int dl = 0; dl < 32; ++dl) {
      if (dl + 2 < 32) ld((dl + 2) % 3, dl + 2);
      __builtin_amdgcn_sched_barrier(0);
      const int sl = dl % 3;
      u32x4 A0u = {ra[sl][0][0], ra[sl][0][1], ra[sl][1][0], ra[sl][1][1]};
      u32x4 A1u = {ra[sl][2][0], ra[sl][2][1], ra[sl][3][0], ra[sl][3][1]};
      bf16x8 A0 = __builtin_bit_cast(bf16x8, A0u), A1 = __builtin_bit_cast(bf16x8, A1u);
      acc0 = __builtin_amdgcn_mfma_f32_32x32x16_bf16(A0, rbB[sl][0], acc0, 0, 0, 0);
      acc1 = __builtin_amdgcn_mfma_f32_32x32x16_bf16(A0, rbB[sl][1], acc1, 0, 0, 0);
      acc1 = __builtin_amdgcn_mfma_f32_32x32x16_bf16(A1, rbB[sl][0], acc1, 0, 0, 0);
      __builtin_amdgcn_sched_barrier(0);
    }
    TS(4);
    float* red = (float*)(smem + CV_OFF_R + ch * 8192);
    if (kb == 1) {
#pragma unroll
      for (int r = 0; r < 16; ++r) { red[r * 64 + lane] = acc0[r]; red[(16 + r) * 64 + lane] = acc1[r]; }
    }
    __syncthreads();
    if (kb == 0) {
      const float Dv = bf2f(ssm_D[c0 + ch]);
      bf16_t* yb = (bf16_t*)(smem + CV_OFF_Y + ch * 4096);
#pragma unroll
      for (int half = 0; half < 2; ++half) {
        const int a = m + 32 * half;
#pragma unroll
        for (int rq = 0; rq < 4; ++rq) {
          const int b0 = 8 * rq + 4 * hh;
          u32x2 kvw = *(const u32x2*)(smem + CV_OFF_U + ch * CV_UCH + (a + 32) * 80 + b0 * 2);
          float kvv[4] = {lo16(kvw[0]), hi16(kvw[0]), lo16(kvw[1]), hi16(kvw[1])};
          float o[4];
#pragma unroll
          for (int i = 0; i < 4; ++i) {
            const int r = 4 * rq + i;
            float y = (half ? acc1[r] : acc0[r]) + red[(16 * half + r) * 64 + lane];
            o[i] = rbf(y) + rbf(kvv[i] * Dv);
          }
          u32x2 ow = {pk2(o[0], o[1]), pk2(o[2], o[3])};
          *(u32x2*)(yb + 32 * a + b0) = ow;
        }
      }
    }
    __syncthreads();
    TS(5);
#pragma unroll
    for (int i = 0; i < 2; ++i) {
      const int e = tid + 256 * i, tb = e >> 2, cc = (e >> 1) & 1, hf = e & 1;
      *(u32x4*)(YT + ((size_t)tb * D_ + c0 + cc) * 16 + 8 * hf) = *(const u32x4*)(smem + CV_OFF_Y + cc * 4096 + (16 * tb + 8 * hf) * 2);
    }
    TS(6);
  }
}

template <int I> struct IC { static constexpr int value = I; };
template <int N, int I = 0, typename F>
__device__ __forceinline__ void static_for(F&& f) {
  if constexpr (I < N) { f(IC<I>{}); static_for<N, I + 1>(f); }
}

__device__ __forceinline__ float dpp_add(float v, int ctrl_sel) {
  int x = __float_as_int(v), y;
  switch (ctrl_sel) {
    case 0: y = __builtin_amdgcn_update_dpp(0, x, 0xB1, 0xf, 0xf, false); break;
    case 1: y = __builtin_amdgcn_update_dpp(0, x, 0x4E, 0xf, 0xf, false); break;
    case 2: y = __builtin_amdgcn_update_dpp(0, x, 0x141, 0xf, 0xf, false); break;
    default: y = __builtin_amdgcn_update_dpp(0, x, 0x140, 0xf, 0xf, false); break;
  }
  return v + __int_as_float(y);
}
__device__ __forceinline__ float wave_sum(float v) {
  v = dpp_add(v, 0); v = dpp_add(v, 1); v = dpp_add(v, 2); v = dpp_add(v, 3);
  int x = __float_as_int(v);
  return (__int_as_float(__builtin_amdgcn_readlane(x, 0)) + __int_as_float(__builtin_amdgcn_readlane(x, 16))) +
         (__int_as_float(__builtin_amdgcn_readlane(x, 32)) + __int_as_float(__builtin_amdgcn_readlane(x, 48)));
}

__global__ __launch_bounds__(256) void xln_kernel(const bf16_t* __restrict__ x0, const bf16_t* __restrict__ Yp,
                                                  bf16_t* __restrict__ x1, bf16_t* __restrict__ a1P) {
  __shared__ __attribute__((aligned(16))) char T[D_ * 32];
  __shared__ float R[2][4][16][2];
  const int tid = threadIdx.x, w = tid >> 6, lane = tid & 63, fr = lane & 15, g = lane >> 4;
  const int a = blockIdx.x;
  u32x4 yl[5], xv[5];
#pragma unroll
  for (int i = 0; i < 5; ++i) yl[i] = *(const u32x4*)(Yp + (size_t)a * (D_ * 16) + (tid + 256 * i) * 8);
  const size_t xo = (size_t)(16 * a + fr) * D_ + 32 * w + 8 * g;
#pragma unroll
  for (int i = 0; i < 5; ++i) xv[i] = *(const u32x4*)(x0 + xo + 128 * i);
#pragma unroll
  for (int i = 0; i < 5; ++i) *(u32x4*)(T + (tid + 256 * i) * 16) = yl[i];
  float s0 = 0.f, s1 = 0.f;
#pragma unroll
  for (int i = 0; i < 5; ++i)
#pragma unroll
    for (int e = 0; e < 4; ++e) { float x = lo16(xv[i][e]), y = hi16(xv[i][e]); s0 += x + y; s1 += x * x + y * y; }
  s0 += __shfl_xor(s0, 16); s0 += __shfl_xor(s0, 32);
  s1 += __shfl_xor(s1, 16); s1 += __shfl_xor(s1, 32);
  if (g == 0) { R[0][w][fr][0] = s0; R[0][w][fr][1] = s1; }
  __syncthreads();
  const f32x2 st = ln_stats((R[0][0][fr][0] + R[0][1][fr][0]) + (R[0][2][fr][0] + R[0][3][fr][0]),
                            (R[0][0][fr][1] + R[0][1][fr][1]) + (R[0][2][fr][1] + R[0][3][fr][1]), 1.0f / D_);
  typedef __attribute__((address_space(3))) s16x4 lds_s16x4;
  float t0 = 0.f, t1 = 0.f;
#pragma unroll
  for (int i = 0; i < 5; ++i) {
    const char* q = T + (32 * (w + 4 * i) + 8 * g + (fr >> 2)) * 32 + 8 * (fr & 3);
    s16x4 ya = __builtin_amdgcn_ds_read_tr16_b64_v4i16((lds_s16x4*)q);
    s16x4 yb = __builtin_amdgcn_ds_read_tr16_b64_v4i16((lds_s16x4*)(q + 128));
    u32x4 yv = __builtin_bit_cast(u32x4, __builtin_shufflevector(ya, yb, 0, 1, 2, 3, 4, 5, 6, 7));
    u32x4 o;
#pragma unroll
    for (int e = 0; e < 4; ++e) {
      float xa = lo16(xv[i][e]), xb = hi16(xv[i][e]);
      float da = rbf(rbf(xa - st[0]) * st[1]), db = rbf(rbf(xb - st[0]) * st[1]);
      float ta = rbf(da * lo16(yv[e])), tb = rbf(db * hi16(yv[e]));
      uint32_t wd = pk2(xa + ta, xb + tb);
      o[e] = wd;
      float pa = lo16(wd), pb = hi16(wd);
      t0 += pa + pb; t1 += pa * pa + pb * pb;
    }
    xv[i] = o;
    *(u32x4*)(x1 + xo + 128 * i) = o;
  }
  t0 += __shfl_xor(t0, 16); t0 += __shfl_xor(t0, 32);
  t1 += __shfl_xor(t1, 16); t1 += __shfl_xor(t1, 32);
  if (g == 0) { R[1][w][fr][0] = t0; R[1][w][fr][1] = t1; }
  __syncthreads();
  const f32x2 su = ln_stats((R[1][0][fr][0] + R[1][1][fr][0]) + (R[1][2][fr][0] + R[1][3][fr][0]),
                            (R[1][0][fr][1] + R[1][1][fr][1]) + (R[1][2][fr][1] + R[1][3][fr][1]), 1.0f / D_);
#pragma unroll
  for (int i = 0; i < 5; ++i) {
    u32x4 o;
#pragma unroll
    for (int e = 0; e < 4; ++e) o[e] = pk2(rbf(lo16(xv[i][e]) - su[0]) * su[1], rbf(hi16(xv[i][e]) - su[0]) * su[1]);
    *(u32x4*)(a1P + ((size_t)a * 20 + w + 4 * i) * 512 + lane * 8) = o;
  }
}

__device__ __forceinline__ void glds16(const void* gsrc, uint32_t lds_dst) {
  uint32_t keep;
  asm volatile("s_mov_b32 %0, m0\n\ts_mov_b32 m0, %2\n\ts_nop 0\n\tglobal_load_lds_dwordx4 %1, off\n\ts_mov_b32 m0, %0"
               : "=&s"(keep) : "v"(gsrc), "s"(lds_dst) : "memory");
}
__device__ __forceinline__ uint32_t lds_addr(const void* p) {
  return (uint32_t)(uintptr_t)(const __attribute__((address_space(3))) char*)p;
}
__device__ __forceinline__ void gload16(bf16x8& dst, const void* p) {
  asm volatile("global_load_dwordx4 %0, %1, off" : "=v"(dst) : "v"(p) : "memory");
}
template <int N>
__device__ __forceinline__ void wait_vm2(bf16x8& a, bf16x8& b) {
  asm volatile("s_waitcnt vmcnt(%2)" : "+v"(a), "+v"(b) : "n"(N) : "memory");
}
#define WAIT_VM(n) asm volatile("s_waitcnt vmcnt(" #n ")" ::: "memory")
#define WAIT_LGKM0() asm volatile("s_waitcnt lgkmcnt(0)" ::: "memory")
#define RAW_BAR() __builtin_amdgcn_s_barrier()

constexpr int G1_LDS = 81920;

__device__ __forceinline__ f32x2 gelu2(f32x2 x) {
  const float c1 = -2.f * 0.7978845608028654f * 1.4426950408889634f, c2 = c1 * 0.044715f;
  f32x2 t = x * x;
  f32x2 a = x * (t * c2 + c1);
  f32x2 d = {1.f + __builtin_amdgcn_exp2f(a[0]), 1.f + __builtin_amdgcn_exp2f(a[1])};
  f32x2 r = {__builtin_amdgcn_rcpf(d[0]), __builtin_amdgcn_rcpf(d[1])};
  return x * r;
}
__device__ __forceinline__ f32x2 rbf2(f32x2 v) {
  uint32_t w = pk2(v[0], v[1]);
  f32x2 r = {lo16(w), hi16(w)};
  return r;
}

constexpr int G1_P = 10;
constexpr int g1_wait(int kind, int idx) {
  int n = 0, endD[3] = {}, endW[20] = {};
  n += 4; endD[0] = n;
  for (int s = 0; s < 5; ++s) { n += 2; endW[s] = n; }
  n += 4; endD[1] = n;
  for (int s = 5; s < G1_P; ++s) { n += 2; endW[s] = n; }
  n += 2; endD[2] = n;
  for (int s = 0; s < 20; ++s) {
    if (kind == 0 && 8 * idx == s) return n - endD[idx];
    if (kind == 1 && idx == s) return n - endW[s];
    if (s + G1_P < 20) { n += 2; endW[s + G1_P] = n; }
  }
  return 0;
}
template <int N>
__device__ __forceinline__ void wait_vm() { asm volatile("s_waitcnt vmcnt(%0)" ::"n"(N) : "memory"); }

__global__ __launch_bounds__(512, 1) void g1_kernel(
    const bf16_t* __restrict__ a1P, const bf16_t* __restrict__ winT, bf16_t* __restrict__ z TSP) {
  extern __shared__ __attribute__((aligned(16))) char smem[];
  const int tid = threadIdx.x;
  TS(0);
  const int xcd = blockIdx.x & 7, idx = blockIdx.x >> 3;
  const int mt = 8 * (xcd >> 1) + (idx & 7);
  const int nt = 4 * (xcd & 1) + (idx >> 3);
  const int ps = nt < 6 ? 7 * nt : 42 + 6 * (nt - 6);
  const int np = nt < 6 ? 7 : 6;
  const int wid = __builtin_amdgcn_readfirstlane(tid >> 6), lane = tid & 63;
  const bf16_t* asrc = a1P + (size_t)(4 * mt) * (20 * 512) + lane * 8;
  const uint32_t lb = lds_addr(smem);
  auto issueD = [&](int c) {
    if (c < 2) {
      const int ks = 8 * c + wid;
#pragma unroll
      for (int tf = 0; tf < 4; ++tf) glds16(asrc + (size_t)(tf * 20 + ks) * 512, lb + (4 * ks + tf) * 1024);
    } else {
      const int ks = 16 + (wid & 3), t2 = 2 * (wid >> 2);
#pragma unroll
      for (int tf = 0; tf < 2; ++tf) glds16(asrc + (size_t)((t2 + tf) * 20 + ks) * 512, lb + (4 * ks + t2 + tf) * 1024);
    }
  };
  if (wid >= np) {
    issueD(0); issueD(1); issueD(2);
    WAIT_VM(6); RAW_BAR();
    WAIT_VM(2); RAW_BAR();
    WAIT_VM(0); RAW_BAR();
    return;
  }
  constexpr int P = G1_P;
  const bf16_t* wp[2];
#pragma unroll
  for (int i = 0; i < 2; ++i) wp[i] = winT + (size_t)(2 * (ps + wid) + i) * (20 * 512) + lane * 8;
  bf16x8 wv[P][2];
  issueD(0);
#pragma unroll
  for (int s = 0; s < 5; ++s)
#pragma unroll
    for (int i = 0; i < 2; ++i) gload16(wv[s][i], wp[i] + 512 * s);
  issueD(1);
#pragma unroll
  for (int s = 5; s < P; ++s)
#pragma unroll
    for (int i = 0; i < 2; ++i) gload16(wv[s][i], wp[i] + 512 * s);
  issueD(2);
  TS(1);
  f32x4 acc[2][4] = {};
  {
    auto step = [&](auto KS) {
      constexpr int ks = decltype(KS)::value;
      if constexpr ((ks & 7) == 0) { wait_vm<g1_wait(0, ks >> 3)>(); RAW_BAR(); }
      bf16x8 X[4];
#pragma unroll
      for (int j = 0; j < 4; ++j) X[j] = *(const bf16x8*)(smem + (4 * ks + j) * 1024 + lane * 16);
      wait_vm2<g1_wait(1, ks)>(wv[ks % P][0], wv[ks % P][1]);
#pragma unroll
      for (int j = 0; j < 4; ++j) {
        acc[0][j] = __builtin_amdgcn_mfma_f32_16x16x32_bf16(wv[ks % P][0], X[j], acc[0][j], 0, 0, 0);
        acc[1][j] = __builtin_amdgcn_mfma_f32_16x16x32_bf16(wv[ks % P][1], X[j], acc[1][j], 0, 0, 0);
      }
      if constexpr (ks + P < 20) {
        gload16(wv[ks % P][0], wp[0] + 512 * (ks + P));
        gload16(wv[ks % P][1], wp[1] + 512 * (ks + P));
      }
    };
    static_for<20>(step);
  }
  TS(3);
  bf16_t* zp = z + ((size_t)(4 * mt) * 54 + ps + wid) * 512 + lane * 8;
#pragma unroll
  for (int j = 0; j < 4; ++j) {
    u32x4 o;
#pragma unroll
    for (int i = 0; i < 2; ++i) {
      f32x2 lo = {acc[i][j][0], acc[i][j][1]}, hi = {acc[i][j][2], acc[i][j][3]};
      lo = gelu2(rbf2(lo)); hi = gelu2(rbf2(hi));
      o[2 * i] = pk2(lo[0], lo[1]); o[2 * i + 1] = pk2(hi[0], hi[1]);
    }
    *(u32x4*)(zp + (size_t)j * (54 * 512)) = o;
  }
  TS(4);
}

constexpr int ZT_P = 1744;
__global__ __launch_bounds__(256) void znt_kernel(const bf16_t* __restrict__ zP, bf16_t* __restrict__ znP) {
  __shared__ __attribute__((aligned(16))) char T[16 * ZT_P];
  __shared__ float R[4][16][2];
  const int tid = threadIdx.x, w = tid >> 6, lane = tid & 63, fr = lane & 15, g = lane >> 4;
  const int a = blockIdx.x;
  const bf16_t* src = zP + ((size_t)a * 54 + 27) * 512 + lane * 8;
  u32x4 v[7];
#pragma unroll
  for (int i = 0; i < 7; ++i) {
    u32x4 zz = {0u, 0u, 0u, 0u};
    v[i] = (w + 4 * i < 27) ? *(const u32x4*)(src + (size_t)(w + 4 * i) * 512) : zz;
  }
  float s0 = 0.f, s1 = 0.f;
#pragma unroll
  for (int i = 0; i < 7; ++i)
#pragma unroll
    for (int e = 0; e < 4; ++e) { float x = lo16(v[i][e]), y = hi16(v[i][e]); s0 += x + y; s1 += x * x + y * y; }
  s0 += __shfl_xor(s0, 16); s0 += __shfl_xor(s0, 32);
  s1 += __shfl_xor(s1, 16); s1 += __shfl_xor(s1, 32);
  if (g == 0) { R[w][fr][0] = s0; R[w][fr][1] = s1; }
  __syncthreads();
  const f32x2 st = ln_stats((R[0][fr][0] + R[1][fr][0]) + (R[2][fr][0] + R[3][fr][0]),
                            (R[0][fr][1] + R[1][fr][1]) + (R[2][fr][1] + R[3][fr][1]), 1.0f / FH_);
#pragma unroll
  for (int i = 0; i < 7; ++i) {
    if (w + 4 * i < 27) {
      u32x4 o;
#pragma unroll
      for (int e = 0; e < 4; ++e) o[e] = pk2(rbf(lo16(v[i][e]) - st[0]) * st[1], rbf(hi16(v[i][e]) - st[0]) * st[1]);
      *(u32x4*)(T + fr * ZT_P + (32 * (w + 4 * i) + 8 * g) * 2) = o;
    }
  }
  __syncthreads();
  typedef __attribute__((address_space(3))) s16x4 lds_s16x4;
  const int h2 = g >> 1, gl = g & 1;
#pragma unroll
  for (int i = 0; i < 7; ++i) {
    const int p = w + 4 * i;
    if (p < 27) {
      const char* q = T + (8 * gl + (fr >> 2)) * ZT_P + (32 * p + 8 * (fr & 3) + 4 * h2) * 2;
      s16x4 x = __builtin_amdgcn_ds_read_tr16_b64_v4i16((lds_s16x4*)q);
      s16x4 y = __builtin_amdgcn_ds_read_tr16_b64_v4i16((lds_s16x4*)(q + 4 * ZT_P));
      bf16x8 o = __builtin_shufflevector(x, y, 0, 1, 2, 3, 4, 5, 6, 7);
      *(bf16x8*)(znP + ((size_t)(2 * p + h2) * 64 + (a >> 1)) * 512 + (16 * (2 * (a & 1) + gl) + fr) * 8) = o;
    }
  }
}

constexpr int G2_LDS = 4 * 32768;
constexpr int g2_wait(int kind, int idx) {
  int n = 0, endD[8] = {}, endW[32] = {};
  n += 4; endD[0] = n;
  for (int s = 0; s < 4; ++s) { n += 2; endW[s] = n; }
  n += 4; endD[1] = n;
  for (int s = 4; s < 8; ++s) { n += 2; endW[s] = n; }
  n += 4; endD[2] = n;
  for (int c = 0; c < 8; ++c) {
    if (kind == 0 && idx == c) return n - endD[c];
    if (c + 3 < 8) { n += 4; endD[c + 3] = n; }
    for (int q = 0; q < 4; ++q) {
      const int s = 4 * c + q;
      if (kind == 1 && idx == s) return n - endW[s];
      if (s + 8 < 32) { n += 2; endW[s + 8] = n; }
    }
  }
  return 0;
}

__global__ __launch_bounds__(512, 1) void g2_kernel(
    const bf16_t* __restrict__ wspP, const bf16_t* __restrict__ zP, const bf16_t* __restrict__ znP,
    const bf16_t* __restrict__ bsp, bf16_t* __restrict__ uP TSP) {
  extern __shared__ __attribute__((aligned(16))) char smem[];
  const int tid = threadIdx.x;
  TS(0);
  const int xcd = blockIdx.x & 7, idx = blockIdx.x >> 3;
  const int mt = 4 * (xcd >> 1) + (idx & 3);
  const int nt = 7 * (xcd & 1) + (idx >> 2);
  const int wid = __builtin_amdgcn_readfirstlane(tid >> 6), lane = tid & 63;
  const int tg = wid & 3, h = wid >> 2;
  const int rot = (2 * idx + 9 * xcd) & 63;
  const bf16_t* zsrc[4];
#pragma unroll
  for (int j = 0; j < 4; ++j) { int ff = 4 * nt + j; ff = ff < 54 ? ff : ff - 2; zsrc[j] = znP + (size_t)ff * (64 * 512) + lane * 8; }
  const uint32_t lbase = lds_addr(smem) + wid * 4096;
  auto issueD = [&](int c) {
    const int ke = (8 * c + wid + rot) & 63;
#pragma unroll
    for (int j = 0; j < 4; ++j) glds16(zsrc[j] + 512 * ke, lbase + (c & 3) * 32768 + j * 1024);
  };
  const bf16_t* wsrc[2];
  int wrot[2];
#pragma unroll
  for (int i = 0; i < 2; ++i) {
    const int a = 8 * mt + 2 * tg + i;
    wsrc[i] = wspP + (size_t)a * (64 * 512) + lane * 8;
    wrot[i] = h + rot + wsp_skew(a);
  }
  bf16x8 wv[8][2];
  auto issueW = [&](int s) {
#pragma unroll
    for (int i = 0; i < 2; ++i) gload16(wv[s & 7][i], wsrc[i] + 512 * ((2 * s + wrot[i]) & 63));
  };
  issueD(0);
#pragma unroll
  for (int s = 0; s < 4; ++s) issueW(s);
  issueD(1);
#pragma unroll
  for (int s = 4; s < 8; ++s) issueW(s);
  issueD(2);
  TS(1);
  f32x4 acc[2][4] = {};
  const char* lrd = smem + h * 4096 + lane * 16;
  auto stage = [&](auto C) {
    constexpr int c = decltype(C)::value;
    wait_vm<g2_wait(0, c)>();
    RAW_BAR();
    if constexpr (c + 3 < 8) issueD(c + 3);
    auto step = [&](auto Q) {
      constexpr int q = decltype(Q)::value, s = 4 * c + q;
      bf16x8 Z[4];
#pragma unroll
      for (int j = 0; j < 4; ++j) Z[j] = *(const bf16x8*)(lrd + (c & 3) * 32768 + q * 8192 + j * 1024);
      wait_vm2<g2_wait(1, s)>(wv[s & 7][0], wv[s & 7][1]);
#pragma unroll
      for (int j = 0; j < 4; ++j) {
        acc[0][j] = __builtin_amdgcn_mfma_f32_16x16x32_bf16(Z[j], wv[s & 7][0], acc[0][j], 0, 0, 0);
        acc[1][j] = __builtin_amdgcn_mfma_f32_16x16x32_bf16(Z[j], wv[s & 7][1], acc[1][j], 0, 0, 0);
      }
      if constexpr (s + 8 < 32) issueW(s + 8);
    };
    static_for<4>(step);
  };
  static_for<8>(stage);
  TS(3);
  __syncthreads();
  {
    f32x4* X = (f32x4*)smem + wid * 256 + lane;
#pragma unroll
    for (int i = 0; i < 2; ++i)
#pragma unroll
      for (int jj = 0; jj < 2; ++jj) X[(2 * i + jj) * 64] = h ? acc[i][jj] : acc[i][2 + jj];
  }
  __syncthreads();
  const int pair = 2 * nt + h;
  if (pair < 27) {
    const f32x4* X = (const f32x4*)smem + (wid ^ 4) * 256 + lane;
    const int fr = lane & 15;
#pragma unroll
    for (int i = 0; i < 2; ++i) {
      const int a = 8 * mt + 2 * tg + i;
      const float bb = bf2f(bsp[16 * a + fr]);
      const u32x4 z1 = *(const u32x4*)(zP + ((size_t)a * 54 + pair) * 512 + lane * 8);
      u32x4 o;
#pragma unroll
      for (int jj = 0; jj < 2; ++jj) {
        f32x4 v = (h ? acc[i][2 + jj] : acc[i][jj]) + X[(2 * i + jj) * 64];
        float g0 = rbf(rbf(v[0]) + bb), g1 = rbf(rbf(v[1]) + bb), g2 = rbf(rbf(v[2]) + bb), g3 = rbf(rbf(v[3]) + bb);
        o[2 * jj] = pk2(lo16(z1[2 * jj]) * g0, hi16(z1[2 * jj]) * g1);
        o[2 * jj + 1] = pk2(lo16(z1[2 * jj + 1]) * g2, hi16(z1[2 * jj + 1]) * g3);
      }
      *(u32x4*)(uP + ((size_t)a * 27 + pair) * 512 + lane * 8) = o;
    }
  }
  TS(4);
}

constexpr int G3_LDS = 28 * 4096;
constexpr int G3_P = 10;
constexpr int g3_nd(int c) { return c < 3 ? 4 : 2; }
constexpr int g3_wait(int kind, int idx) {
  int n = 0, endD[4] = {}, endW[27] = {};
  n += g3_nd(0); endD[0] = n;
  for (int s = 0; s < 5; ++s) { n += 1; endW[s] = n; }
  n += g3_nd(1); endD[1] = n;
  for (int s = 5; s < G3_P; ++s) { n += 1; endW[s] = n; }
  n += g3_nd(2); endD[2] = n;
  n += g3_nd(3); endD[3] = n;
  for (int s = 0; s < 27; ++s) {
    if (kind == 0 && 8 * idx == s) return n - endD[idx];
    if (kind == 1 && idx == s) return n - endW[s];
    if (s + G3_P < 27) { n += 1; endW[s + G3_P] = n; }
  }
  return 0;
}

__global__ __launch_bounds__(512, 1) void g3_kernel(
    const bf16_t* __restrict__ uP, const bf16_t* __restrict__ woutP, const bf16_t* __restrict__ x1,
    bf16_t* __restrict__ xout TSP) {
  extern __shared__ __attribute__((aligned(16))) char smem[];
  const int tid = threadIdx.x;
  TS(0);
  const int xcd = blockIdx.x & 7, idx = blockIdx.x >> 3;
  const int mt = 8 * (xcd >> 1) + (idx & 7);
  const int nt = 4 * (xcd & 1) + (idx >> 3);
  const int wid = __builtin_amdgcn_readfirstlane(tid >> 6), lane = tid & 63;
  const bf16_t* usrc = uP + (size_t)(4 * mt) * (27 * 512) + lane * 8;
  const uint32_t lb = lds_addr(smem);
  auto issueD = [&](int c) {
    if (c < 3) {
      const int ks = 8 * c + wid;
#pragma unroll
      for (int tf = 0; tf < 4; ++tf) glds16(usrc + (size_t)(tf * 27 + ks) * 512, lb + (4 * ks + tf) * 1024);
    } else {
      const int ks = 24 + (wid % 3), t2 = 2 * ((wid / 3) & 1);
#pragma unroll
      for (int tf = 0; tf < 2; ++tf) glds16(usrc + (size_t)((t2 + tf) * 27 + ks) * 512, lb + (4 * ks + t2 + tf) * 1024);
    }
  };
  if (wid >= 5) {
    issueD(0); issueD(1); issueD(2); issueD(3);
    WAIT_VM(10); RAW_BAR();
    WAIT_VM(6); RAW_BAR();
    WAIT_VM(2); RAW_BAR();
    WAIT_VM(0); RAW_BAR();
    return;
  }
  const bf16_t* wsrc = woutP + (size_t)(5 * nt + wid) * (27 * 512) + lane * 8;
  bf16x8 wv[G3_P];
  issueD(0);
#pragma unroll
  for (int s = 0; s < 5; ++s) gload16(wv[s], wsrc + 512 * s);
  issueD(1);
#pragma unroll
  for (int s = 5; s < G3_P; ++s) gload16(wv[s], wsrc + 512 * s);
  issueD(2);
  issueD(3);
  const int fr = lane & 15, g = lane >> 4;
  u32x2 xr[4];
  TS(1);
  f32x4 acc[4] = {};
  auto step = [&](auto S) {
    constexpr int s = decltype(S)::value;
    if constexpr ((s & 7) == 0) { wait_vm<g3_wait(0, s >> 3)>(); RAW_BAR(); }
    bf16x8 U[4];
#pragma unroll
    for (int tf = 0; tf < 4; ++tf) U[tf] = *(const bf16x8*)(smem + (4 * s + tf) * 1024 + lane * 16);
    asm volatile("s_waitcnt vmcnt(%1)" : "+v"(wv[s % G3_P]) : "n"(g3_wait(1, s)) : "memory");
#pragma unroll
    for (int tf = 0; tf < 4; ++tf) acc[tf] = __builtin_amdgcn_mfma_f32_16x16x32_bf16(wv[s % G3_P], U[tf], acc[tf], 0, 0, 0);
    if constexpr (s + G3_P < 27) gload16(wv[s % G3_P], wsrc + 512 * (s + G3_P));
  };
  static_for<27>(step);
  TS(2);
  const int d = 80 * nt + 16 * wid + 4 * g;
#pragma unroll
  for (int tf = 0; tf < 4; ++tf) xr[tf] = *(const u32x2*)(x1 + (size_t)(64 * mt + 16 * tf + fr) * D_ + d);
#pragma unroll
  for (int tf = 0; tf < 4; ++tf) {
    u32x2 w = {pk2(lo16(xr[tf][0]) + rbf(acc[tf][0]), hi16(xr[tf][0]) + rbf(acc[tf][1])),
               pk2(lo16(xr[tf][1]) + rbf(acc[tf][2]), hi16(xr[tf][1]) + rbf(acc[tf][3]))};
    *(u32x2*)(xout + (size_t)(64 * mt + 16 * tf + fr) * D_ + d) = w;
  }
  TS(3);
}

extern "C" void kernel_launch(void* const* d_in, const int* in_sizes, int n_in,
                              void* d_out, int out_size, void* d_ws, size_t ws_size,
                              hipStream_t stream) {
  const bf16_t* q = (const bf16_t*)d_in[0];
  const bf16_t* k = (const bf16_t*)d_in[1];
  const bf16_t* v = (const bf16_t*)d_in[2];
  const bf16_t* shift_k = (const bf16_t*)d_in[3];
  const bf16_t* ssm_k = (const bf16_t*)d_in[4];
  const bf16_t* ssm_D = (const bf16_t*)d_in[5];
  const bf16_t* w_in = (const bf16_t*)d_in[7];
  const bf16_t* w_sp = (const bf16_t*)d_in[8];
  const bf16_t* b_sp = (const bf16_t*)d_in[9];
  const bf16_t* w_out = (const bf16_t*)d_in[10];
  bf16_t* out = (bf16_t*)d_out;

  char* ws = (char*)d_ws;
  size_t off = 0;
  auto alloc = [&](size_t bytes) { char* p = ws + off; off += (bytes + 255) & ~(size_t)255; return p; };
  bf16_t* winP = (bf16_t*)alloc((size_t)F_ * D_ * 2);
  bf16_t* woutP = (bf16_t*)alloc((size_t)D_ * FH_ * 2);
  bf16_t* wspP = (bf16_t*)alloc((size_t)S_ * S_ * 2);
  bf16_t* kvT = (bf16_t*)alloc((size_t)S_ * D_ * 2);
  bf16_t* YT = (bf16_t*)alloc((size_t)S_ * D_ * 2);
  bf16_t* x1 = (bf16_t*)alloc((size_t)S_ * D_ * 2);
  bf16_t* x2 = (bf16_t*)alloc((size_t)S_ * D_ * 2);
  bf16_t* x3 = (bf16_t*)alloc((size_t)S_ * D_ * 2);
  bf16_t* zP = (bf16_t*)alloc((size_t)S_ * F_ * 2);
  bf16_t* uP = (bf16_t*)alloc((size_t)S_ * FH_ * 2);
  bf16_t* znP = (bf16_t*)alloc((size_t)S_ * FH_ * 2);
  bf16_t* a1 = (bf16_t*)alloc((size_t)S_ * D_ * 2);

  (void)hipFuncSetAttribute((const void*)kconv_kernel, hipFuncAttributeMaxDynamicSharedMemorySize, CV_LDS);
  (void)hipFuncSetAttribute((const void*)g1_kernel, hipFuncAttributeMaxDynamicSharedMemorySize, G1_LDS);
  (void)hipFuncSetAttribute((const void*)g2_kernel, hipFuncAttributeMaxDynamicSharedMemorySize, G2_LDS);
  (void)hipFuncSetAttribute((const void*)g3_kernel, hipFuncAttributeMaxDynamicSharedMemorySize, G3_LDS);

  kpre_kernel<<<320, 256, 0, stream>>>(k, v, shift_k, kvT);
  kconv_kernel<<<320 + 270 + 140 + 256, 256, CV_LDS, stream>>>(kvT, ssm_k, ssm_D, YT, w_in, w_out, w_sp, winP, woutP, wspP TSA(0));
  xln_kernel<<<128, 256, 0, stream>>>(q, YT, x1, a1);
  g1_kernel<<<256, 512, G1_LDS, stream>>>(a1, winP, zP TSA(1));
  znt_kernel<<<128, 256, 0, stream>>>(zP, znP);
  g2_kernel<<<224, 512, G2_LDS, stream>>>(wspP, zP, znP, b_sp, uP TSA(2));
  g3_kernel<<<256, 512, G3_LDS, stream>>>(uP, woutP, x1, x2 TSA(3));
  xln_kernel<<<128, 256, 0, stream>>>(x2, YT, x3, a1);
  g1_kernel<<<256, 512, G1_LDS, stream>>>(a1, winP, zP TSA(4));
  znt_kernel<<<128, 256, 0, stream>>>(zP, znP);
  g2_kernel<<<224, 512, G2_LDS, stream>>>(wspP, zP, znP, b_sp, uP TSA(5));
  g3_kernel<<<256, 512, G3_LDS, stream>>>(uP, woutP, x3, out TSA(6));
}
```

```cpp
#include <hip/hip_runtime.h>
#include <hip/hip_bf16.h>
#include <stdint.h>

typedef __attribute__((ext_vector_type(8))) short bf16x8;
typedef __attribute__((ext_vector_type(4))) short s16x4;
typedef __attribute__((ext_vector_type(4))) float f32x4;
typedef __attribute__((ext_vector_type(16))) float f32x16;
typedef __attribute__((ext_vector_type(2))) float f32x2;
typedef __attribute__((ext_vector_type(2))) __bf16 bf16x2_t;
typedef __attribute__((ext_vector_type(4))) uint32_t u32x4;
typedef __attribute__((ext_vector_type(2))) uint32_t u32x2;
typedef uint16_t bf16_t;

#define TSP
#define TSA(k)
#define TS(i)

constexpr int S_ = 2048, D_ = 640, F_ = 1728, FH_ = 864, FHP_ = 896;

__device__ __forceinline__ uint32_t pk2(float lo, float hi) {
  f32x2 f = {lo, hi};
  bf16x2_t b = __builtin_convertvector(f, bf16x2_t);
  return __builtin_bit_cast(uint32_t, b);
}
__device__ __forceinline__ float lo16(uint32_t v) { return __uint_as_float(v << 16); }
__device__ __forceinline__ float hi16(uint32_t v) { return __uint_as_float(v & 0xffff0000u); }
__device__ __forceinline__ float rbf(float x) { return lo16(pk2(x, x)); }
__device__ __forceinline__ float bf2f(bf16_t v) { return __uint_as_float(((uint32_t)v) << 16); }

__device__ __forceinline__ f32x2 ln_stats(float sum, float sumsq, float inv_n) {
  float mean = sum * inv_n;
  float var = fmaxf(sumsq * inv_n - mean * mean, 0.f);
  float mb = rbf(mean);
  float vb = rbf(var);
  float sq = rbf(sqrtf(rbf(vb + 1e-5f)));
  f32x2 r = {mb, 1.0f / sq};
  return r;
}

constexpr int TP = 144;
__device__ __forceinline__ void tile_transpose_out(const char* T, bf16_t* __restrict__ dst, size_t dstride, int tid) {
  const int w = tid >> 6, lane = tid & 63, gq = lane >> 4, i = lane & 15;
  typedef __attribute__((address_space(3))) s16x4 lds_s16x4;
#pragma unroll
  for (int h = 0; h < 2; ++h) {
    const int R0 = 8 * gq + 32 * h;
    const char* p = T + (R0 + (i >> 2)) * TP + (16 * w + 4 * (i & 3)) * 2;
    s16x4 a = __builtin_amdgcn_ds_read_tr16_b64_v4i16((lds_s16x4*)p);
    s16x4 b = __builtin_amdgcn_ds_read_tr16_b64_v4i16((lds_s16x4*)(p + 4 * TP));
    bf16x8 o = __builtin_shufflevector(a, b, 0, 1, 2, 3, 4, 5, 6, 7);
    *(bf16x8*)(dst + (size_t)(16 * w + i) * dstride + R0) = o;
  }
}

template <bool PERM>
__device__ __forceinline__ void tile_transpose_out_packed(const char* T, bf16_t* __restrict__ dst, int nks, int col0, int row0, int nh, int tid) {
  const int w = tid >> 6, lane = tid & 63, gq = lane >> 4, i = lane & 15;
  typedef __attribute__((address_space(3))) s16x4 lds_s16x4;
#pragma unroll
  for (int h = 0; h < 2; ++h) {
    const int R0 = 8 * gq + 32 * h;
    const char* p = T + (R0 + (i >> 2)) * TP + (PERM ? 32 * (w >> 1) + 8 * (i & 3) + 4 * (w & 1) : 16 * w + 4 * (i & 3)) * 2;
    s16x4 a = __builtin_amdgcn_ds_read_tr16_b64_v4i16((lds_s16x4*)p);
    s16x4 b = __builtin_amdgcn_ds_read_tr16_b64_v4i16((lds_s16x4*)(p + 4 * TP));
    bf16x8 o = __builtin_shufflevector(a, b, 0, 1, 2, 3, 4, 5, 6, 7);
    if (h < nh) *(bf16x8*)(dst + ((size_t)((col0 >> 4) + w) * nks + (row0 >> 5) + h) * 512 + lane * 8) = o;
  }
}

__global__ __launch_bounds__(256) void kpre_kernel(
    const bf16_t* __restrict__ kin, const bf16_t* __restrict__ vin, const bf16_t* __restrict__ shift_k,
    bf16_t* __restrict__ kvT) {
  __shared__ __attribute__((aligned(16))) char T[64 * TP];
  const int tid = threadIdx.x;
  int j = blockIdx.x;
  const int r = tid >> 2, cq = tid & 3;
  {
    const int t0 = 64 * (j & 31), d0 = 64 * (j >> 5);
    const int t = t0 + r;
    u32x4 kr[2][4], vr[2], skr[2][4];
#pragma unroll
    for (int h = 0; h < 2; ++h) {
      const int d = d0 + 8 * (cq + 4 * h);
#pragma unroll
      for (int i = 0; i < 4; ++i) {
        u32x4 zz = {0u, 0u, 0u, 0u};
        kr[h][i] = (t - 3 + i >= 0) ? *(const u32x4*)(kin + (size_t)(t - 3 + i) * D_ + d) : zz;
      }
      vr[h] = *(const u32x4*)(vin + (size_t)t * D_ + d);
#pragma unroll
      for (int e2 = 0; e2 < 4; ++e2) skr[h][e2] = *(const u32x4*)(shift_k + (size_t)(d + 2 * e2) * 4);
    }
#pragma unroll
    for (int h = 0; h < 2; ++h) {
      u32x4 o;
#pragma unroll
      for (int e2 = 0; e2 < 4; ++e2) {
        const u32x4 sk = skr[h][e2];
        float res[2];
#pragma unroll
        for (int e = 0; e < 2; ++e) {
          float w0 = lo16(sk[2 * e]), w1 = hi16(sk[2 * e]), w2 = lo16(sk[2 * e + 1]), w3 = hi16(sk[2 * e + 1]);
          float k0 = e ? hi16(kr[h][0][e2]) : lo16(kr[h][0][e2]);
          float k1 = e ? hi16(kr[h][1][e2]) : lo16(kr[h][1][e2]);
          float k2 = e ? hi16(kr[h][2][e2]) : lo16(kr[h][2][e2]);
          float k3 = e ? hi16(kr[h][3][e2]) : lo16(kr[h][3][e2]);
          float ks = rbf(k0 * w3);
          ks = rbf(ks + rbf(k1 * w2));
          ks = rbf(ks + rbf(k2 * w1));
          ks = rbf(ks + rbf(k3 * w0));
          float vv = e ? hi16(vr[h][e2]) : lo16(vr[h][e2]);
          res[e] = ks * vv;
        }
        o[e2] = pk2(res[0], res[1]);
      }
      *(u32x4*)(T + r * TP + (cq + 4 * h) * 16) = o;
    }
    __syncthreads();
    tile_transpose_out(T, kvT + (size_t)d0 * S_ + t0, S_, tid);
  }
}

__device__ __forceinline__ int wsp_skew(int a) { return a & 3; }
__device__ __forceinline__ void prep_job(int j, int tid, char* T, const bf16_t* __restrict__ w_in, const bf16_t* __restrict__ w_out,
                                         const bf16_t* __restrict__ w_sp, bf16_t* __restrict__ winP,
                                         bf16_t* __restrict__ woutP, bf16_t* __restrict__ wspP) {
  const int r = tid >> 2, cq = tid & 3;
  if (j < 270) {
    const int d0 = 64 * (j % 10), f0 = 64 * (j / 10);
#pragma unroll
    for (int h = 0; h < 2; ++h)
      *(u32x4*)(T + r * TP + (cq + 4 * h) * 16) = *(const u32x4*)(w_in + (size_t)(d0 + r) * F_ + f0 + 8 * (cq + 4 * h));
    __syncthreads();
    tile_transpose_out_packed<true>(T, winP, 20, f0, d0, 2, tid);
    return;
  }
  j -= 270;
  if (j < 140) {
    const int d0 = 64 * (j % 10), f0 = 64 * (j / 10);
#pragma unroll
    for (int h = 0; h < 2; ++h) {
      u32x4 zz = {0u, 0u, 0u, 0u};
      *(u32x4*)(T + r * TP + (cq + 4 * h) * 16) =
          (f0 + r < FH_) ? *(const u32x4*)(w_out + (size_t)(f0 + r) * D_ + d0 + 8 * (cq + 4 * h)) : zz;
    }
    __syncthreads();
    tile_transpose_out_packed<false>(T, woutP, 27, d0, f0, f0 + 32 < FH_ ? 2 : 1, tid);
    return;
  }
  j -= 140;
  {
    const int wv = 4 * j + (tid >> 6), lane = tid & 63, fr = lane & 15, g = lane >> 4;
    const int a = wv >> 3, k0 = 8 * (wv & 7);
    const bf16_t* src = w_sp + (size_t)(16 * a + fr) * S_ + 32 * k0 + 8 * g;
    u32x4 v[8];
#pragma unroll
    for (int i = 0; i < 8; ++i) v[i] = *(const u32x4*)(src + 32 * i);
#pragma unroll
    for (int i = 0; i < 8; ++i)
      *(u32x4*)(wspP + ((size_t)a * 64 + ((k0 + i + wsp_skew(a)) & 63)) * 512 + lane * 8) = v[i];
  }
}

constexpr int CV_HP = 4416;
constexpr int CV_HCH = 4 * CV_HP;
constexpr int CV_UCH = 96 * 80;
constexpr int CV_OFF_H = 0;
constexpr int CV_OFF_U = CV_OFF_H + 2 * CV_HCH;
constexpr int CV_OFF_R = CV_OFF_U + 2 * CV_UCH;
constexpr int CV_OFF_Y = CV_OFF_R + 2 * 8192;
constexpr int CV_LDS = CV_OFF_Y + 2 * 4096;

__global__ __launch_bounds__(256, 2) void kconv_kernel(
    const bf16_t* __restrict__ kvT, const bf16_t* __restrict__ ssm_k, const bf16_t* __restrict__ ssm_D,
    bf16_t* __restrict__ YT, const bf16_t* __restrict__ w_in, const bf16_t* __restrict__ w_out, const bf16_t* __restrict__ w_sp,
    bf16_t* __restrict__ winP, bf16_t* __restrict__ woutP, bf16_t* __restrict__ wspP TSP) {
  extern __shared__ __attribute__((aligned(16))) char smem[];
  const int tid = threadIdx.x;
  if (blockIdx.x >= 320) {
    prep_job(blockIdx.x - 320, tid, smem, w_in, w_out, w_sp, winP, woutP, wspP);
    return;
  }
  TS(0);
  const int c0 = 2 * blockIdx.x;
  u32x4 ur[2];
#pragma unroll
  for (int i = 0; i < 2; ++i) ur[i] = *(const u32x4*)(kvT + (size_t)c0 * S_ + 8 * (tid + 256 * i));
  u32x2 hr[3][3];
#pragma unroll
  for (int rd = 0; rd < 3; ++rd) {
    int task = tid + 256 * rd;
    int ch = task / 262, c = task % 262 - 6;
#pragma unroll
    for (int i = 0; i < 3; ++i) {
      int idx = 8 * c + 4 * i;
      u32x2 z = {0u, 0u};
      hr[rd][i] = (task < 524 && idx >= 0 && idx < 2048) ? *(const u32x2*)(ssm_k + (size_t)(c0 + ch) * S_ + idx) : z;
    }
  }
  for (int i = tid; i < 2 * 160; i += 256) {
    int ch = i / 160, o = i % 160;
    u32x4 z = {0u, 0u, 0u, 0u};
    *(u32x4*)(smem + CV_OFF_U + ch * CV_UCH + o * 16) = z;
  }
  TS(1);
#pragma unroll
  for (int i = 0; i < 2; ++i) {
    const int e = 8 * (tid + 256 * i), ch = e >> 11, s = e & 2047;
    *(u32x4*)(smem + CV_OFF_U + ch * CV_UCH + ((s >> 5) + 32) * 80 + (s & 31) * 2) = ur[i];
  }
#pragma unroll
  for (int rd = 0; rd < 3; ++rd) {
    int task = tid + 256 * rd;
    if (task < 524) {
      int ch = task / 262, c = task % 262 - 6;
      uint32_t w[6] = {hr[rd][0][0], hr[rd][0][1], hr[rd][1][0], hr[rd][1][1], hr[rd][2][0], hr[rd][2][1]};
#pragma unroll
      for (int p = 0; p < 4; ++p) {
#pragma unroll
        for (int gi = 0; gi < 2; ++gi) {
          const int ao = ((1 - p) & 3) + 4 * gi;
          int a = 8 * c + ao;
          int qx = 2045 - p - a;
          auto el = [&](int e) -> uint32_t { return (e & 1) ? (w[e >> 1] >> 16) : (w[e >> 1] & 0xffff); };
          u32x2 o = {el(ao + 3) | (el(ao + 2) << 16), el(ao + 1) | (el(ao) << 16)};
          if (qx >= 0 && qx < 2088) *(u32x2*)(smem + CV_OFF_H + ch * CV_HCH + p * CV_HP + qx * 2) = o;
        }
      }
    }
  }
  TS(2);
  __syncthreads();
  TS(3);
  const int wid = tid >> 6, lane = tid & 63;
  const int ch = wid & 1, kb = wid >> 1;
  const int m = lane & 31, hh = lane >> 5;
  {
    const int p = (4 - (m & 3)) & 3;
    const char* abase = smem + CV_OFF_H + ch * CV_HCH + p * CV_HP + (2048 - m + 8 * hh - p + 16 * kb) * 2;
    const char* bbase = smem + CV_OFF_U + ch * CV_UCH + (m + 32) * 80 + 32 * kb + 16 * hh;
    f32x16 acc0 = {}, acc1 = {};
    u32x2 ra[3][4];
    bf16x8 rbB[3][2];
    auto ld = [&](int sl, int dl) {
      typedef const volatile __attribute__((address_space(3))) u32x2* lds_v2;
      ra[sl][0] = *(lds_v2)(abase - 64 * dl);
      ra[sl][1] = *(lds_v2)(abase - 64 * dl + 8);
      ra[sl][2] = *(lds_v2)(abase - 64 * (dl + 32));
      ra[sl][3] = *(lds_v2)(abase - 64 * (dl + 32) + 8);
      rbB[sl][0] = *(const bf16x8*)(bbase - 80 * dl);
      rbB[sl][1] = *(const bf16x8*)(bbase + 80 * (32 - dl));
    };
    ld(0, 0);
    ld(1, 1);
#pragma unroll
    for (int dl = 0; dl < 32; ++dl) {
      if (dl + 2 < 32) ld((dl + 2) % 3, dl + 2);
      __builtin_amdgcn_sched_barrier(0);
      const int sl = dl % 3;
      u32x4 A0u = {ra[sl][0][0], ra[sl][0][1], ra[sl][1][0], ra[sl][1][1]};
      u32x4 A1u = {ra[sl][2][0], ra[sl][2][1], ra[sl][3][0], ra[sl][3][1]};
      bf16x8 A0 = __builtin_bit_cast(bf16x8, A0u), A1 = __builtin_bit_cast(bf16x8, A1u);
      acc0 = __builtin_amdgcn_mfma_f32_32x32x16_bf16(A0, rbB[sl][0], acc0, 0, 0, 0);
      acc1 = __builtin_amdgcn_mfma_f32_32x32x16_bf16(A0, rbB[sl][1], acc1, 0, 0, 0);
      acc1 = __builtin_amdgcn_mfma_f32_32x32x16_bf16(A1, rbB[sl][0], acc1, 0, 0, 0);
      __builtin_amdgcn_sched_barrier(0);
    }
    TS(4);
    float* red = (float*)(smem + CV_OFF_R + ch * 8192);
    if (kb == 1) {
#pragma unroll
      for (int r = 0; r < 16; ++r) { red[r * 64 + lane] = acc0[r]; red[(16 + r) * 64 + lane] = acc1[r]; }
    }
    __syncthreads();
    if (kb == 0) {
      const float Dv = bf2f(ssm_D[c0 + ch]);
      bf16_t* yb = (bf16_t*)(smem + CV_OFF_Y + ch * 4096);
#pragma unroll
      for (int half = 0; half < 2; ++half) {
        const int a = m + 32 * half;
#pragma unroll
        for (int rq = 0; rq < 4; ++rq) {
          const int b0 = 8 * rq + 4 * hh;
          u32x2 kvw = *(const u32x2*)(smem + CV_OFF_U + ch * CV_UCH + (a + 32) * 80 + b0 * 2);
          float kvv[4] = {lo16(kvw[0]), hi16(kvw[0]), lo16(kvw[1]), hi16(kvw[1])};
          float o[4];
#pragma unroll
          for (int i = 0; i < 4; ++i) {
            const int r = 4 * rq + i;
            float y = (half ? acc1[r] : acc0[r]) + red[(16 * half + r) * 64 + lane];
            o[i] = rbf(y) + rbf(kvv[i] * Dv);
          }
          u32x2 ow = {pk2(o[0], o[1]), pk2(o[2], o[3])};
          *(u32x2*)(yb + 32 * a + b0) = ow;
        }
      }
    }
    __syncthreads();
    TS(5);
#pragma unroll
    for (int i = 0; i < 2; ++i) {
      const int e = tid + 256 * i, tb = e >> 2, cc = (e >> 1) & 1, hf = e & 1;
      *(u32x4*)(YT + ((size_t)tb * D_ + c0 + cc) * 16 + 8 * hf) = *(const u32x4*)(smem + CV_OFF_Y + cc * 4096 + (16 * tb + 8 * hf) * 2);
    }
    TS(6);
  }
}

template <int I> struct IC { static constexpr int value = I; };
template <int N, int I = 0, typename F>
__device__ __forceinline__ void static_for(F&& f) {
  if constexpr (I < N) { f(IC<I>{}); static_for<N, I + 1>(f); }
}

__device__ __forceinline__ float dpp_add(float v, int ctrl_sel) {
  int x = __float_as_int(v), y;
  switch (ctrl_sel) {
    case 0: y = __builtin_amdgcn_update_dpp(0, x, 0xB1, 0xf, 0xf, false); break;
    case 1: y = __builtin_amdgcn_update_dpp(0, x, 0x4E, 0xf, 0xf, false); break;
    case 2: y = __builtin_amdgcn_update_dpp(0, x, 0x141, 0xf, 0xf, false); break;
    default: y = __builtin_amdgcn_update_dpp(0, x, 0x140, 0xf, 0xf, false); break;
  }
  return v + __int_as_float(y);
}
__device__ __forceinline__ float wave_sum(float v) {
  v = dpp_add(v, 0); v = dpp_add(v, 1); v = dpp_add(v, 2); v = dpp_add(v, 3);
  int x = __float_as_int(v);
  return (__int_as_float(__builtin_amdgcn_readlane(x, 0)) + __int_as_float(__builtin_amdgcn_readlane(x, 16))) +
         (__int_as_float(__builtin_amdgcn_readlane(x, 32)) + __int_as_float(__builtin_amdgcn_readlane(x, 48)));
}

__global__ __launch_bounds__(256) void xln_kernel(const bf16_t* __restrict__ x0, const bf16_t* __restrict__ Yp,
                                                  bf16_t* __restrict__ x1, bf16_t* __restrict__ a1P) {
  __shared__ __attribute__((aligned(16))) char T[D_ * 32];
  __shared__ float R[2][4][16][2];
  const int tid = threadIdx.x, w = tid >> 6, lane = tid & 63, fr = lane & 15, g = lane >> 4;
  const int a = blockIdx.x;
  u32x4 yl[5], xv[5];
#pragma unroll
  for (int i = 0; i < 5; ++i) yl[i] = *(const u32x4*)(Yp + (size_t)a * (D_ * 16) + (tid + 256 * i) * 8);
  const size_t xo = (size_t)(16 * a + fr) * D_ + 32 * w + 8 * g;
#pragma unroll
  for (int i = 0; i < 5; ++i) xv[i] = *(const u32x4*)(x0 + xo + 128 * i);
#pragma unroll
  for (int i = 0; i < 5; ++i) *(u32x4*)(T + (tid + 256 * i) * 16) = yl[i];
  float s0 = 0.f, s1 = 0.f;
#pragma unroll
  for (int i = 0; i < 5; ++i)
#pragma unroll
    for (int e = 0; e < 4; ++e) { float x = lo16(xv[i][e]), y = hi16(xv[i][e]); s0 += x + y; s1 += x * x + y * y; }
  s0 += __shfl_xor(s0, 16); s0 += __shfl_xor(s0, 32);
  s1 += __shfl_xor(s1, 16); s1 += __shfl_xor(s1, 32);
  if (g == 0) { R[0][w][fr][0] = s0; R[0][w][fr][1] = s1; }
  __syncthreads();
  const f32x2 st = ln_stats((R[0][0][fr][0] + R[0][1][fr][0]) + (R[0][2][fr][0] + R[0][3][fr][0]),
                            (R[0][0][fr][1] + R[0][1][fr][1]) + (R[0][2][fr][1] + R[0][3][fr][1]), 1.0f / D_);
  typedef __attribute__((address_space(3))) s16x4 lds_s16x4;
  float t0 = 0.f, t1 = 0.f;
#pragma unroll
  for (int i = 0; i < 5; ++i) {
    const char* q = T + (32 * (w + 4 * i) + 8 * g + (fr >> 2)) * 32 + 8 * (fr & 3);
    s16x4 ya = __builtin_amdgcn_ds_read_tr16_b64_v4i16((lds_s16x4*)q);
    s16x4 yb = __builtin_amdgcn_ds_read_tr16_b64_v4i16((lds_s16x4*)(q + 128));
    u32x4 yv = __builtin_bit_cast(u32x4, __builtin_shufflevector(ya, yb, 0, 1, 2, 3, 4, 5, 6, 7));
    u32x4 o;
#pragma unroll
    for (int e = 0; e < 4; ++e) {
      float xa = lo16(xv[i][e]), xb = hi16(xv[i][e]);
      float da = rbf(rbf(xa - st[0]) * st[1]), db = rbf(rbf(xb - st[0]) * st[1]);
      float ta = rbf(da * lo16(yv[e])), tb = rbf(db * hi16(yv[e]));
      uint32_t wd = pk2(xa + ta, xb + tb);
      o[e] = wd;
      float pa = lo16(wd), pb = hi16(wd);
      t0 += pa + pb; t1 += pa * pa + pb * pb;
    }
    xv[i] = o;
    *(u32x4*)(x1 + xo + 128 * i) = o;
  }
  t0 += __shfl_xor(t0, 16); t0 += __shfl_xor(t0, 32);
  t1 += __shfl_xor(t1, 16); t1 += __shfl_xor(t1, 32);
  if (g == 0) { R[1][w][fr][0] = t0; R[1][w][fr][1] = t1; }
  __syncthreads();
  const f32x2 su = ln_stats((R[1][0][fr][0] + R[1][1][fr][0]) + (R[1][2][fr][0] + R[1][3][fr][0]),
                            (R[1][0][fr][1] + R[1][1][fr][1]) + (R[1][2][fr][1] + R[1][3][fr][1]), 1.0f / D_);
#pragma unroll
  for (int i = 0; i < 5; ++i) {
    u32x4 o;
#pragma unroll
    for (int e = 0; e < 4; ++e) o[e] = pk2(rbf(lo16(xv[i][e]) - su[0]) * su[1], rbf(hi16(xv[i][e]) - su[0]) * su[1]);
    *(u32x4*)(a1P + ((size_t)a * 20 + w + 4 * i) * 512 + lane * 8) = o;
  }
}

__device__ __forceinline__ void glds16(const void* gsrc, uint32_t lds_dst) {
  uint32_t keep;
  asm volatile("s_mov_b32 %0, m0\n\ts_mov_b32 m0, %2\n\ts_nop 0\n\tglobal_load_lds_dwordx4 %1, off\n\ts_mov_b32 m0, %0"
               : "=&s"(keep) : "v"(gsrc), "s"(lds_dst) : "memory");
}
__device__ __forceinline__ uint32_t lds_addr(const void* p) {
  return (uint32_t)(uintptr_t)(const __attribute__((address_space(3))) char*)p;
}
__device__ __forceinline__ void gload16(bf16x8& dst, const void* p) {
  asm volatile("global_load_dwordx4 %0, %1, off" : "=v"(dst) : "v"(p) : "memory");
}
template <int N>
__device__ __forceinline__ void wait_vm2(bf16x8& a, bf16x8& b) {
  asm volatile("s_waitcnt vmcnt(%2)" : "+v"(a), "+v"(b) : "n"(N) : "memory");
}
#define WAIT_VM(n) asm volatile("s_waitcnt vmcnt(" #n ")" ::: "memory")
#define WAIT_LGKM0() asm volatile("s_waitcnt lgkmcnt(0)" ::: "memory")
#define RAW_BAR() __builtin_amdgcn_s_barrier()

constexpr int G1_LDS = 81920;

__device__ __forceinline__ f32x2 gelu2(f32x2 x) {
  const float c1 = -2.f * 0.7978845608028654f * 1.4426950408889634f, c2 = c1 * 0.044715f;
  f32x2 t = x * x;
  f32x2 a = x * (t * c2 + c1);
  f32x2 d = {1.f + __builtin_amdgcn_exp2f(a[0]), 1.f + __builtin_amdgcn_exp2f(a[1])};
  f32x2 r = {__builtin_amdgcn_rcpf(d[0]), __builtin_amdgcn_rcpf(d[1])};
  return x * r;
}
__device__ __forceinline__ f32x2 rbf2(f32x2 v) {
  uint32_t w = pk2(v[0], v[1]);
  f32x2 r = {lo16(w), hi16(w)};
  return r;
}

constexpr int G1_P = 10;
constexpr int g1_wait(int kind, int idx) {
  int n = 0, endD[3] = {}, endW[20] = {};
  n += 4; endD[0] = n;
  for (int s = 0; s < 5; ++s) { n += 2; endW[s] = n; }
  n += 4; endD[1] = n;
  for (int s = 5; s < G1_P; ++s) { n += 2; endW[s] = n; }
  n += 2; endD[2] = n;
  for (int s = 0; s < 20; ++s) {
    if (kind == 0 && 8 * idx == s) return n - endD[idx];
    if (kind == 1 && idx == s) return n - endW[s];
    if (s + G1_P < 20) { n += 2; endW[s + G1_P] = n; }
  }
  return 0;
}
template <int N>
__device__ __forceinline__ void wait_vm() { asm volatile("s_waitcnt vmcnt(%0)" ::"n"(N) : "memory"); }

__global__ __launch_bounds__(512, 1) void g1_kernel(
    const bf16_t* __restrict__ a1P, const bf16_t* __restrict__ winT, bf16_t* __restrict__ z TSP) {
  extern __shared__ __attribute__((aligned(16))) char smem[];
  const int tid = threadIdx.x;
  TS(0);
  const int xcd = blockIdx.x & 7, idx = blockIdx.x >> 3;
  const int mt = 8 * (xcd >> 1) + (idx & 7);
  const int nt = 4 * (xcd & 1) + (idx >> 3);
  const int ps = nt < 6 ? 7 * nt : 42 + 6 * (nt - 6);
  const int np = nt < 6 ? 7 : 6;
  const int wid = __builtin_amdgcn_readfirstlane(tid >> 6), lane = tid & 63;
  const bf16_t* asrc = a1P + (size_t)(4 * mt) * (20 * 512) + lane * 8;
  const uint32_t lb = lds_addr(smem);
  auto issueD = [&](int c) {
    if (c < 2) {
      const int ks = 8 * c + wid;
#pragma unroll
      for (int tf = 0; tf < 4; ++tf) glds16(asrc + (size_t)(tf * 20 + ks) * 512, lb + (4 * ks + tf) * 1024);
    } else {
      const int ks = 16 + (wid & 3), t2 = 2 * (wid >> 2);
#pragma unroll
      for (int tf = 0; tf < 2; ++tf) glds16(asrc + (size_t)((t2 + tf) * 20 + ks) * 512, lb + (4 * ks + t2 + tf) * 1024);
    }
  };
  if (wid >= np) {
    issueD(0); issueD(1); issueD(2);
    WAIT_VM(6); RAW_BAR();
    WAIT_VM(2); RAW_BAR();
    WAIT_VM(0); RAW_BAR();
    return;
  }
  constexpr int P = G1_P;
  const bf16_t* wp[2];
#pragma unroll
  for (int i = 0; i < 2; ++i) wp[i] = winT + (size_t)(2 * (ps + wid) + i) * (20 * 512) + lane * 8;
  bf16x8 wv[P][2];
  issueD(0);
#pragma unroll
  for (int s = 0; s < 5; ++s)
#pragma unroll
    for (int i = 0; i < 2; ++i) gload16(wv[s][i], wp[i] + 512 * s);
  issueD(1);
#pragma unroll
  for (int s = 5; s < P; ++s)
#pragma unroll
    for (int i = 0; i < 2; ++i) gload16(wv[s][i], wp[i] + 512 * s);
  issueD(2);
  TS(1);
  f32x4 acc[2][4] = {};
  {
    auto step = [&](auto KS) {
      constexpr int ks = decltype(KS)::value;
      if constexpr ((ks & 7) == 0) { wait_vm<g1_wait(0, ks >> 3)>(); RAW_BAR(); }
      bf16x8 X[4];
#pragma unroll
      for (int j = 0; j < 4; ++j) X[j] = *(const bf16x8*)(smem + (4 * ks + j) * 1024 + lane * 16);
      wait_vm2<g1_wait(1, ks)>(wv[ks % P][0], wv[ks % P][1]);
#pragma unroll
      for (int j = 0; j < 4; ++j) {
        acc[0][j] = __builtin_amdgcn_mfma_f32_16x16x32_bf16(wv[ks % P][0], X[j], acc[0][j], 0, 0, 0);
        acc[1][j] = __builtin_amdgcn_mfma_f32_16x16x32_bf16(wv[ks % P][1], X[j], acc[1][j], 0, 0, 0);
      }
      if constexpr (ks + P < 20) {
        gload16(wv[ks % P][0], wp[0] + 512 * (ks + P));
        gload16(wv[ks % P][1], wp[1] + 512 * (ks + P));
      }
    };
    static_for<20>(step);
  }
  TS(3);
  bf16_t* zp = z + ((size_t)(4 * mt) * 54 + ps + wid) * 512 + lane * 8;
#pragma unroll
  for (int j = 0; j < 4; ++j) {
    u32x4 o;
#pragma unroll
    for (int i = 0; i < 2; ++i) {
      f32x2 lo = {acc[i][j][0], acc[i][j][1]}, hi = {acc[i][j][2], acc[i][j][3]};
      lo = gelu2(rbf2(lo)); hi = gelu2(rbf2(hi));
      o[2 * i] = pk2(lo[0], lo[1]); o[2 * i + 1] = pk2(hi[0], hi[1]);
    }
    *(u32x4*)(zp + (size_t)j * (54 * 512)) = o;
  }
  TS(4);
}

constexpr int ZT_P = 1744;
__global__ __launch_bounds__(256) void znt_kernel(const bf16_t* __restrict__ zP, bf16_t* __restrict__ znP) {
  __shared__ __attribute__((aligned(16))) char T[16 * ZT_P];
  __shared__ float R[4][16][2];
  const int tid = threadIdx.x, w = tid >> 6, lane = tid & 63, fr = lane & 15, g = lane >> 4;
  const int a = blockIdx.x;
  const bf16_t* src = zP + ((size_t)a * 54 + 27) * 512 + lane * 8;
  u32x4 v[7];
#pragma unroll
  for (int i = 0; i < 7; ++i) {
    u32x4 zz = {0u, 0u, 0u, 0u};
    v[i] = (w + 4 * i < 27) ? *(const u32x4*)(src + (size_t)(w + 4 * i) * 512) : zz;
  }
  float s0 = 0.f, s1 = 0.f;
#pragma unroll
  for (int i = 0; i < 7; ++i)
#pragma unroll
    for (int e = 0; e < 4; ++e) { float x = lo16(v[i][e]), y = hi16(v[i][e]); s0 += x + y; s1 += x * x + y * y; }
  s0 += __shfl_xor(s0, 16); s0 += __shfl_xor(s0, 32);
  s1 += __shfl_xor(s1, 16); s1 += __shfl_xor(s1, 32);
  if (g == 0) { R[w][fr][0] = s0; R[w][fr][1] = s1; }
  __syncthreads();
  const f32x2 st = ln_stats((R[0][fr][0] + R[1][fr][0]) + (R[2][fr][0] + R[3][fr][0]),
                            (R[0][fr][1] + R[1][fr][1]) + (R[2][fr][1] + R[3][fr][1]), 1.0f / FH_);
#pragma unroll
  for (int i = 0; i < 7; ++i) {
    if (w + 4 * i < 27) {
      u32x4 o;
#pragma unroll
      for (int e = 0; e < 4; ++e) o[e] = pk2(rbf(lo16(v[i][e]) - st[0]) * st[1], rbf(hi16(v[i][e]) - st[0]) * st[1]);
      *(u32x4*)(T + fr * ZT_P + (32 * (w + 4 * i) + 8 * g) * 2) = o;
    }
  }
  __syncthreads();
  typedef __attribute__((address_space(3))) s16x4 lds_s16x4;
  const int h2 = g >> 1, gl = g & 1;
#pragma unroll
  for (int i = 0; i < 7; ++i) {
    const int p = w + 4 * i;
    if (p < 27) {
      const char* q = T + (8 * gl + (fr >> 2)) * ZT_P + (32 * p + 8 * (fr & 3) + 4 * h2) * 2;
      s16x4 x = __builtin_amdgcn_ds_read_tr16_b64_v4i16((lds_s16x4*)q);
      s16x4 y = __builtin_amdgcn_ds_read_tr16_b64_v4i16((lds_s16x4*)(q + 4 * ZT_P));
      bf16x8 o = __builtin_shufflevector(x, y, 0, 1, 2, 3, 4, 5, 6, 7);
      *(bf16x8*)(znP + ((size_t)(2 * p + h2) * 64 + (a >> 1)) * 512 + (16 * (2 * (a & 1) + gl) + fr) * 8) = o;
    }
  }
}

constexpr int G2_LDS = 4 * 32768;
constexpr int g2_wait(int kind, int idx) {
  int n = 0, endD[8] = {}, endW[32] = {};
  n += 4; endD[0] = n;
  for (int s = 0; s < 4; ++s) { n += 2; endW[s] = n; }
  n += 4; endD[1] = n;
  for (int s = 4; s < 8; ++s) { n += 2; endW[s] = n; }
  n += 4; endD[2] = n;
  n += 4;
  for (int c = 0; c < 8; ++c) {
    if (kind == 0 && idx == c) return n - endD[c];
    if (c + 3 < 8) { n += 4; endD[c + 3] = n; }
    for (int q = 0; q < 4; ++q) {
      const int s = 4 * c + q;
      if (kind == 1 && idx == s) return n - endW[s];
      if (s + 8 < 32) { n += 2; endW[s + 8] = n; }
    }
  }
  return 0;
}

__global__ __launch_bounds__(512, 1) void g2_kernel(
    const bf16_t* __restrict__ wspP, const bf16_t* __restrict__ zP, const bf16_t* __restrict__ znP,
    const bf16_t* __restrict__ bsp, bf16_t* __restrict__ uP TSP) {
  extern __shared__ __attribute__((aligned(16))) char smem[];
  const int tid = threadIdx.x;
  TS(0);
  const int xcd = blockIdx.x & 7, idx = blockIdx.x >> 3;
  const int mt = 4 * (xcd >> 1) + (idx & 3);
  const int nt = 7 * (xcd & 1) + (idx >> 2);
  const int wid = __builtin_amdgcn_readfirstlane(tid >> 6), lane = tid & 63;
  const int tg = wid & 3, h = wid >> 2;
  const int rot = (2 * idx + 9 * xcd) & 63;
  const bf16_t* zsrc[4];
#pragma unroll
  for (int j = 0; j < 4; ++j) { int ff = 4 * nt + j; ff = ff < 54 ? ff : ff - 2; zsrc[j] = znP + (size_t)ff * (64 * 512) + lane * 8; }
  const uint32_t lbase = lds_addr(smem) + wid * 4096;
  auto issueD = [&](int c) {
    const int ke = (8 * c + wid + rot) & 63;
#pragma unroll
    for (int j = 0; j < 4; ++j) glds16(zsrc[j] + 512 * ke, lbase + (c & 3) * 32768 + j * 1024);
  };
  const bf16_t* wsrc[2];
  int wrot[2];
#pragma unroll
  for (int i = 0; i < 2; ++i) {
    const int a = 8 * mt + 2 * tg + i;
    wsrc[i] = wspP + (size_t)a * (64 * 512) + lane * 8;
    wrot[i] = h + rot + wsp_skew(a);
  }
  bf16x8 wv[8][2];
  auto issueW = [&](int s) {
#pragma unroll
    for (int i = 0; i < 2; ++i) gload16(wv[s & 7][i], wsrc[i] + 512 * ((2 * s + wrot[i]) & 63));
  };
  issueD(0);
#pragma unroll
  for (int s = 0; s < 4; ++s) issueW(s);
  issueD(1);
#pragma unroll
  for (int s = 4; s < 8; ++s) issueW(s);
  issueD(2);
  const int pair = 2 * nt + h, pairc = pair < 27 ? pair : 26;
  bf16x8 z1v[2];
  uint32_t bbv[2];
#pragma unroll
  for (int i = 0; i < 2; ++i) {
    const int a = 8 * mt + 2 * tg + i;
    gload16(z1v[i], zP + ((size_t)a * 54 + pairc) * 512 + lane * 8);
    asm volatile("global_load_ushort %0, %1, off" : "=v"(bbv[i]) : "v"(bsp + 16 * a + (lane & 15)) : "memory");
  }
  TS(1);
  f32x4 acc[2][4] = {};
  const char* lrd = smem + h * 4096 + lane * 16;
  auto stage = [&](auto C) {
    constexpr int c = decltype(C)::value;
    wait_vm<g2_wait(0, c)>();
    RAW_BAR();
    if constexpr (c + 3 < 8) issueD(c + 3);
    auto step = [&](auto Q) {
      constexpr int q = decltype(Q)::value, s = 4 * c + q;
      bf16x8 Z[4];
#pragma unroll
      for (int j = 0; j < 4; ++j) Z[j] = *(const bf16x8*)(lrd + (c & 3) * 32768 + q * 8192 + j * 1024);
      wait_vm2<g2_wait(1, s)>(wv[s & 7][0], wv[s & 7][1]);
#pragma unroll
      for (int j = 0; j < 4; ++j) {
        acc[0][j] = __builtin_amdgcn_mfma_f32_16x16x32_bf16(Z[j], wv[s & 7][0], acc[0][j], 0, 0, 0);
        acc[1][j] = __builtin_amdgcn_mfma_f32_16x16x32_bf16(Z[j], wv[s & 7][1], acc[1][j], 0, 0, 0);
      }
      if constexpr (s + 8 < 32) issueW(s + 8);
    };
    static_for<4>(step);
  };
  static_for<8>(stage);
  TS(3);
  __syncthreads();
  {
    f32x4* X = (f32x4*)smem + wid * 256 + lane;
#pragma unroll
    for (int i = 0; i < 2; ++i)
#pragma unroll
      for (int jj = 0; jj < 2; ++jj) X[(2 * i + jj) * 64] = h ? acc[i][jj] : acc[i][2 + jj];
  }
  __syncthreads();
  asm volatile("s_waitcnt vmcnt(0)" : "+v"(z1v[0]), "+v"(z1v[1]), "+v"(bbv[0]), "+v"(bbv[1])::"memory");
  if (pair < 27) {
    const f32x4* X = (const f32x4*)smem + (wid ^ 4) * 256 + lane;
#pragma unroll
    for (int i = 0; i < 2; ++i) {
      const int a = 8 * mt + 2 * tg + i;
      const float bb = __uint_as_float(bbv[i] << 16);
      const u32x4 z1 = __builtin_bit_cast(u32x4, z1v[i]);
      u32x4 o;
#pragma unroll
      for (int jj = 0; jj < 2; ++jj) {
        f32x4 v = (h ? acc[i][2 + jj] : acc[i][jj]) + X[(2 * i + jj) * 64];
        float g0 = rbf(rbf(v[0]) + bb), g1 = rbf(rbf(v[1]) + bb), g2 = rbf(rbf(v[2]) + bb), g3 = rbf(rbf(v[3]) + bb);
        o[2 * jj] = pk2(lo16(z1[2 * jj]) * g0, hi16(z1[2 * jj]) * g1);
        o[2 * jj + 1] = pk2(lo16(z1[2 * jj + 1]) * g2, hi16(z1[2 * jj + 1]) * g3);
      }
      *(u32x4*)(uP + ((size_t)a * 27 + pair) * 512 + lane * 8) = o;
    }
  }
  TS(4);
}

constexpr int G3_LDS = 28 * 4096;
constexpr int G3_P = 10;
constexpr int g3_nd(int c) { return c < 3 ? 4 : 2; }
constexpr int g3_wait(int kind, int idx) {
  int n = 0, endD[4] = {}, endW[27] = {};
  n += g3_nd(0); endD[0] = n;
  for (int s = 0; s < 5; ++s) { n += 1; endW[s] = n; }
  n += g3_nd(1); endD[1] = n;
  for (int s = 5; s < G3_P; ++s) { n += 1; endW[s] = n; }
  n += g3_nd(2); endD[2] = n;
  n += g3_nd(3); endD[3] = n;
  n += 4;
  for (int s = 0; s < 27; ++s) {
    if (kind == 0 && 8 * idx == s) return n - endD[idx];
    if (kind == 1 && idx == s) return n - endW[s];
    if (s + G3_P < 27) { n += 1; endW[s + G3_P] = n; }
  }
  return 0;
}

__global__ __launch_bounds__(512, 1) void g3_kernel(
    const bf16_t* __restrict__ uP, const bf16_t* __restrict__ woutP, const bf16_t* __restrict__ x1,
    bf16_t* __restrict__ xout TSP) {
  extern __shared__ __attribute__((aligned(16))) char smem[];
  const int tid = threadIdx.x;
  TS(0);
  const int xcd = blockIdx.x & 7, idx = blockIdx.x >> 3;
  const int mt = 8 * (xcd >> 1) + (idx & 7);
  const int nt = 4 * (xcd & 1) + (idx >> 3);
  const int wid = __builtin_amdgcn_readfirstlane(tid >> 6), lane = tid & 63;
  const bf16_t* usrc = uP + (size_t)(4 * mt) * (27 * 512) + lane * 8;
  const uint32_t lb = lds_addr(smem);
  auto issueD = [&](int c) {
    if (c < 3) {
      const int ks = 8 * c + wid;
#pragma unroll
      for (int tf = 0; tf < 4; ++tf) glds16(usrc + (size_t)(tf * 27 + ks) * 512, lb + (4 * ks + tf) * 1024);
    } else {
      const int ks = 24 + (wid % 3), t2 = 2 * ((wid / 3) & 1);
#pragma unroll
      for (int tf = 0; tf < 2; ++tf) glds16(usrc + (size_t)((t2 + tf) * 27 + ks) * 512, lb + (4 * ks + t2 + tf) * 1024);
    }
  };
  if (wid >= 5) {
    issueD(0); issueD(1); issueD(2); issueD(3);
    WAIT_VM(10); RAW_BAR();
    WAIT_VM(6); RAW_BAR();
    WAIT_VM(2); RAW_BAR();
    WAIT_VM(0); RAW_BAR();
    return;
  }
  const bf16_t* wsrc = woutP + (size_t)(5 * nt + wid) * (27 * 512) + lane * 8;
  bf16x8 wv[G3_P];
  issueD(0);
#pragma unroll
  for (int s = 0; s < 5; ++s) gload16(wv[s], wsrc + 512 * s);
  issueD(1);
#pragma unroll
  for (int s = 5; s < G3_P; ++s) gload16(wv[s], wsrc + 512 * s);
  issueD(2);
  issueD(3);
  const int fr = lane & 15, g = lane >> 4;
  const int d = 80 * nt + 16 * wid + 4 * g;
  u32x2 xr[4];
#pragma unroll
  for (int tf = 0; tf < 4; ++tf)
    asm volatile("global_load_dwordx2 %0, %1, off" : "=v"(xr[tf]) : "v"(x1 + (size_t)(64 * mt + 16 * tf + fr) * D_ + d) : "memory");
  TS(1);
  f32x4 acc[4] = {};
  auto step = [&](auto S) {
    constexpr int s = decltype(S)::value;
    if constexpr ((s & 7) == 0) { wait_vm<g3_wait(0, s >> 3)>(); RAW_BAR(); }
    bf16x8 U[4];
#pragma unroll
    for (int tf = 0; tf < 4; ++tf) U[tf] = *(const bf16x8*)(smem + (4 * s + tf) * 1024 + lane * 16);
    asm volatile("s_waitcnt vmcnt(%1)" : "+v"(wv[s % G3_P]) : "n"(g3_wait(1, s)) : "memory");
#pragma unroll
    for (int tf = 0; tf < 4; ++tf) acc[tf] = __builtin_amdgcn_mfma_f32_16x16x32_bf16(wv[s % G3_P], U[tf], acc[tf], 0, 0, 0);
    if constexpr (s + G3_P < 27) gload16(wv[s % G3_P], wsrc + 512 * (s + G3_P));
  };
  static_for<27>(step);
  TS(2);
  asm volatile("s_waitcnt vmcnt(0)" : "+v"(xr[0]), "+v"(xr[1]), "+v"(xr[2]), "+v"(xr[3])::"memory");
#pragma unroll
  for (int tf = 0; tf < 4; ++tf) {
    u32x2 w = {pk2(lo16(xr[tf][0]) + rbf(acc[tf][0]), hi16(xr[tf][0]) + rbf(acc[tf][1])),
               pk2(lo16(xr[tf][1]) + rbf(acc[tf][2]), hi16(xr[tf][1]) + rbf(acc[tf][3]))};
    *(u32x2*)(xout + (size_t)(64 * mt + 16 * tf + fr) * D_ + d) = w;
  }
  TS(3);
}

extern "C" void kernel_launch(void* const* d_in, const int* in_sizes, int n_in,
                              void* d_out, int out_size, void* d_ws, size_t ws_size,
                              hipStream_t stream) {
  const bf16_t* q = (const bf16_t*)d_in[0];
  const bf16_t* k = (const bf16_t*)d_in[1];
  const bf16_t* v = (const bf16_t*)d_in[2];
  const bf16_t* shift_k = (const bf16_t*)d_in[3];
  const bf16_t* ssm_k = (const bf16_t*)d_in[4];
  const bf16_t* ssm_D = (const bf16_t*)d_in[5];
  const bf16_t* w_in = (const bf16_t*)d_in[7];
  const bf16_t* w_sp = (const bf16_t*)d_in[8];
  const bf16_t* b_sp = (const bf16_t*)d_in[9];
  const bf16_t* w_out = (const bf16_t*)d_in[10];
  bf16_t* out = (bf16_t*)d_out;

  char* ws = (char*)d_ws;
  size_t off = 0;
  auto alloc = [&](size_t bytes) { char* p = ws + off; off += (bytes + 255) & ~(size_t)255; return p; };
  bf16_t* winP = (bf16_t*)alloc((size_t)F_ * D_ * 2);
  bf16_t* woutP = (bf16_t*)alloc((size_t)D_ * FH_ * 2);
  bf16_t* wspP = (bf16_t*)alloc((size_t)S_ * S_ * 2);
  bf16_t* kvT = (bf16_t*)alloc((size_t)S_ * D_ * 2);
  bf16_t* YT = (bf16_t*)alloc((size_t)S_ * D_ * 2);
  bf16_t* x1 = (bf16_t*)alloc((size_t)S_ * D_ * 2);
  bf16_t* x2 = (bf16_t*)alloc((size_t)S_ * D_ * 2);
  bf16_t* x3 = (bf16_t*)alloc((size_t)S_ * D_ * 2);
  bf16_t* zP = (bf16_t*)alloc((size_t)S_ * F_ * 2);
  bf16_t* uP = (bf16_t*)alloc((size_t)S_ * FH_ * 2);
  bf16_t* znP = (bf16_t*)alloc((size_t)S_ * FH_ * 2);
  bf16_t* a1 = (bf16_t*)alloc((size_t)S_ * D_ * 2);

  (void)hipFuncSetAttribute((const void*)kconv_kernel, hipFuncAttributeMaxDynamicSharedMemorySize, CV_LDS);
  (void)hipFuncSetAttribute((const void*)g1_kernel, hipFuncAttributeMaxDynamicSharedMemorySize, G1_LDS);
  (void)hipFuncSetAttribute((const void*)g2_kernel, hipFuncAttributeMaxDynamicSharedMemorySize, G2_LDS);
  (void)hipFuncSetAttribute((const void*)g3_kernel, hipFuncAttributeMaxDynamicSharedMemorySize, G3_LDS);

  kpre_kernel<<<320, 256, 0, stream>>>(k, v, shift_k, kvT);
  kconv_kernel<<<320 + 270 + 140 + 256, 256, CV_LDS, stream>>>(kvT, ssm_k, ssm_D, YT, w_in, w_out, w_sp, winP, woutP, wspP TSA(0));
  xln_kernel<<<128, 256, 0, stream>>>(q, YT, x1, a1);
  g1_kernel<<<256, 512, G1_LDS, stream>>>(a1, winP, zP TSA(1));
  znt_kernel<<<128, 256, 0, stream>>>(zP, znP);
  g2_kernel<<<224, 512, G2_LDS, stream>>>(wspP, zP, znP, b_sp, uP TSA(2));
  g3_kernel<<<256, 512, G3_LDS, stream>>>(uP, woutP, x1, x2 TSA(3));
  xln_kernel<<<128, 256, 0, stream>>>(x2, YT, x3, a1);
  g1_kernel<<<256, 512, G1_LDS, stream>>>(a1, winP, zP TSA(4));
  znt_kernel<<<128, 256, 0, stream>>>(zP, znP);
  g2_kernel<<<224, 512, G2_LDS, stream>>>(wspP, zP, znP, b_sp, uP TSA(5));
  g3_kernel<<<256, 512, G3_LDS, stream>>>(uP, woutP, x3, out TSA(6));
}
```

```cpp
#include <hip/hip_runtime.h>
#include <hip/hip_bf16.h>
#include <stdint.h>

typedef __attribute__((ext_vector_type(8))) short bf16x8;
typedef __attribute__((ext_vector_type(4))) short s16x4;
typedef __attribute__((ext_vector_type(4))) float f32x4;
typedef __attribute__((ext_vector_type(16))) float f32x16;
typedef __attribute__((ext_vector_type(2))) float f32x2;
typedef __attribute__((ext_vector_type(2))) __bf16 bf16x2_t;
typedef __attribute__((ext_vector_type(4))) uint32_t u32x4;
typedef __attribute__((ext_vector_type(2))) uint32_t u32x2;
typedef uint16_t bf16_t;

#define TSP
#define TSA(k)
#define TS(i)

constexpr int S_ = 2048, D_ = 640, F_ = 1728, FH_ = 864, FHP_ = 896;

__device__ __forceinline__ uint32_t pk2(float lo, float hi) {
  f32x2 f = {lo, hi};
  bf16x2_t b = __builtin_convertvector(f, bf16x2_t);
  return __builtin_bit_cast(uint32_t, b);
}
__device__ __forceinline__ float lo16(uint32_t v) { return __uint_as_float(v << 16); }
__device__ __forceinline__ float hi16(uint32_t v) { return __uint_as_float(v & 0xffff0000u); }
__device__ __forceinline__ float rbf(float x) { return lo16(pk2(x, x)); }
__device__ __forceinline__ float bf2f(bf16_t v) { return __uint_as_float(((uint32_t)v) << 16); }

__device__ __forceinline__ f32x2 ln_stats(float sum, float sumsq, float inv_n) {
  float mean = sum * inv_n;
  float var = fmaxf(sumsq * inv_n - mean * mean, 0.f);
  float mb = rbf(mean);
  float vb = rbf(var);
  float sq = rbf(sqrtf(rbf(vb + 1e-5f)));
  f32x2 r = {mb, 1.0f / sq};
  return r;
}

constexpr int TP = 144;
__device__ __forceinline__ void tile_transpose_out(const char* T, bf16_t* __restrict__ dst, size_t dstride, int tid) {
  const int w = tid >> 6, lane = tid & 63, gq = lane >> 4, i = lane & 15;
  typedef __attribute__((address_space(3))) s16x4 lds_s16x4;
#pragma unroll
  for (int h = 0; h < 2; ++h) {
    const int R0 = 8 * gq + 32 * h;
    const char* p = T + (R0 + (i >> 2)) * TP + (16 * w + 4 * (i & 3)) * 2;
    s16x4 a = __builtin_amdgcn_ds_read_tr16_b64_v4i16((lds_s16x4*)p);
    s16x4 b = __builtin_amdgcn_ds_read_tr16_b64_v4i16((lds_s16x4*)(p + 4 * TP));
    bf16x8 o = __builtin_shufflevector(a, b, 0, 1, 2, 3, 4, 5, 6, 7);
    *(bf16x8*)(dst + (size_t)(16 * w + i) * dstride + R0) = o;
  }
}

template <bool PERM>
__device__ __forceinline__ void tile_transpose_out_packed(const char* T, bf16_t* __restrict__ dst, int nks, int col0, int row0, int nh, int tid) {
  const int w = tid >> 6, lane = tid & 63, gq = lane >> 4, i = lane & 15;
  typedef __attribute__((address_space(3))) s16x4 lds_s16x4;
#pragma unroll
  for (int h = 0; h < 2; ++h) {
    const int R0 = 8 * gq + 32 * h;
    const char* p = T + (R0 + (i >> 2)) * TP + (PERM ? 32 * (w >> 1) + 8 * (i & 3) + 4 * (w & 1) : 16 * w + 4 * (i & 3)) * 2;
    s16x4 a = __builtin_amdgcn_ds_read_tr16_b64_v4i16((lds_s16x4*)p);
    s16x4 b = __builtin_amdgcn_ds_read_tr16_b64_v4i16((lds_s16x4*)(p + 4 * TP));
    bf16x8 o = __builtin_shufflevector(a, b, 0, 1, 2, 3, 4, 5, 6, 7);
    if (h < nh) *(bf16x8*)(dst + ((size_t)((col0 >> 4) + w) * nks + (row0 >> 5) + h) * 512 + lane * 8) = o;
  }
}

__global__ __launch_bounds__(256) void kpre_kernel(
    const bf16_t* __restrict__ kin, const bf16_t* __restrict__ vin, const bf16_t* __restrict__ shift_k,
    bf16_t* __restrict__ kvT) {
  __shared__ __attribute__((aligned(16))) char T[64 * TP];
  const int tid = threadIdx.x;
  int j = blockIdx.x;
  const int r = tid >> 2, cq = tid & 3;
  {
    const int t0 = 64 * (j & 31), d0 = 64 * (j >> 5);
    const int t = t0 + r;
    u32x4 kr[2][4], vr[2], skr[2][4];
#pragma unroll
    for (int h = 0; h < 2; ++h) {
      const int d = d0 + 8 * (cq + 4 * h);
#pragma unroll
      for (int i = 0; i < 4; ++i) {
        const int tt = t - 3 + i;
        const u32x4 kvv = *(const u32x4*)(kin + (size_t)(tt < 0 ? 0 : tt) * D_ + d);
#pragma unroll
        for (int e = 0; e < 4; ++e) kr[h][i][e] = tt < 0 ? 0u : kvv[e];
      }
      vr[h] = *(const u32x4*)(vin + (size_t)t * D_ + d);
#pragma unroll
      for (int e2 = 0; e2 < 4; ++e2) skr[h][e2] = *(const u32x4*)(shift_k + (size_t)(d + 2 * e2) * 4);
    }
    __builtin_amdgcn_sched_barrier(0);
#pragma unroll
    for (int h = 0; h < 2; ++h) {
      u32x4 o;
#pragma unroll
      for (int e2 = 0; e2 < 4; ++e2) {
        const u32x4 sk = skr[h][e2];
        float res[2];
#pragma unroll
        for (int e = 0; e < 2; ++e) {
          float w0 = lo16(sk[2 * e]), w1 = hi16(sk[2 * e]), w2 = lo16(sk[2 * e + 1]), w3 = hi16(sk[2 * e + 1]);
          float k0 = e ? hi16(kr[h][0][e2]) : lo16(kr[h][0][e2]);
          float k1 = e ? hi16(kr[h][1][e2]) : lo16(kr[h][1][e2]);
          float k2 = e ? hi16(kr[h][2][e2]) : lo16(kr[h][2][e2]);
          float k3 = e ? hi16(kr[h][3][e2]) : lo16(kr[h][3][e2]);
          float ks = rbf(k0 * w3);
          ks = rbf(ks + rbf(k1 * w2));
          ks = rbf(ks + rbf(k2 * w1));
          ks = rbf(ks + rbf(k3 * w0));
          float vv = e ? hi16(vr[h][e2]) : lo16(vr[h][e2]);
          res[e] = ks * vv;
        }
        o[e2] = pk2(res[0], res[1]);
      }
      *(u32x4*)(T + r * TP + (cq + 4 * h) * 16) = o;
    }
    __syncthreads();
    tile_transpose_out(T, kvT + (size_t)d0 * S_ + t0, S_, tid);
  }
}

__device__ __forceinline__ int wsp_skew(int a) { return a & 3; }
__device__ __forceinline__ void prep_job(int j, int tid, char* T, const bf16_t* __restrict__ w_in, const bf16_t* __restrict__ w_out,
                                         const bf16_t* __restrict__ w_sp, bf16_t* __restrict__ winP,
                                         bf16_t* __restrict__ woutP, bf16_t* __restrict__ wspP) {
  const int r = tid >> 2, cq = tid & 3;
  if (j < 270) {
    const int d0 = 64 * (j % 10), f0 = 64 * (j / 10);
#pragma unroll
    for (int h = 0; h < 2; ++h)
      *(u32x4*)(T + r * TP + (cq + 4 * h) * 16) = *(const u32x4*)(w_in + (size_t)(d0 + r) * F_ + f0 + 8 * (cq + 4 * h));
    __syncthreads();
    tile_transpose_out_packed<true>(T, winP, 20, f0, d0, 2, tid);
    return;
  }
  j -= 270;
  if (j < 140) {
    const int d0 = 64 * (j % 10), f0 = 64 * (j / 10);
#pragma unroll
    for (int h = 0; h < 2; ++h) {
      const int fr_ = f0 + r < FH_ ? f0 + r : FH_ - 1;
      *(u32x4*)(T + r * TP + (cq + 4 * h) * 16) = *(const u32x4*)(w_out + (size_t)fr_ * D_ + d0 + 8 * (cq + 4 * h));
    }
    __syncthreads();
    tile_transpose_out_packed<false>(T, woutP, 27, d0, f0, f0 + 32 < FH_ ? 2 : 1, tid);
    return;
  }
  j -= 140;
  {
    const int wv = 4 * j + (tid >> 6), lane = tid & 63, fr = lane & 15, g = lane >> 4;
    const int a = wv >> 3, k0 = 8 * (wv & 7);
    const bf16_t* src = w_sp + (size_t)(16 * a + fr) * S_ + 32 * k0 + 8 * g;
    u32x4 v[8];
#pragma unroll
    for (int i = 0; i < 8; ++i) v[i] = *(const u32x4*)(src + 32 * i);
#pragma unroll
    for (int i = 0; i < 8; ++i)
      *(u32x4*)(wspP + ((size_t)a * 64 + ((k0 + i + wsp_skew(a)) & 63)) * 512 + lane * 8) = v[i];
  }
}

__global__ __launch_bounds__(256) void kt_kernel(const bf16_t* __restrict__ YT, bf16_t* __restrict__ Y) {
  __shared__ __attribute__((aligned(16))) char T[64 * TP];
  const int tid = threadIdx.x;
  const int j = blockIdx.x;
  const int r = tid >> 2, cq = tid & 3;
  const int t0 = 64 * (j & 31), d0 = 64 * (j >> 5);
#pragma unroll
  for (int h = 0; h < 2; ++h)
    *(u32x4*)(T + r * TP + (cq + 4 * h) * 16) = *(const u32x4*)(YT + (size_t)(d0 + r) * S_ + t0 + 8 * (cq + 4 * h));
  __syncthreads();
  tile_transpose_out(T, Y + (size_t)t0 * D_ + d0, D_, tid);
}

constexpr int CV_HP = 4416;
constexpr int CV_HCH = 4 * CV_HP;
constexpr int CV_UCH = 96 * 80;
constexpr int CV_OFF_H = 0;
constexpr int CV_OFF_U = CV_OFF_H + 2 * CV_HCH;
constexpr int CV_OFF_R = CV_OFF_U + 2 * CV_UCH;
constexpr int CV_OFF_Y = CV_OFF_R + 2 * 8192;
constexpr int CV_LDS = CV_OFF_Y + 2 * 4096;

__global__ __launch_bounds__(256, 2) void kconv_kernel(
    const bf16_t* __restrict__ kvT, const bf16_t* __restrict__ ssm_k, const bf16_t* __restrict__ ssm_D,
    bf16_t* __restrict__ YT, const bf16_t* __restrict__ w_in, const bf16_t* __restrict__ w_out, const bf16_t* __restrict__ w_sp,
    bf16_t* __restrict__ winP, bf16_t* __restrict__ woutP, bf16_t* __restrict__ wspP TSP) {
  extern __shared__ __attribute__((aligned(16))) char smem[];
  const int tid = threadIdx.x;
  if (blockIdx.x >= 320) {
    prep_job(blockIdx.x - 320, tid, smem, w_in, w_out, w_sp, winP, woutP, wspP);
    return;
  }
  TS(0);
  const int c0 = 2 * blockIdx.x;
  u32x4 ur[2];
#pragma unroll
  for (int i = 0; i < 2; ++i) ur[i] = *(const u32x4*)(kvT + (size_t)c0 * S_ + 8 * (tid + 256 * i));
  u32x2 hr[3][3];
#pragma unroll
  for (int rd = 0; rd < 3; ++rd) {
    int task = tid + 256 * rd;
    task = task < 524 ? task : 523;
    int ch = task / 262, c = task % 262 - 6;
#pragma unroll
    for (int i = 0; i < 3; ++i) {
      const int idx = 8 * c + 4 * i;
      const int ic = idx < 0 ? 0 : (idx > 2044 ? 2044 : idx);
      const u32x2 v = *(const u32x2*)(ssm_k + (size_t)(c0 + ch) * S_ + ic);
      const bool ok = idx == ic;
      hr[rd][i][0] = ok ? v[0] : 0u;
      hr[rd][i][1] = ok ? v[1] : 0u;
    }
  }
  for (int i = tid; i < 2 * 160; i += 256) {
    int ch = i / 160, o = i % 160;
    u32x4 z = {0u, 0u, 0u, 0u};
    *(u32x4*)(smem + CV_OFF_U + ch * CV_UCH + o * 16) = z;
  }
  TS(1);
#pragma unroll
  for (int i = 0; i < 2; ++i) {
    const int e = 8 * (tid + 256 * i), ch = e >> 11, s = e & 2047;
    *(u32x4*)(smem + CV_OFF_U + ch * CV_UCH + ((s >> 5) + 32) * 80 + (s & 31) * 2) = ur[i];
  }
#pragma unroll
  for (int rd = 0; rd < 3; ++rd) {
    int task = tid + 256 * rd;
    if (task < 524) {
      int ch = task / 262, c = task % 262 - 6;
      uint32_t w[6] = {hr[rd][0][0], hr[rd][0][1], hr[rd][1][0], hr[rd][1][1], hr[rd][2][0], hr[rd][2][1]};
#pragma unroll
      for (int p = 0; p < 4; ++p) {
#pragma unroll
        for (int gi = 0; gi < 2; ++gi) {
          const int ao = ((1 - p) & 3) + 4 * gi;
          int a = 8 * c + ao;
          int qx = 2045 - p - a;
          auto el = [&](int e) -> uint32_t { return (e & 1) ? (w[e >> 1] >> 16) : (w[e >> 1] & 0xffff); };
          u32x2 o = {el(ao + 3) | (el(ao + 2) << 16), el(ao + 1) | (el(ao) << 16)};
          if (qx >= 0 && qx < 2088) *(u32x2*)(smem + CV_OFF_H + ch * CV_HCH + p * CV_HP + qx * 2) = o;
        }
      }
    }
  }
  TS(2);
  __syncthreads();
  TS(3);
  const int wid = tid >> 6, lane = tid & 63;
  const int ch = wid & 1, kb = wid >> 1;
  const int m = lane & 31, hh = lane >> 5;
  {
    const int p = (4 - (m & 3)) & 3;
    const char* abase = smem + CV_OFF_H + ch * CV_HCH + p * CV_HP + (2048 - m + 8 * hh - p + 16 * kb) * 2;
    const char* bbase = smem + CV_OFF_U + ch * CV_UCH + (m + 32) * 80 + 32 * kb + 16 * hh;
    f32x16 acc0 = {}, acc1 = {};
    u32x2 ra[3][4];
    bf16x8 rbB[3][2];
    auto ld = [&](int sl, int dl) {
      typedef const volatile __attribute__((address_space(3))) u32x2* lds_v2;
      ra[sl][0] = *(lds_v2)(abase - 64 * dl);
      ra[sl][1] = *(lds_v2)(abase - 64 * dl + 8);
      ra[sl][2] = *(lds_v2)(abase - 64 * (dl + 32));
      ra[sl][3] = *(lds_v2)(abase - 64 * (dl + 32) + 8);
      rbB[sl][0] = *(const bf16x8*)(bbase - 80 * dl);
      rbB[sl][1] = *(const bf16x8*)(bbase + 80 * (32 - dl));
    };
    ld(0, 0);
    ld(1, 1);
#pragma unroll
    for (int dl = 0; dl < 32; ++dl) {
      if (dl + 2 < 32) ld((dl + 2) % 3, dl + 2);
      __builtin_amdgcn_sched_barrier(0);
      const int sl = dl % 3;
      u32x4 A0u = {ra[sl][0][0], ra[sl][0][1], ra[sl][1][0], ra[sl][1][1]};
      u32x4 A1u = {ra[sl][2][0], ra[sl][2][1], ra[sl][3][0], ra[sl][3][1]};
      bf16x8 A0 = __builtin_bit_cast(bf16x8, A0u), A1 = __builtin_bit_cast(bf16x8, A1u);
      acc0 = __builtin_amdgcn_mfma_f32_32x32x16_bf16(A0, rbB[sl][0], acc0, 0, 0, 0);
      acc1 = __builtin_amdgcn_mfma_f32_32x32x16_bf16(A0, rbB[sl][1], acc1, 0, 0, 0);
      acc1 = __builtin_amdgcn_mfma_f32_32x32x16_bf16(A1, rbB[sl][0], acc1, 0, 0, 0);
      __builtin_amdgcn_sched_barrier(0);
    }
    TS(4);
    float* red = (float*)(smem + CV_OFF_R + ch * 8192);
    if (kb == 1) {
#pragma unroll
      for (int r = 0; r < 16; ++r) { red[r * 64 + lane] = acc0[r]; red[(16 + r) * 64 + lane] = acc1[r]; }
    }
    __syncthreads();
    if (kb == 0) {
      const float Dv = bf2f(ssm_D[c0 + ch]);
      bf16_t* yb = (bf16_t*)(smem + CV_OFF_Y + ch * 4096);
#pragma unroll
      for (int half = 0; half < 2; ++half) {
        const int a = m + 32 * half;
#pragma unroll
        for (int rq = 0; rq < 4; ++rq) {
          const int b0 = 8 * rq + 4 * hh;
          u32x2 kvw = *(const u32x2*)(smem + CV_OFF_U + ch * CV_UCH + (a + 32) * 80 + b0 * 2);
          float kvv[4] = {lo16(kvw[0]), hi16(kvw[0]), lo16(kvw[1]), hi16(kvw[1])};
          float o[4];
#pragma unroll
          for (int i = 0; i < 4; ++i) {
            const int r = 4 * rq + i;
            float y = (half ? acc1[r] : acc0[r]) + red[(16 * half + r) * 64 + lane];
            o[i] = rbf(y) + rbf(kvv[i] * Dv);
          }
          u32x2 ow = {pk2(o[0], o[1]), pk2(o[2], o[3])};
          *(u32x2*)(yb + 32 * a + b0) = ow;
        }
      }
    }
    __syncthreads();
    TS(5);
#pragma unroll
    for (int i = 0; i < 2; ++i) {
      const int e = tid + 256 * i;
      *(u32x4*)(YT + (size_t)c0 * S_ + 8 * e) = *(const u32x4*)(smem + CV_OFF_Y + 16 * e);
    }
    TS(6);
  }
}

template <int I> struct IC { static constexpr int value = I; };
template <int N, int I = 0, typename F>
__device__ __forceinline__ void static_for(F&& f) {
  if constexpr (I < N) { f(IC<I>{}); static_for<N, I + 1>(f); }
}

__device__ __forceinline__ float dpp_add(float v, int ctrl_sel) {
  int x = __float_as_int(v), y;
  switch (ctrl_sel) {
    case 0: y = __builtin_amdgcn_update_dpp(0, x, 0xB1, 0xf, 0xf, false); break;
    case 1: y = __builtin_amdgcn_update_dpp(0, x, 0x4E, 0xf, 0xf, false); break;
    case 2: y = __builtin_amdgcn_update_dpp(0, x, 0x141, 0xf, 0xf, false); break;
    default: y = __builtin_amdgcn_update_dpp(0, x, 0x140, 0xf, 0xf, false); break;
  }
  return v + __int_as_float(y);
}
__device__ __forceinline__ float wave_sum(float v) {
  v = dpp_add(v, 0); v = dpp_add(v, 1); v = dpp_add(v, 2); v = dpp_add(v, 3);
  int x = __float_as_int(v);
  return (__int_as_float(__builtin_amdgcn_readlane(x, 0)) + __int_as_float(__builtin_amdgcn_readlane(x, 16))) +
         (__int_as_float(__builtin_amdgcn_readlane(x, 32)) + __int_as_float(__builtin_amdgcn_readlane(x, 48)));
}

__global__ __launch_bounds__(256) void xln_kernel(const bf16_t* __restrict__ x0, const bf16_t* __restrict__ Yb,
                                                  bf16_t* __restrict__ x1, bf16_t* __restrict__ a1) {
  const int lane = threadIdx.x & 63;
  const int row = 4 * blockIdx.x + (threadIdx.x >> 6);
  const uint32_t* px = (const uint32_t*)(x0 + (size_t)row * D_);
  const uint32_t* py = (const uint32_t*)(Yb + (size_t)row * D_);
  uint32_t xv[5], yv[5];
#pragma unroll
  for (int i = 0; i < 5; ++i) { xv[i] = px[lane + 64 * i]; yv[i] = py[lane + 64 * i]; }
  float s0 = 0.f, s1 = 0.f;
#pragma unroll
  for (int i = 0; i < 5; ++i) { float a = lo16(xv[i]), b = hi16(xv[i]); s0 += a + b; s1 += a * a + b * b; }
  const f32x2 st = ln_stats(wave_sum(s0), wave_sum(s1), 1.0f / D_);
  float t0 = 0.f, t1 = 0.f;
  uint32_t* p1 = (uint32_t*)(x1 + (size_t)row * D_);
#pragma unroll
  for (int i = 0; i < 5; ++i) {
    float a = lo16(xv[i]), b = hi16(xv[i]);
    float da = rbf(rbf(a - st[0]) * st[1]), db = rbf(rbf(b - st[0]) * st[1]);
    float ta = rbf(da * lo16(yv[i])), tb = rbf(db * hi16(yv[i]));
    uint32_t w = pk2(a + ta, b + tb);
    xv[i] = w;
    p1[lane + 64 * i] = w;
    float xa = lo16(w), xb = hi16(w);
    t0 += xa + xb; t1 += xa * xa + xb * xb;
  }
  const f32x2 su = ln_stats(wave_sum(t0), wave_sum(t1), 1.0f / D_);
  uint32_t* pa = (uint32_t*)(a1 + (size_t)row * D_);
#pragma unroll
  for (int i = 0; i < 5; ++i)
    pa[lane + 64 * i] = pk2(rbf(lo16(xv[i]) - su[0]) * su[1], rbf(hi16(xv[i]) - su[0]) * su[1]);
}

__device__ __forceinline__ void glds16(const void* gsrc, uint32_t lds_dst) {
  uint32_t keep;
  asm volatile("s_mov_b32 %0, m0\n\ts_mov_b32 m0, %2\n\ts_nop 0\n\tglobal_load_lds_dwordx4 %1, off\n\ts_mov_b32 m0, %0"
               : "=&s"(keep) : "v"(gsrc), "s"(lds_dst) : "memory");
}
__device__ __forceinline__ uint32_t lds_addr(const void* p) {
  return (uint32_t)(uintptr_t)(const __attribute__((address_space(3))) char*)p;
}
__device__ __forceinline__ void gload16(bf16x8& dst, const void* p) {
  asm volatile("global_load_dwordx4 %0, %1, off" : "=v"(dst) : "v"(p) : "memory");
}
template <int N>
__device__ __forceinline__ void wait_vm2(bf16x8& a, bf16x8& b) {
  asm volatile("s_waitcnt vmcnt(%2)" : "+v"(a), "+v"(b) : "n"(N) : "memory");
}
#define WAIT_VM(n) asm volatile("s_waitcnt vmcnt(" #n ")" ::: "memory")
#define WAIT_LGKM0() asm volatile("s_waitcnt lgkmcnt(0)" ::: "memory")
#define RAW_BAR() __builtin_amdgcn_s_barrier()

constexpr int G1_LDS = 81920;

__device__ __forceinline__ f32x2 gelu2(f32x2 x) {
  const float c1 = -2.f * 0.7978845608028654f * 1.4426950408889634f, c2 = c1 * 0.044715f;
  f32x2 t = x * x;
  f32x2 a = x * (t * c2 + c1);
  f32x2 d = {1.f + __builtin_amdgcn_exp2f(a[0]), 1.f + __builtin_amdgcn_exp2f(a[1])};
  f32x2 r = {__builtin_amdgcn_rcpf(d[0]), __builtin_amdgcn_rcpf(d[1])};
  return x * r;
}
__device__ __forceinline__ f32x2 rbf2(f32x2 v) {
  uint32_t w = pk2(v[0], v[1]);
  f32x2 r = {lo16(w), hi16(w)};
  return r;
}

__global__ __launch_bounds__(512, 1) void g1_kernel(
    const bf16_t* __restrict__ a1, const bf16_t* __restrict__ winT, bf16_t* __restrict__ z TSP) {
  extern __shared__ __attribute__((aligned(16))) char smem[];
  const int tid = threadIdx.x;
  TS(0);
  const int xcd = blockIdx.x & 7, idx = blockIdx.x >> 3;
  const int mt = 8 * (xcd >> 1) + (idx & 7);
  const int nt = 4 * (xcd & 1) + (idx >> 3);
  const int ps = nt < 6 ? 7 * nt : 42 + 6 * (nt - 6);
  const int np = nt < 6 ? 7 : 6;
  const int m0 = 64 * mt;
  const int wid = __builtin_amdgcn_readfirstlane(tid >> 6), lane = tid & 63;
  {
    const int lr = lane >> 3, lc = (lane & 7) ^ lr;
    const bf16_t* ap = a1 + (size_t)(m0 + 8 * wid + lr) * D_ + 8 * lc;
    const uint32_t lbase = lds_addr(smem) + wid * 1024;
#pragma unroll
    for (int kt = 0; kt < 10; ++kt) glds16(ap + 64 * kt, lbase + kt * 8192);
  }
  if (wid >= np) {
    WAIT_VM(0);
    RAW_BAR();
    return;
  }
  const int fr = lane & 15, g = lane >> 4;
  constexpr int P = 10;
  const bf16_t* wp[2];
#pragma unroll
  for (int i = 0; i < 2; ++i) wp[i] = winT + (size_t)(2 * (ps + wid) + i) * (20 * 512) + lane * 8;
  bf16x8 wv[P][2];
#pragma unroll
  for (int s = 0; s < P; ++s)
#pragma unroll
    for (int i = 0; i < 2; ++i) gload16(wv[s][i], wp[i] + 512 * s);
  TS(1);
  WAIT_VM(20);
  RAW_BAR();
  TS(2);
  f32x4 acc[2][4] = {};
  {
    int xoff[4];
#pragma unroll
    for (int j = 0; j < 4; ++j) xoff[j] = (16 * j + fr) * 128;
    auto step = [&](auto KS) {
      constexpr int ks = decltype(KS)::value;
      bf16x8 X[4];
#pragma unroll
      for (int j = 0; j < 4; ++j)
        X[j] = *(const bf16x8*)(smem + (ks >> 1) * 8192 + xoff[j] + (((4 * (ks & 1) + g) ^ (fr & 7)) << 4));
      constexpr int newest = (ks + P - 1 < 19) ? ks + P - 1 : 19;
      wait_vm2<2 * (newest - ks)>(wv[ks % P][0], wv[ks % P][1]);
#pragma unroll
      for (int j = 0; j < 4; ++j) {
        acc[0][j] = __builtin_amdgcn_mfma_f32_16x16x32_bf16(wv[ks % P][0], X[j], acc[0][j], 0, 0, 0);
        acc[1][j] = __builtin_amdgcn_mfma_f32_16x16x32_bf16(wv[ks % P][1], X[j], acc[1][j], 0, 0, 0);
      }
      if constexpr (ks + P < 20) {
        gload16(wv[ks % P][0], wp[0] + 512 * (ks + P));
        gload16(wv[ks % P][1], wp[1] + 512 * (ks + P));
      }
    };
    static_for<20>(step);
  }
  TS(3);
  bf16_t* zp = z + ((size_t)(4 * mt) * 54 + ps + wid) * 512 + lane * 8;
#pragma unroll
  for (int j = 0; j < 4; ++j) {
    u32x4 o;
#pragma unroll
    for (int i = 0; i < 2; ++i) {
      f32x2 lo = {acc[i][j][0], acc[i][j][1]}, hi = {acc[i][j][2], acc[i][j][3]};
      lo = gelu2(rbf2(lo)); hi = gelu2(rbf2(hi));
      o[2 * i] = pk2(lo[0], lo[1]); o[2 * i + 1] = pk2(hi[0], hi[1]);
    }
    *(u32x4*)(zp + (size_t)j * (54 * 512)) = o;
  }
  TS(4);
}

constexpr int ZT_P = 1744;
__global__ __launch_bounds__(256) void znt_kernel(const bf16_t* __restrict__ zP, bf16_t* __restrict__ znP) {
  __shared__ __attribute__((aligned(16))) char T[16 * ZT_P];
  __shared__ float R[4][16][2];
  const int tid = threadIdx.x, w = tid >> 6, lane = tid & 63, fr = lane & 15, g = lane >> 4;
  const int a = blockIdx.x;
  const bf16_t* src = zP + ((size_t)a * 54 + 27) * 512 + lane * 8;
  u32x4 v[7];
#pragma unroll
  for (int i = 0; i < 7; ++i) {
    const int pp = w + 4 * i;
    const u32x4 t4 = *(const u32x4*)(src + (size_t)(pp < 27 ? pp : 26) * 512);
#pragma unroll
    for (int e = 0; e < 4; ++e) v[i][e] = pp < 27 ? t4[e] : 0u;
  }
  float s0 = 0.f, s1 = 0.f;
#pragma unroll
  for (int i = 0; i < 7; ++i)
#pragma unroll
    for (int e = 0; e < 4; ++e) { float x = lo16(v[i][e]), y = hi16(v[i][e]); s0 += x + y; s1 += x * x + y * y; }
  s0 += __shfl_xor(s0, 16); s0 += __shfl_xor(s0, 32);
  s1 += __shfl_xor(s1, 16); s1 += __shfl_xor(s1, 32);
  if (g == 0) { R[w][fr][0] = s0; R[w][fr][1] = s1; }
  __syncthreads();
  const f32x2 st = ln_stats((R[0][fr][0] + R[1][fr][0]) + (R[2][fr][0] + R[3][fr][0]),
                            (R[0][fr][1] + R[1][fr][1]) + (R[2][fr][1] + R[3][fr][1]), 1.0f / FH_);
#pragma unroll
  for (int i = 0; i < 7; ++i) {
    if (w + 4 * i < 27) {
      u32x4 o;
#pragma unroll
      for (int e = 0; e < 4; ++e) o[e] = pk2(rbf(lo16(v[i][e]) - st[0]) * st[1], rbf(hi16(v[i][e]) - st[0]) * st[1]);
      *(u32x4*)(T + fr * ZT_P + (32 * (w + 4 * i) + 8 * g) * 2) = o;
    }
  }
  __syncthreads();
  typedef __attribute__((address_space(3))) s16x4 lds_s16x4;
  const int h2 = g >> 1, gl = g & 1;
#pragma unroll
  for (int i = 0; i < 7; ++i) {
    const int p = w + 4 * i;
    if (p < 27) {
      const char* q = T + (8 * gl + (fr >> 2)) * ZT_P + (32 * p + 8 * (fr & 3) + 4 * h2) * 2;
      s16x4 x = __builtin_amdgcn_ds_read_tr16_b64_v4i16((lds_s16x4*)q);
      s16x4 y = __builtin_amdgcn_ds_read_tr16_b64_v4i16((lds_s16x4*)(q + 4 * ZT_P));
      bf16x8 o = __builtin_shufflevector(x, y, 0, 1, 2, 3, 4, 5, 6, 7);
      *(bf16x8*)(znP + ((size_t)(2 * p + h2) * 64 + (a >> 1)) * 512 + (16 * (2 * (a & 1) + gl) + fr) * 8) = o;
    }
  }
}

constexpr int G2_LDS = 4 * 32768;
constexpr int g2_wait(int kind, int idx) {
  int n = 0, endD[8] = {}, endW[32] = {};
  n += 4; endD[0] = n;
  for (int s = 0; s < 4; ++s) { n += 2; endW[s] = n; }
  n += 4; endD[1] = n;
  for (int s = 4; s < 8; ++s) { n += 2; endW[s] = n; }
  n += 4; endD[2] = n;
  for (int c = 0; c < 8; ++c) {
    if (kind == 0 && idx == c) return n - endD[c];
    if (c + 3 < 8) { n += 4; endD[c + 3] = n; }
    for (int q = 0; q < 4; ++q) {
      const int s = 4 * c + q;
      if (kind == 1 && idx == s) return n - endW[s];
      if (s + 8 < 32) { n += 2; endW[s + 8] = n; }
    }
  }
  return 0;
}

template <int N>
__device__ __forceinline__ void wait_vm() { asm volatile("s_waitcnt vmcnt(%0)" ::"n"(N) : "memory"); }

__global__ __launch_bounds__(512, 1) void g2_kernel(
    const bf16_t* __restrict__ wspP, const bf16_t* __restrict__ zP, const bf16_t* __restrict__ znP,
    const bf16_t* __restrict__ bsp, bf16_t* __restrict__ uP TSP) {
  extern __shared__ __attribute__((aligned(16))) char smem[];
  const int tid = threadIdx.x;
  TS(0);
  const int xcd = blockIdx.x & 7, idx = blockIdx.x >> 3;
  const int mt = 4 * (xcd >> 1) + (idx & 3);
  const int nt = 7 * (xcd & 1) + (idx >> 2);
  const int wid = __builtin_amdgcn_readfirstlane(tid >> 6), lane = tid & 63;
  const int tg = wid & 3, h = wid >> 2;
  const int rot = (2 * idx + 9 * xcd) & 63;
  const bf16_t* zsrc[4];
#pragma unroll
  for (int j = 0; j < 4; ++j) { int ff = 4 * nt + j; ff = ff < 54 ? ff : ff - 2; zsrc[j] = znP + (size_t)ff * (64 * 512) + lane * 8; }
  const uint32_t lbase = lds_addr(smem) + wid * 4096;
  auto issueD = [&](int c) {
    const int ke = (8 * c + wid + rot) & 63;
#pragma unroll
    for (int j = 0; j < 4; ++j) glds16(zsrc[j] + 512 * ke, lbase + (c & 3) * 32768 + j * 1024);
  };
  const bf16_t* wsrc[2];
  int wrot[2];
#pragma unroll
  for (int i = 0; i < 2; ++i) {
    const int a = 8 * mt + 2 * tg + i;
    wsrc[i] = wspP + (size_t)a * (64 * 512) + lane * 8;
    wrot[i] = h + rot + wsp_skew(a);
  }
  bf16x8 wv[8][2];
  auto issueW = [&](int s) {
#pragma unroll
    for (int i = 0; i < 2; ++i) gload16(wv[s & 7][i], wsrc[i] + 512 * ((2 * s + wrot[i]) & 63));
  };
  issueD(0);
#pragma unroll
  for (int s = 0; s < 4; ++s) issueW(s);
  issueD(1);
#pragma unroll
  for (int s = 4; s < 8; ++s) issueW(s);
  issueD(2);
  TS(1);
  f32x4 acc[2][4] = {};
  const char* lrd = smem + h * 4096 + lane * 16;
  auto stage = [&](auto C) {
    constexpr int c = decltype(C)::value;
    wait_vm<g2_wait(0, c)>();
    RAW_BAR();
    if constexpr (c + 3 < 8) issueD(c + 3);
    auto step = [&](auto Q) {
      constexpr int q = decltype(Q)::value, s = 4 * c + q;
      bf16x8 Z[4];
#pragma unroll
      for (int j = 0; j < 4; ++j) Z[j] = *(const bf16x8*)(lrd + (c & 3) * 32768 + q * 8192 + j * 1024);
      wait_vm2<g2_wait(1, s)>(wv[s & 7][0], wv[s & 7][1]);
#pragma unroll
      for (int j = 0; j < 4; ++j) {
        acc[0][j] = __builtin_amdgcn_mfma_f32_16x16x32_bf16(Z[j], wv[s & 7][0], acc[0][j], 0, 0, 0);
        acc[1][j] = __builtin_amdgcn_mfma_f32_16x16x32_bf16(Z[j], wv[s & 7][1], acc[1][j], 0, 0, 0);
      }
      if constexpr (s + 8 < 32) issueW(s + 8);
    };
    static_for<4>(step);
  };
  static_for<8>(stage);
  TS(3);
  __syncthreads();
  {
    f32x4* X = (f32x4*)smem + wid * 256 + lane;
#pragma unroll
    for (int i = 0; i < 2; ++i)
#pragma unroll
      for (int jj = 0; jj < 2; ++jj) X[(2 * i + jj) * 64] = h ? acc[i][jj] : acc[i][2 + jj];
  }
  __syncthreads();
  const int pair = 2 * nt + h;
  if (pair < 27) {
    const f32x4* X = (const f32x4*)smem + (wid ^ 4) * 256 + lane;
    const int fr = lane & 15;
#pragma unroll
    for (int i = 0; i < 2; ++i) {
      const int a = 8 * mt + 2 * tg + i;
      const float bb = bf2f(bsp[16 * a + fr]);
      const u32x4 z1 = *(const u32x4*)(zP + ((size_t)a * 54 + pair) * 512 + lane * 8);
      u32x4 o;
#pragma unroll
      for (int jj = 0; jj < 2; ++jj) {
        f32x4 v = (h ? acc[i][2 + jj] : acc[i][jj]) + X[(2 * i + jj) * 64];
        float g0 = rbf(rbf(v[0]) + bb), g1 = rbf(rbf(v[1]) + bb), g2 = rbf(rbf(v[2]) + bb), g3 = rbf(rbf(v[3]) + bb);
        o[2 * jj] = pk2(lo16(z1[2 * jj]) * g0, hi16(z1[2 * jj]) * g1);
        o[2 * jj + 1] = pk2(lo16(z1[2 * jj + 1]) * g2, hi16(z1[2 * jj + 1]) * g3);
      }
      *(u32x4*)(uP + ((size_t)a * 27 + pair) * 512 + lane * 8) = o;
    }
  }
  TS(4);
}

constexpr int G3_LDS = 28 * 4096;
constexpr int G3_P = 10;
constexpr int g3_nd(int c) { return c < 3 ? 4 : 2; }
constexpr int g3_wait(int kind, int idx) {
  int n = 0, endD[4] = {}, endW[27] = {};
  n += g3_nd(0); endD[0] = n;
  for (int s = 0; s < 5; ++s) { n += 1; endW[s] = n; }
  n += g3_nd(1); endD[1] = n;
  for (int s = 5; s < G3_P; ++s) { n += 1; endW[s] = n; }
  n += g3_nd(2); endD[2] = n;
  n += g3_nd(3); endD[3] = n;
  for (int s = 0; s < 27; ++s) {
    if (kind == 0 && 8 * idx == s) return n - endD[idx];
    if (kind == 1 && idx == s) return n - endW[s];
    if (s + G3_P < 27) { n += 1; endW[s + G3_P] = n; }
  }
  return 0;
}

__global__ __launch_bounds__(512, 1) void g3_kernel(
    const bf16_t* __restrict__ uP, const bf16_t* __restrict__ woutP, const bf16_t* __restrict__ x1,
    bf16_t* __restrict__ xout TSP) {
  extern __shared__ __attribute__((aligned(16))) char smem[];
  const int tid = threadIdx.x;
  TS(0);
  const int xcd = blockIdx.x & 7, idx = blockIdx.x >> 3;
  const int mt = 8 * (xcd >> 1) + (idx & 7);
  const int nt = 4 * (xcd & 1) + (idx >> 3);
  const int wid = __builtin_amdgcn_readfirstlane(tid >> 6), lane = tid & 63;
  const bf16_t* usrc = uP + (size_t)(4 * mt) * (27 * 512) + lane * 8;
  const uint32_t lb = lds_addr(smem);
  auto issueD = [&](int c) {
    if (c < 3) {
      const int ks = 8 * c + wid;
#pragma unroll
      for (int tf = 0; tf < 4; ++tf) glds16(usrc + (size_t)(tf * 27 + ks) * 512, lb + (4 * ks + tf) * 1024);
    } else {
      const int ks = 24 + (wid % 3), t2 = 2 * ((wid / 3) & 1);
#pragma unroll
      for (int tf = 0; tf < 2; ++tf) glds16(usrc + (size_t)((t2 + tf) * 27 + ks) * 512, lb + (4 * ks + t2 + tf) * 1024);
    }
  };
  if (wid >= 5) {
    issueD(0); issueD(1); issueD(2); issueD(3);
    WAIT_VM(10); RAW_BAR();
    WAIT_VM(6); RAW_BAR();
    WAIT_VM(2); RAW_BAR();
    WAIT_VM(0); RAW_BAR();
    return;
  }
  const bf16_t* wsrc = woutP + (size_t)(5 * nt + wid) * (27 * 512) + lane * 8;
  bf16x8 wv[G3_P];
  issueD(0);
#pragma unroll
  for (int s = 0; s < 5; ++s) gload16(wv[s], wsrc + 512 * s);
  issueD(1);
#pragma unroll
  for (int s = 5; s < G3_P; ++s) gload16(wv[s], wsrc + 512 * s);
  issueD(2);
  issueD(3);
  const int fr = lane & 15, g = lane >> 4;
  u32x2 xr[4];
  TS(1);
  f32x4 acc[4] = {};
  auto step = [&](auto S) {
    constexpr int s = decltype(S)::value;
    if constexpr ((s & 7) == 0) { wait_vm<g3_wait(0, s >> 3)>(); RAW_BAR(); }
    bf16x8 U[4];
#pragma unroll
    for (int tf = 0; tf < 4; ++tf) U[tf] = *(const bf16x8*)(smem + (4 * s + tf) * 1024 + lane * 16);
    asm volatile("s_waitcnt vmcnt(%1)" : "+v"(wv[s % G3_P]) : "n"(g3_wait(1, s)) : "memory");
#pragma unroll
    for (int tf = 0; tf < 4; ++tf) acc[tf] = __builtin_amdgcn_mfma_f32_16x16x32_bf16(wv[s % G3_P], U[tf], acc[tf], 0, 0, 0);
    if constexpr (s + G3_P < 27) gload16(wv[s % G3_P], wsrc + 512 * (s + G3_P));
  };
  static_for<27>(step);
  TS(2);
  const int d = 80 * nt + 16 * wid + 4 * g;
#pragma unroll
  for (int tf = 0; tf < 4; ++tf) xr[tf] = *(const u32x2*)(x1 + (size_t)(64 * mt + 16 * tf + fr) * D_ + d);
#pragma unroll
  for (int tf = 0; tf < 4; ++tf) {
    u32x2 w = {pk2(lo16(xr[tf][0]) + rbf(acc[tf][0]), hi16(xr[tf][0]) + rbf(acc[tf][1])),
               pk2(lo16(xr[tf][1]) + rbf(acc[tf][2]), hi16(xr[tf][1]) + rbf(acc[tf][3]))};
    *(u32x2*)(xout + (size_t)(64 * mt + 16 * tf + fr) * D_ + d) = w;
  }
  TS(3);
}

extern "C" void kernel_launch(void* const* d_in, const int* in_sizes, int n_in,
                              void* d_out, int out_size, void* d_ws, size_t ws_size,
                              hipStream_t stream) {
  const bf16_t* q = (const bf16_t*)d_in[0];
  const bf16_t* k = (const bf16_t*)d_in[1];
  const bf16_t* v = (const bf16_t*)d_in[2];
  const bf16_t* shift_k = (const bf16_t*)d_in[3];
  const bf16_t* ssm_k = (const bf16_t*)d_in[4];
  const bf16_t* ssm_D = (const bf16_t*)d_in[5];
  const bf16_t* w_in = (const bf16_t*)d_in[7];
  const bf16_t* w_sp = (const bf16_t*)d_in[8];
  const bf16_t* b_sp = (const bf16_t*)d_in[9];
  const bf16_t* w_out = (const bf16_t*)d_in[10];
  bf16_t* out = (bf16_t*)d_out;

  char* ws = (char*)d_ws;
  size_t off = 0;
  auto alloc = [&](size_t bytes) { char* p = ws + off; off += (bytes + 255) & ~(size_t)255; return p; };
  bf16_t* winP = (bf16_t*)alloc((size_t)F_ * D_ * 2);
  bf16_t* woutP = (bf16_t*)alloc((size_t)D_ * FH_ * 2);
  bf16_t* wspP = (bf16_t*)alloc((size_t)S_ * S_ * 2);
  bf16_t* Y = (bf16_t*)alloc((size_t)S_ * D_ * 2);
  bf16_t* kvT = (bf16_t*)alloc((size_t)S_ * D_ * 2);
  bf16_t* YT = (bf16_t*)alloc((size_t)S_ * D_ * 2);
  bf16_t* x1 = (bf16_t*)alloc((size_t)S_ * D_ * 2);
  bf16_t* x2 = (bf16_t*)alloc((size_t)S_ * D_ * 2);
  bf16_t* x3 = (bf16_t*)alloc((size_t)S_ * D_ * 2);
  bf16_t* zP = (bf16_t*)alloc((size_t)S_ * F_ * 2);
  bf16_t* uP = (bf16_t*)alloc((size_t)S_ * FH_ * 2);
  bf16_t* znP = (bf16_t*)alloc((size_t)S_ * FH_ * 2);
  bf16_t* a1 = (bf16_t*)alloc((size_t)S_ * D_ * 2);

  (void)hipFuncSetAttribute((const void*)kconv_kernel, hipFuncAttributeMaxDynamicSharedMemorySize, CV_LDS);
  (void)hipFuncSetAttribute((const void*)g1_kernel, hipFuncAttributeMaxDynamicSharedMemorySize, G1_LDS);
  (void)hipFuncSetAttribute((const void*)g2_kernel, hipFuncAttributeMaxDynamicSharedMemorySize, G2_LDS);
  (void)hipFuncSetAttribute((const void*)g3_kernel, hipFuncAttributeMaxDynamicSharedMemorySize, G3_LDS);

  kpre_kernel<<<320, 256, 0, stream>>>(k, v, shift_k, kvT);
  kconv_kernel<<<320 + 270 + 140 + 256, 256, CV_LDS, stream>>>(kvT, ssm_k, ssm_D, YT, w_in, w_out, w_sp, winP, woutP, wspP TSA(0));
  kt_kernel<<<320, 256, 0, stream>>>(YT, Y);
  xln_kernel<<<512, 256, 0, stream>>>(q, Y, x1, a1);
  g1_kernel<<<256, 512, G1_LDS, stream>>>(a1, winP, zP TSA(1));
  znt_kernel<<<128, 256, 0, stream>>>(zP, znP);
  g2_kernel<<<224, 512, G2_LDS, stream>>>(wspP, zP, znP, b_sp, uP TSA(2));
  g3_kernel<<<256, 512, G3_LDS, stream>>>(uP, woutP, x1, x2 TSA(3));
  xln_kernel<<<512, 256, 0, stream>>>(x2, Y, x3, a1);
  g1_kernel<<<256, 512, G1_LDS, stream>>>(a1, winP, zP TSA(4));
  znt_kernel<<<128, 256, 0, stream>>>(zP, znP);
  g2_kernel<<<224, 512, G2_LDS, stream>>>(wspP, zP, znP, b_sp, uP TSA(5));
  g3_kernel<<<256, 512, G3_LDS, stream>>>(uP, woutP, x3, out TSA(6));
}
```

```cpp
#include <hip/hip_runtime.h>
#include <hip/hip_bf16.h>
#include <stdint.h>

typedef __attribute__((ext_vector_type(8))) short bf16x8;
typedef __attribute__((ext_vector_type(4))) short s16x4;
typedef __attribute__((ext_vector_type(4))) float f32x4;
typedef __attribute__((ext_vector_type(16))) float f32x16;
typedef __attribute__((ext_vector_type(2))) float f32x2;
typedef __attribute__((ext_vector_type(2))) __bf16 bf16x2_t;
typedef __attribute__((ext_vector_type(4))) uint32_t u32x4;
typedef __attribute__((ext_vector_type(2))) uint32_t u32x2;
typedef uint16_t bf16_t;

#define TSP
#define TSA(k)
#define TS(i)

constexpr int S_ = 2048, D_ = 640, F_ = 1728, FH_ = 864, FHP_ = 896;

__device__ __forceinline__ uint32_t pk2(float lo, float hi) {
  f32x2 f = {lo, hi};
  bf16x2_t b = __builtin_convertvector(f, bf16x2_t);
  return __builtin_bit_cast(uint32_t, b);
}
__device__ __forceinline__ float lo16(uint32_t v) { return __uint_as_float(v << 16); }
__device__ __forceinline__ float hi16(uint32_t v) { return __uint_as_float(v & 0xffff0000u); }
__device__ __forceinline__ float rbf(float x) { return lo16(pk2(x, x)); }
__device__ __forceinline__ float bf2f(bf16_t v) { return __uint_as_float(((uint32_t)v) << 16); }

__device__ __forceinline__ f32x2 ln_stats(float sum, float sumsq, float inv_n) {
  float mean = sum * inv_n;
  float var = fmaxf(sumsq * inv_n - mean * mean, 0.f);
  float mb = rbf(mean);
  float vb = rbf(var);
  float sq = rbf(sqrtf(rbf(vb + 1e-5f)));
  f32x2 r = {mb, 1.0f / sq};
  return r;
}

constexpr int TP = 144;
__device__ __forceinline__ void tile_transpose_out(const char* T, bf16_t* __restrict__ dst, size_t dstride, int tid) {
  const int w = tid >> 6, lane = tid & 63, gq = lane >> 4, i = lane & 15;
  typedef __attribute__((address_space(3))) s16x4 lds_s16x4;
#pragma unroll
  for (int h = 0; h < 2; ++h) {
    const int R0 = 8 * gq + 32 * h;
    const char* p = T + (R0 + (i >> 2)) * TP + (16 * w + 4 * (i & 3)) * 2;
    s16x4 a = __builtin_amdgcn_ds_read_tr16_b64_v4i16((lds_s16x4*)p);
    s16x4 b = __builtin_amdgcn_ds_read_tr16_b64_v4i16((lds_s16x4*)(p + 4 * TP));
    bf16x8 o = __builtin_shufflevector(a, b, 0, 1, 2, 3, 4, 5, 6, 7);
    *(bf16x8*)(dst + (size_t)(16 * w + i) * dstride + R0) = o;
  }
}

template <bool PERM>
__device__ __forceinline__ void tile_transpose_out_packed(const char* T, bf16_t* __restrict__ dst, int nks, int col0, int row0, int nh, int tid) {
  const int w = tid >> 6, lane = tid & 63, gq = lane >> 4, i = lane & 15;
  typedef __attribute__((address_space(3))) s16x4 lds_s16x4;
#pragma unroll
  for (int h = 0; h < 2; ++h) {
    const int R0 = 8 * gq + 32 * h;
    const char* p = T + (R0 + (i >> 2)) * TP + (PERM ? 32 * (w >> 1) + 8 * (i & 3) + 4 * (w & 1) : 16 * w + 4 * (i & 3)) * 2;
    s16x4 a = __builtin_amdgcn_ds_read_tr16_b64_v4i16((lds_s16x4*)p);
    s16x4 b = __builtin_amdgcn_ds_read_tr16_b64_v4i16((lds_s16x4*)(p + 4 * TP));
    bf16x8 o = __builtin_shufflevector(a, b, 0, 1, 2, 3, 4, 5, 6, 7);
    if (h < nh) *(bf16x8*)(dst + ((size_t)((col0 >> 4) + w) * nks + (row0 >> 5) + h) * 512 + lane * 8) = o;
  }
}

__global__ __launch_bounds__(256) void kpre_kernel(
    const bf16_t* __restrict__ kin, const bf16_t* __restrict__ vin, const bf16_t* __restrict__ shift_k,
    bf16_t* __restrict__ kvT) {
  __shared__ __attribute__((aligned(16))) char T[64 * TP];
  const int tid = threadIdx.x;
  int j = blockIdx.x;
  const int r = tid >> 2, cq = tid & 3;
  {
    const int t0 = 64 * (j & 31), d0 = 64 * (j >> 5);
    const int t = t0 + r;
    u32x4 kr[2][4], vr[2], skr[2][4];
#pragma unroll
    for (int h = 0; h < 2; ++h) {
      const int d = d0 + 8 * (cq + 4 * h);
#pragma unroll
      for (int i = 0; i < 4; ++i) {
        const int tt = t - 3 + i;
        const u32x4 kvv = *(const u32x4*)(kin + (size_t)(tt < 0 ? 0 : tt) * D_ + d);
#pragma unroll
        for (int e = 0; e < 4; ++e) kr[h][i][e] = tt < 0 ? 0u : kvv[e];
      }
      vr[h] = *(const u32x4*)(vin + (size_t)t * D_ + d);
#pragma unroll
      for (int e2 = 0; e2 < 4; ++e2) skr[h][e2] = *(const u32x4*)(shift_k + (size_t)(d + 2 * e2) * 4);
    }
    __builtin_amdgcn_sched_barrier(0);
#pragma unroll
    for (int h = 0; h < 2; ++h) {
      u32x4 o;
#pragma unroll
      for (int e2 = 0; e2 < 4; ++e2) {
        const u32x4 sk = skr[h][e2];
        float res[2];
#pragma unroll
        for (int e = 0; e < 2; ++e) {
          float w0 = lo16(sk[2 * e]), w1 = hi16(sk[2 * e]), w2 = lo16(sk[2 * e + 1]), w3 = hi16(sk[2 * e + 1]);
          float k0 = e ? hi16(kr[h][0][e2]) : lo16(kr[h][0][e2]);
          float k1 = e ? hi16(kr[h][1][e2]) : lo16(kr[h][1][e2]);
          float k2 = e ? hi16(kr[h][2][e2]) : lo16(kr[h][2][e2]);
          float k3 = e ? hi16(kr[h][3][e2]) : lo16(kr[h][3][e2]);
          float ks = rbf(k0 * w3);
          ks = rbf(ks + rbf(k1 * w2));
          ks = rbf(ks + rbf(k2 * w1));
          ks = rbf(ks + rbf(k3 * w0));
          float vv = e ? hi16(vr[h][e2]) : lo16(vr[h][e2]);
          res[e] = ks * vv;
        }
        o[e2] = pk2(res[0], res[1]);
      }
      *(u32x4*)(T + r * TP + (cq + 4 * h) * 16) = o;
    }
    __syncthreads();
    tile_transpose_out(T, kvT + (size_t)d0 * S_ + t0, S_, tid);
  }
}

__device__ __forceinline__ int wsp_skew(int a) { return a & 3; }
__device__ __forceinline__ void prep_job(int j, int tid, char* T, const bf16_t* __restrict__ w_in, const bf16_t* __restrict__ w_out,
                                         const bf16_t* __restrict__ w_sp, bf16_t* __restrict__ winP,
                                         bf16_t* __restrict__ woutP, bf16_t* __restrict__ wspP) {
  if (j < 270) {
    const int d0 = 64 * (j % 10), f0 = 64 * (j / 10);
    u32x4 v[4];
#pragma unroll
    for (int i = 0; i < 4; ++i) {
      const int vt = tid + 128 * (i >> 1), r = vt >> 2, cq = (vt & 3) + 4 * (i & 1);
      v[i] = *(const u32x4*)(w_in + (size_t)(d0 + r) * F_ + f0 + 8 * cq);
    }
#pragma unroll
    for (int i = 0; i < 4; ++i) {
      const int vt = tid + 128 * (i >> 1), r = vt >> 2, cq = (vt & 3) + 4 * (i & 1);
      *(u32x4*)(T + r * TP + cq * 16) = v[i];
    }
    __syncthreads();
    tile_transpose_out_packed<true>(T, winP, 20, f0, d0, 2, tid);
    tile_transpose_out_packed<true>(T, winP, 20, f0, d0, 2, tid + 128);
    return;
  }
  j -= 270;
  if (j < 140) {
    const int d0 = 64 * (j % 10), f0 = 64 * (j / 10);
    u32x4 v[4];
#pragma unroll
    for (int i = 0; i < 4; ++i) {
      const int vt = tid + 128 * (i >> 1), r = vt >> 2, cq = (vt & 3) + 4 * (i & 1);
      const int fr_ = f0 + r < FH_ ? f0 + r : FH_ - 1;
      v[i] = *(const u32x4*)(w_out + (size_t)fr_ * D_ + d0 + 8 * cq);
    }
#pragma unroll
    for (int i = 0; i < 4; ++i) {
      const int vt = tid + 128 * (i >> 1), r = vt >> 2, cq = (vt & 3) + 4 * (i & 1);
      *(u32x4*)(T + r * TP + cq * 16) = v[i];
    }
    __syncthreads();
    tile_transpose_out_packed<false>(T, woutP, 27, d0, f0, f0 + 32 < FH_ ? 2 : 1, tid);
    tile_transpose_out_packed<false>(T, woutP, 27, d0, f0, f0 + 32 < FH_ ? 2 : 1, tid + 128);
    return;
  }
  j -= 140;
  {
    const int wv = 2 * j + (tid >> 6), lane = tid & 63, fr = lane & 15, g = lane >> 4;
    const int a = wv >> 3, k0 = 8 * (wv & 7);
    const bf16_t* src = w_sp + (size_t)(16 * a + fr) * S_ + 32 * k0 + 8 * g;
    u32x4 v[8];
#pragma unroll
    for (int i = 0; i < 8; ++i) v[i] = *(const u32x4*)(src + 32 * i);
#pragma unroll
    for (int i = 0; i < 8; ++i)
      *(u32x4*)(wspP + ((size_t)a * 64 + ((k0 + i + wsp_skew(a)) & 63)) * 512 + lane * 8) = v[i];
  }
}

__global__ __launch_bounds__(256) void kt_kernel(const bf16_t* __restrict__ YT, bf16_t* __restrict__ Y) {
  __shared__ __attribute__((aligned(16))) char T[64 * TP];
  const int tid = threadIdx.x;
  const int j = blockIdx.x;
  const int r = tid >> 2, cq = tid & 3;
  const int t0 = 64 * (j & 31), d0 = 64 * (j >> 5);
#pragma unroll
  for (int h = 0; h < 2; ++h)
    *(u32x4*)(T + r * TP + (cq + 4 * h) * 16) = *(const u32x4*)(YT + (size_t)(d0 + r) * S_ + t0 + 8 * (cq + 4 * h));
  __syncthreads();
  tile_transpose_out(T, Y + (size_t)t0 * D_ + d0, D_, tid);
}

constexpr int CV_HP = 4416;
constexpr int CV_UCH = 96 * 80;
constexpr int CV_OFF_H = 0;
constexpr int CV_OFF_U = CV_OFF_H + 4 * CV_HP;
constexpr int CV_OFF_R = CV_OFF_U + CV_UCH;
constexpr int CV_OFF_Y = CV_OFF_R + 8192;
constexpr int CV_LDS = CV_OFF_Y + 5120;

__global__ __launch_bounds__(128, 2) void kconv_kernel(
    const bf16_t* __restrict__ kvT, const bf16_t* __restrict__ ssm_k, const bf16_t* __restrict__ ssm_D,
    bf16_t* __restrict__ YT, const bf16_t* __restrict__ w_in, const bf16_t* __restrict__ w_out, const bf16_t* __restrict__ w_sp,
    bf16_t* __restrict__ winP, bf16_t* __restrict__ woutP, bf16_t* __restrict__ wspP TSP) {
  extern __shared__ __attribute__((aligned(16))) char smem[];
  const int tid = threadIdx.x;
  if (blockIdx.x >= 640) {
    prep_job(blockIdx.x - 640, tid, smem, w_in, w_out, w_sp, winP, woutP, wspP);
    return;
  }
  TS(0);
  const int c0 = blockIdx.x;
  const int kb = tid >> 6, lane = tid & 63;
  const float Dv = bf2f(ssm_D[c0]);
  u32x4 ur[2];
#pragma unroll
  for (int i = 0; i < 2; ++i) ur[i] = *(const u32x4*)(kvT + (size_t)c0 * S_ + 8 * (tid + 128 * i));
  u32x2 hr[3][3];
#pragma unroll
  for (int rd = 0; rd < 3; ++rd) {
    int task = tid + 128 * rd;
    task = task < 262 ? task : 261;
    const int c = task - 6;
#pragma unroll
    for (int i = 0; i < 3; ++i) {
      const int idx = 8 * c + 4 * i;
      const int ic = idx < 0 ? 0 : (idx > 2044 ? 2044 : idx);
      const u32x2 v = *(const u32x2*)(ssm_k + (size_t)c0 * S_ + ic);
      const bool ok = idx == ic;
      hr[rd][i][0] = ok ? v[0] : 0u;
      hr[rd][i][1] = ok ? v[1] : 0u;
    }
  }
  for (int i = tid; i < 160; i += 128) {
    u32x4 z = {0u, 0u, 0u, 0u};
    *(u32x4*)(smem + CV_OFF_U + i * 16) = z;
  }
  TS(1);
#pragma unroll
  for (int i = 0; i < 2; ++i) {
    const int s = 8 * (tid + 128 * i);
    *(u32x4*)(smem + CV_OFF_U + ((s >> 5) + 32) * 80 + (s & 31) * 2) = ur[i];
  }
#pragma unroll
  for (int rd = 0; rd < 3; ++rd) {
    const int task = tid + 128 * rd;
    if (task < 262) {
      const int c = task - 6;
      uint32_t w[6] = {hr[rd][0][0], hr[rd][0][1], hr[rd][1][0], hr[rd][1][1], hr[rd][2][0], hr[rd][2][1]};
#pragma unroll
      for (int p = 0; p < 4; ++p) {
#pragma unroll
        for (int gi = 0; gi < 2; ++gi) {
          const int ao = ((1 - p) & 3) + 4 * gi;
          int a = 8 * c + ao;
          int qx = 2045 - p - a;
          auto el = [&](int e) -> uint32_t { return (e & 1) ? (w[e >> 1] >> 16) : (w[e >> 1] & 0xffff); };
          u32x2 o = {el(ao + 3) | (el(ao + 2) << 16), el(ao + 1) | (el(ao) << 16)};
          if (qx >= 0 && qx < 2088) *(u32x2*)(smem + CV_OFF_H + p * CV_HP + qx * 2) = o;
        }
      }
    }
  }
  TS(2);
  __syncthreads();
  TS(3);
  const int m = lane & 31, hh = lane >> 5;
  {
    const int p = (4 - (m & 3)) & 3;
    const char* abase = smem + CV_OFF_H + p * CV_HP + (2048 - m + 8 * hh - p + 16 * kb) * 2;
    const char* bbase = smem + CV_OFF_U + (m + 32) * 80 + 32 * kb + 16 * hh;
    f32x16 acc0 = {}, acc1 = {};
    u32x2 ra[3][4];
    bf16x8 rbB[3][2];
    auto ld = [&](int sl, int dl) {
      typedef const volatile __attribute__((address_space(3))) u32x2* lds_v2;
      ra[sl][0] = *(lds_v2)(abase - 64 * dl);
      ra[sl][1] = *(lds_v2)(abase - 64 * dl + 8);
      ra[sl][2] = *(lds_v2)(abase - 64 * (dl + 32));
      ra[sl][3] = *(lds_v2)(abase - 64 * (dl + 32) + 8);
      rbB[sl][0] = *(const bf16x8*)(bbase - 80 * dl);
      rbB[sl][1] = *(const bf16x8*)(bbase + 80 * (32 - dl));
    };
    ld(0, 0);
    ld(1, 1);
#pragma unroll
    for (int dl = 0; dl < 32; ++dl) {
      if (dl + 2 < 32) ld((dl + 2) % 3, dl + 2);
      __builtin_amdgcn_sched_barrier(0);
      const int sl = dl % 3;
      u32x4 A0u = {ra[sl][0][0], ra[sl][0][1], ra[sl][1][0], ra[sl][1][1]};
      u32x4 A1u = {ra[sl][2][0], ra[sl][2][1], ra[sl][3][0], ra[sl][3][1]};
      bf16x8 A0 = __builtin_bit_cast(bf16x8, A0u), A1 = __builtin_bit_cast(bf16x8, A1u);
      acc0 = __builtin_amdgcn_mfma_f32_32x32x16_bf16(A0, rbB[sl][0], acc0, 0, 0, 0);
      acc1 = __builtin_amdgcn_mfma_f32_32x32x16_bf16(A0, rbB[sl][1], acc1, 0, 0, 0);
      acc1 = __builtin_amdgcn_mfma_f32_32x32x16_bf16(A1, rbB[sl][0], acc1, 0, 0, 0);
      __builtin_amdgcn_sched_barrier(0);
    }
    TS(4);
    float* red = (float*)(smem + CV_OFF_R);
#pragma unroll
    for (int r = 0; r < 16; ++r) red[(16 * kb + r) * 64 + lane] = kb ? acc0[r] : acc1[r];
    __syncthreads();
    {
      char* yb = smem + CV_OFF_Y;
      const int a = m + 32 * kb;
#pragma unroll
      for (int rq = 0; rq < 4; ++rq) {
        const int b0 = 8 * rq + 4 * hh;
        u32x2 kvw = *(const u32x2*)(smem + CV_OFF_U + (a + 32) * 80 + b0 * 2);
        float kvv[4] = {lo16(kvw[0]), hi16(kvw[0]), lo16(kvw[1]), hi16(kvw[1])};
        float o[4];
#pragma unroll
        for (int i = 0; i < 4; ++i) {
          const int r = 4 * rq + i;
          float y = (kb ? acc1[r] : acc0[r]) + red[(16 * (1 - kb) + r) * 64 + lane];
          o[i] = rbf(y) + rbf(kvv[i] * Dv);
        }
        u32x2 ow = {pk2(o[0], o[1]), pk2(o[2], o[3])};
        *(u32x2*)(yb + 80 * a + b0 * 2) = ow;
      }
    }
    __syncthreads();
    TS(5);
#pragma unroll
    for (int i = 0; i < 2; ++i) {
      const int e = tid + 128 * i;
      *(u32x4*)(YT + (size_t)c0 * S_ + 8 * e) = *(const u32x4*)(smem + CV_OFF_Y + (e >> 2) * 80 + (e & 3) * 16);
    }
    TS(6);
  }
}

template <int I> struct IC { static constexpr int value = I; };
template <int N, int I = 0, typename F>
__device__ __forceinline__ void static_for(F&& f) {
  if constexpr (I < N) { f(IC<I>{}); static_for<N, I + 1>(f); }
}

__device__ __forceinline__ float dpp_add(float v, int ctrl_sel) {
  int x = __float_as_int(v), y;
  switch (ctrl_sel) {
    case 0: y = __builtin_amdgcn_update_dpp(0, x, 0xB1, 0xf, 0xf, false); break;
    case 1: y = __builtin_amdgcn_update_dpp(0, x, 0x4E, 0xf, 0xf, false); break;
    case 2: y = __builtin_amdgcn_update_dpp(0, x, 0x141, 0xf, 0xf, false); break;
    default: y = __builtin_amdgcn_update_dpp(0, x, 0x140, 0xf, 0xf, false); break;
  }
  return v + __int_as_float(y);
}
__device__ __forceinline__ float wave_sum(float v) {
  v = dpp_add(v, 0); v = dpp_add(v, 1); v = dpp_add(v, 2); v = dpp_add(v, 3);
  int x = __float_as_int(v);
  return (__int_as_float(__builtin_amdgcn_readlane(x, 0)) + __int_as_float(__builtin_amdgcn_readlane(x, 16))) +
         (__int_as_float(__builtin_amdgcn_readlane(x, 32)) + __int_as_float(__builtin_amdgcn_readlane(x, 48)));
}

__global__ __launch_bounds__(256) void xln_kernel(const bf16_t* __restrict__ x0, const bf16_t* __restrict__ Yb,
                                                  bf16_t* __restrict__ x1, bf16_t* __restrict__ a1) {
  const int lane = threadIdx.x & 63;
  const int row = 4 * blockIdx.x + (threadIdx.x >> 6);
  const uint32_t* px = (const uint32_t*)(x0 + (size_t)row * D_);
  const uint32_t* py = (const uint32_t*)(Yb + (size_t)row * D_);
  uint32_t xv[5], yv[5];
#pragma unroll
  for (int i = 0; i < 5; ++i) { xv[i] = px[lane + 64 * i]; yv[i] = py[lane + 64 * i]; }
  float s0 = 0.f, s1 = 0.f;
#pragma unroll
  for (int i = 0; i < 5; ++i) { float a = lo16(xv[i]), b = hi16(xv[i]); s0 += a + b; s1 += a * a + b * b; }
  const f32x2 st = ln_stats(wave_sum(s0), wave_sum(s1), 1.0f / D_);
  float t0 = 0.f, t1 = 0.f;
  uint32_t* p1 = (uint32_t*)(x1 + (size_t)row * D_);
#pragma unroll
  for (int i = 0; i < 5; ++i) {
    float a = lo16(xv[i]), b = hi16(xv[i]);
    float da = rbf(rbf(a - st[0]) * st[1]), db = rbf(rbf(b - st[0]) * st[1]);
    float ta = rbf(da * lo16(yv[i])), tb = rbf(db * hi16(yv[i]));
    uint32_t w = pk2(a + ta, b + tb);
    xv[i] = w;
    p1[lane + 64 * i] = w;
    float xa = lo16(w), xb = hi16(w);
    t0 += xa + xb; t1 += xa * xa + xb * xb;
  }
  const f32x2 su = ln_stats(wave_sum(t0), wave_sum(t1), 1.0f / D_);
  uint32_t* pa = (uint32_t*)(a1 + (size_t)row * D_);
#pragma unroll
  for (int i = 0; i < 5; ++i)
    pa[lane + 64 * i] = pk2(rbf(lo16(xv[i]) - su[0]) * su[1], rbf(hi16(xv[i]) - su[0]) * su[1]);
}

__device__ __forceinline__ void glds16(const void* gsrc, uint32_t lds_dst) {
  uint32_t keep;
  asm volatile("s_mov_b32 %0, m0\n\ts_mov_b32 m0, %2\n\ts_nop 0\n\tglobal_load_lds_dwordx4 %1, off\n\ts_mov_b32 m0, %0"
               : "=&s"(keep) : "v"(gsrc), "s"(lds_dst) : "memory");
}
__device__ __forceinline__ uint32_t lds_addr(const void* p) {
  return (uint32_t)(uintptr_t)(const __attribute__((address_space(3))) char*)p;
}
__device__ __forceinline__ void gload16(bf16x8& dst, const void* p) {
  asm volatile("global_load_dwordx4 %0, %1, off" : "=v"(dst) : "v"(p) : "memory");
}
template <int N>
__device__ __forceinline__ void wait_vm2(bf16x8& a, bf16x8& b) {
  asm volatile("s_waitcnt vmcnt(%2)" : "+v"(a), "+v"(b) : "n"(N) : "memory");
}
#define WAIT_VM(n) asm volatile("s_waitcnt vmcnt(" #n ")" ::: "memory")
#define WAIT_LGKM0() asm volatile("s_waitcnt lgkmcnt(0)" ::: "memory")
#define RAW_BAR() __builtin_amdgcn_s_barrier()

constexpr int G1_LDS = 81920;

__device__ __forceinline__ f32x2 gelu2(f32x2 x) {
  const float c1 = -2.f * 0.7978845608028654f * 1.4426950408889634f, c2 = c1 * 0.044715f;
  f32x2 t = x * x;
  f32x2 a = x * (t * c2 + c1);
  f32x2 d = {1.f + __builtin_amdgcn_exp2f(a[0]), 1.f + __builtin_amdgcn_exp2f(a[1])};
  f32x2 r = {__builtin_amdgcn_rcpf(d[0]), __builtin_amdgcn_rcpf(d[1])};
  return x * r;
}
__device__ __forceinline__ f32x2 rbf2(f32x2 v) {
  uint32_t w = pk2(v[0], v[1]);
  f32x2 r = {lo16(w), hi16(w)};
  return r;
}

__global__ __launch_bounds__(512, 1) void g1_kernel(
    const bf16_t* __restrict__ a1, const bf16_t* __restrict__ winT, bf16_t* __restrict__ z TSP) {
  extern __shared__ __attribute__((aligned(16))) char smem[];
  const int tid = threadIdx.x;
  TS(0);
  const int xcd = blockIdx.x & 7, idx = blockIdx.x >> 3;
  const int mt = 8 * (xcd >> 1) + (idx & 7);
  const int nt = 4 * (xcd & 1) + (idx >> 3);
  const int ps = nt < 6 ? 7 * nt : 42 + 6 * (nt - 6);
  const int np = nt < 6 ? 7 : 6;
  const int m0 = 64 * mt;
  const int wid = __builtin_amdgcn_readfirstlane(tid >> 6), lane = tid & 63;
  {
    const int lr = lane >> 3, lc = (lane & 7) ^ lr;
    const bf16_t* ap = a1 + (size_t)(m0 + 8 * wid + lr) * D_ + 8 * lc;
    const uint32_t lbase = lds_addr(smem) + wid * 1024;
#pragma unroll
    for (int kt = 0; kt < 10; ++kt) glds16(ap + 64 * kt, lbase + kt * 8192);
  }
  if (wid >= np) {
    WAIT_VM(0);
    RAW_BAR();
    return;
  }
  const int fr = lane & 15, g = lane >> 4;
  constexpr int P = 10;
  const bf16_t* wp[2];
#pragma unroll
  for (int i = 0; i < 2; ++i) wp[i] = winT + (size_t)(2 * (ps + wid) + i) * (20 * 512) + lane * 8;
  bf16x8 wv[P][2];
#pragma unroll
  for (int s = 0; s < P; ++s)
#pragma unroll
    for (int i = 0; i < 2; ++i) gload16(wv[s][i], wp[i] + 512 * s);
  TS(1);
  WAIT_VM(20);
  RAW_BAR();
  TS(2);
  f32x4 acc[2][4] = {};
  {
    int xoff[4];
#pragma unroll
    for (int j = 0; j < 4; ++j) xoff[j] = (16 * j + fr) * 128;
    auto step = [&](auto KS) {
      constexpr int ks = decltype(KS)::value;
      bf16x8 X[4];
#pragma unroll
      for (int j = 0; j < 4; ++j)
        X[j] = *(const bf16x8*)(smem + (ks >> 1) * 8192 + xoff[j] + (((4 * (ks & 1) + g) ^ (fr & 7)) << 4));
      constexpr int newest = (ks + P - 1 < 19) ? ks + P - 1 : 19;
      wait_vm2<2 * (newest - ks)>(wv[ks % P][0], wv[ks % P][1]);
#pragma unroll
      for (int j = 0; j < 4; ++j) {
        acc[0][j] = __builtin_amdgcn_mfma_f32_16x16x32_bf16(wv[ks % P][0], X[j], acc[0][j], 0, 0, 0);
        acc[1][j] = __builtin_amdgcn_mfma_f32_16x16x32_bf16(wv[ks % P][1], X[j], acc[1][j], 0, 0, 0);
      }
      if constexpr (ks + P < 20) {
        gload16(wv[ks % P][0], wp[0] + 512 * (ks + P));
        gload16(wv[ks % P][1], wp[1] + 512 * (ks + P));
      }
    };
    static_for<20>(step);
  }
  TS(3);
  bf16_t* zp = z + ((size_t)(4 * mt) * 54 + ps + wid) * 512 + lane * 8;
#pragma unroll
  for (int j = 0; j < 4; ++j) {
    u32x4 o;
#pragma unroll
    for (int i = 0; i < 2; ++i) {
      f32x2 lo = {acc[i][j][0], acc[i][j][1]}, hi = {acc[i][j][2], acc[i][j][3]};
      lo = gelu2(rbf2(lo)); hi = gelu2(rbf2(hi));
      o[2 * i] = pk2(lo[0], lo[1]); o[2 * i + 1] = pk2(hi[0], hi[1]);
    }
    *(u32x4*)(zp + (size_t)j * (54 * 512)) = o;
  }
  TS(4);
}

constexpr int ZT_P = 1744;
__global__ __launch_bounds__(256) void znt_kernel(const bf16_t* __restrict__ zP, bf16_t* __restrict__ znP) {
  __shared__ __attribute__((aligned(16))) char T[16 * ZT_P];
  __shared__ float R[4][16][2];
  const int tid = threadIdx.x, w = tid >> 6, lane = tid & 63, fr = lane & 15, g = lane >> 4;
  const int a = blockIdx.x;
  const bf16_t* src = zP + ((size_t)a * 54 + 27) * 512 + lane * 8;
  u32x4 v[7];
#pragma unroll
  for (int i = 0; i < 7; ++i) {
    const int pp = w + 4 * i;
    const u32x4 t4 = *(const u32x4*)(src + (size_t)(pp < 27 ? pp : 26) * 512);
#pragma unroll
    for (int e = 0; e < 4; ++e) v[i][e] = pp < 27 ? t4[e] : 0u;
  }
  float s0 = 0.f, s1 = 0.f;
#pragma unroll
  for (int i = 0; i < 7; ++i)
#pragma unroll
    for (int e = 0; e < 4; ++e) { float x = lo16(v[i][e]), y = hi16(v[i][e]); s0 += x + y; s1 += x * x + y * y; }
  s0 += __shfl_xor(s0, 16); s0 += __shfl_xor(s0, 32);
  s1 += __shfl_xor(s1, 16); s1 += __shfl_xor(s1, 32);
  if (g == 0) { R[w][fr][0] = s0; R[w][fr][1] = s1; }
  __syncthreads();
  const f32x2 st = ln_stats((R[0][fr][0] + R[1][fr][0]) + (R[2][fr][0] + R[3][fr][0]),
                            (R[0][fr][1] + R[1][fr][1]) + (R[2][fr][1] + R[3][fr][1]), 1.0f / FH_);
#pragma unroll
  for (int i = 0; i < 7; ++i) {
    if (w + 4 * i < 27) {
      u32x4 o;
#pragma unroll
      for (int e = 0; e < 4; ++e) o[e] = pk2(rbf(lo16(v[i][e]) - st[0]) * st[1], rbf(hi16(v[i][e]) - st[0]) * st[1]);
      *(u32x4*)(T + fr * ZT_P + (32 * (w + 4 * i) + 8 * g) * 2) = o;
    }
  }
  __syncthreads();
  typedef __attribute__((address_space(3))) s16x4 lds_s16x4;
  const int h2 = g >> 1, gl = g & 1;
#pragma unroll
  for (int i = 0; i < 7; ++i) {
    const int p = w + 4 * i;
    if (p < 27) {
      const char* q = T + (8 * gl + (fr >> 2)) * ZT_P + (32 * p + 8 * (fr & 3) + 4 * h2) * 2;
      s16x4 x = __builtin_amdgcn_ds_read_tr16_b64_v4i16((lds_s16x4*)q);
      s16x4 y = __builtin_amdgcn_ds_read_tr16_b64_v4i16((lds_s16x4*)(q + 4 * ZT_P));
      bf16x8 o = __builtin_shufflevector(x, y, 0, 1, 2, 3, 4, 5, 6, 7);
      *(bf16x8*)(znP + ((size_t)(2 * p + h2) * 64 + (a >> 1)) * 512 + (16 * (2 * (a & 1) + gl) + fr) * 8) = o;
    }
  }
}

constexpr int G2_LDS = 4 * 32768;
constexpr int g2_wait(int kind, int idx) {
  int n = 0, endD[8] = {}, endW[32] = {};
  n += 4; endD[0] = n;
  for (int s = 0; s < 4; ++s) { n += 2; endW[s] = n; }
  n += 4; endD[1] = n;
  for (int s = 4; s < 8; ++s) { n += 2; endW[s] = n; }
  n += 4; endD[2] = n;
  for (int c = 0; c < 8; ++c) {
    if (kind == 0 && idx == c) return n - endD[c];
    if (c + 3 < 8) { n += 4; endD[c + 3] = n; }
    for (int q = 0; q < 4; ++q) {
      const int s = 4 * c + q;
      if (kind == 1 && idx == s) return n - endW[s];
      if (s + 8 < 32) { n += 2; endW[s + 8] = n; }
    }
  }
  return 0;
}

template <int N>
__device__ __forceinline__ void wait_vm() { asm volatile("s_waitcnt vmcnt(%0)" ::"n"(N) : "memory"); }

__global__ __launch_bounds__(512, 1) void g2_kernel(
    const bf16_t* __restrict__ wspP, const bf16_t* __restrict__ zP, const bf16_t* __restrict__ znP,
    const bf16_t* __restrict__ bsp, bf16_t* __restrict__ uP TSP) {
  extern __shared__ __attribute__((aligned(16))) char smem[];
  const int tid = threadIdx.x;
  TS(0);
  const int xcd = blockIdx.x & 7, idx = blockIdx.x >> 3;
  const int mt = 4 * (xcd >> 1) + (idx & 3);
  const int nt = 7 * (xcd & 1) + (idx >> 2);
  const int wid = __builtin_amdgcn_readfirstlane(tid >> 6), lane = tid & 63;
  const int tg = wid & 3, h = wid >> 2;
  const int rot = (2 * idx + 9 * xcd) & 63;
  const bf16_t* zsrc[4];
#pragma unroll
  for (int j = 0; j < 4; ++j) { int ff = 4 * nt + j; ff = ff < 54 ? ff : ff - 2; zsrc[j] = znP + (size_t)ff * (64 * 512) + lane * 8; }
  const uint32_t lbase = lds_addr(smem) + wid * 4096;
  auto issueD = [&](int c) {
    const int ke = (8 * c + wid + rot) & 63;
#pragma unroll
    for (int j = 0; j < 4; ++j) glds16(zsrc[j] + 512 * ke, lbase + (c & 3) * 32768 + j * 1024);
  };
  const bf16_t* wsrc[2];
  int wrot[2];
#pragma unroll
  for (int i = 0; i < 2; ++i) {
    const int a = 8 * mt + 2 * tg + i;
    wsrc[i] = wspP + (size_t)a * (64 * 512) + lane * 8;
    wrot[i] = h + rot + wsp_skew(a);
  }
  bf16x8 wv[8][2];
  auto issueW = [&](int s) {
#pragma unroll
    for (int i = 0; i < 2; ++i) gload16(wv[s & 7][i], wsrc[i] + 512 * ((2 * s + wrot[i]) & 63));
  };
  issueD(0);
#pragma unroll
  for (int s = 0; s < 4; ++s) issueW(s);
  issueD(1);
#pragma unroll
  for (int s = 4; s < 8; ++s) issueW(s);
  issueD(2);
  TS(1);
  f32x4 acc[2][4] = {};
  const char* lrd = smem + h * 4096 + lane * 16;
  auto stage = [&](auto C) {
    constexpr int c = decltype(C)::value;
    wait_vm<g2_wait(0, c)>();
    RAW_BAR();
    if constexpr (c + 3 < 8) issueD(c + 3);
    auto step = [&](auto Q) {
      constexpr int q = decltype(Q)::value, s = 4 * c + q;
      bf16x8 Z[4];
#pragma unroll
      for (int j = 0; j < 4; ++j) Z[j] = *(const bf16x8*)(lrd + (c & 3) * 32768 + q * 8192 + j * 1024);
      wait_vm2<g2_wait(1, s)>(wv[s & 7][0], wv[s & 7][1]);
#pragma unroll
      for (int j = 0; j < 4; ++j) {
        acc[0][j] = __builtin_amdgcn_mfma_f32_16x16x32_bf16(Z[j], wv[s & 7][0], acc[0][j], 0, 0, 0);
        acc[1][j] = __builtin_amdgcn_mfma_f32_16x16x32_bf16(Z[j], wv[s & 7][1], acc[1][j], 0, 0, 0);
      }
      if constexpr (s + 8 < 32) issueW(s + 8);
    };
    static_for<4>(step);
  };
  static_for<8>(stage);
  TS(3);
  __syncthreads();
  {
    f32x4* X = (f32x4*)smem + wid * 256 + lane;
#pragma unroll
    for (int i = 0; i < 2; ++i)
#pragma unroll
      for (int jj = 0; jj < 2; ++jj) X[(2 * i + jj) * 64] = h ? acc[i][jj] : acc[i][2 + jj];
  }
  __syncthreads();
  const int pair = 2 * nt + h;
  if (pair < 27) {
    const f32x4* X = (const f32x4*)smem + (wid ^ 4) * 256 + lane;
    const int fr = lane & 15;
#pragma unroll
    for (int i = 0; i < 2; ++i) {
      const int a = 8 * mt + 2 * tg + i;
      const float bb = bf2f(bsp[16 * a + fr]);
      const u32x4 z1 = *(const u32x4*)(zP + ((size_t)a * 54 + pair) * 512 + lane * 8);
      u32x4 o;
#pragma unroll
      for (int jj = 0; jj < 2; ++jj) {
        f32x4 v = (h ? acc[i][2 + jj] : acc[i][jj]) + X[(2 * i + jj) * 64];
        float g0 = rbf(rbf(v[0]) + bb), g1 = rbf(rbf(v[1]) + bb), g2 = rbf(rbf(v[2]) + bb), g3 = rbf(rbf(v[3]) + bb);
        o[2 * jj] = pk2(lo16(z1[2 * jj]) * g0, hi16(z1[2 * jj]) * g1);
        o[2 * jj + 1] = pk2(lo16(z1[2 * jj + 1]) * g2, hi16(z1[2 * jj + 1]) * g3);
      }
      *(u32x4*)(uP + ((size_t)a * 27 + pair) * 512 + lane * 8) = o;
    }
  }
  TS(4);
}

constexpr int G3_LDS = 28 * 4096;
constexpr int G3_P = 10;
constexpr int g3_nd(int c) { return c < 3 ? 4 : 2; }
constexpr int g3_wait(int kind, int idx) {
  int n = 0, endD[4] = {}, endW[27] = {};
  n += g3_nd(0); endD[0] = n;
  for (int s = 0; s < 5; ++s) { n += 1; endW[s] = n; }
  n += g3_nd(1); endD[1] = n;
  for (int s = 5; s < G3_P; ++s) { n += 1; endW[s] = n; }
  n += g3_nd(2); endD[2] = n;
  n += g3_nd(3); endD[3] = n;
  for (int s = 0; s < 27; ++s) {
    if (kind == 0 && 8 * idx == s) return n - endD[idx];
    if (kind == 1 && idx == s) return n - endW[s];
    if (s + G3_P < 27) { n += 1; endW[s + G3_P] = n; }
  }
  return 0;
}

__global__ __launch_bounds__(512, 1) void g3_kernel(
    const bf16_t* __restrict__ uP, const bf16_t* __restrict__ woutP, const bf16_t* __restrict__ x1,
    bf16_t* __restrict__ xout TSP) {
  extern __shared__ __attribute__((aligned(16))) char smem[];
  const int tid = threadIdx.x;
  TS(0);
  const int xcd = blockIdx.x & 7, idx = blockIdx.x >> 3;
  const int mt = 8 * (xcd >> 1) + (idx & 7);
  const int nt = 4 * (xcd & 1) + (idx >> 3);
  const int wid = __builtin_amdgcn_readfirstlane(tid >> 6), lane = tid & 63;
  const bf16_t* usrc = uP + (size_t)(4 * mt) * (27 * 512) + lane * 8;
  const uint32_t lb = lds_addr(smem);
  auto issueD = [&](int c) {
    if (c < 3) {
      const int ks = 8 * c + wid;
#pragma unroll
      for (int tf = 0; tf < 4; ++tf) glds16(usrc + (size_t)(tf * 27 + ks) * 512, lb + (4 * ks + tf) * 1024);
    } else {
      const int ks = 24 + (wid % 3), t2 = 2 * ((wid / 3) & 1);
#pragma unroll
      for (int tf = 0; tf < 2; ++tf) glds16(usrc + (size_t)((t2 + tf) * 27 + ks) * 512, lb + (4 * ks + t2 + tf) * 1024);
    }
  };
  if (wid >= 5) {
    issueD(0); issueD(1); issueD(2); issueD(3);
    WAIT_VM(10); RAW_BAR();
    WAIT_VM(6); RAW_BAR();
    WAIT_VM(2); RAW_BAR();
    WAIT_VM(0); RAW_BAR();
    return;
  }
  const bf16_t* wsrc = woutP + (size_t)(5 * nt + wid) * (27 * 512) + lane * 8;
  bf16x8 wv[G3_P];
  issueD(0);
#pragma unroll
  for (int s = 0; s < 5; ++s) gload16(wv[s], wsrc + 512 * s);
  issueD(1);
#pragma unroll
  for (int s = 5; s < G3_P; ++s) gload16(wv[s], wsrc + 512 * s);
  issueD(2);
  issueD(3);
  const int fr = lane & 15, g = lane >> 4;
  u32x2 xr[4];
  TS(1);
  f32x4 acc[4] = {};
  auto step = [&](auto S) {
    constexpr int s = decltype(S)::value;
    if constexpr ((s & 7) == 0) { wait_vm<g3_wait(0, s >> 3)>(); RAW_BAR(); }
    bf16x8 U[4];
#pragma unroll
    for (int tf = 0; tf < 4; ++tf) U[tf] = *(const bf16x8*)(smem + (4 * s + tf) * 1024 + lane * 16);
    asm volatile("s_waitcnt vmcnt(%1)" : "+v"(wv[s % G3_P]) : "n"(g3_wait(1, s)) : "memory");
#pragma unroll
    for (int tf = 0; tf < 4; ++tf) acc[tf] = __builtin_amdgcn_mfma_f32_16x16x32_bf16(wv[s % G3_P], U[tf], acc[tf], 0, 0, 0);
    if constexpr (s + G3_P < 27) gload16(wv[s % G3_P], wsrc + 512 * (s + G3_P));
  };
  static_for<27>(step);
  TS(2);
  const int d = 80 * nt + 16 * wid + 4 * g;
#pragma unroll
  for (int tf = 0; tf < 4; ++tf) xr[tf] = *(const u32x2*)(x1 + (size_t)(64 * mt + 16 * tf + fr) * D_ + d);
#pragma unroll
  for (int tf = 0; tf < 4; ++tf) {
    u32x2 w = {pk2(lo16(xr[tf][0]) + rbf(acc[tf][0]), hi16(xr[tf][0]) + rbf(acc[tf][1])),
               pk2(lo16(xr[tf][1]) + rbf(acc[tf][2]), hi16(xr[tf][1]) + rbf(acc[tf][3]))};
    *(u32x2*)(xout + (size_t)(64 * mt + 16 * tf + fr) * D_ + d) = w;
  }
  TS(3);
}

extern "C" void kernel_launch(void* const* d_in, const int* in_sizes, int n_in,
                              void* d_out, int out_size, void* d_ws, size_t ws_size,
                              hipStream_t stream) {
  const bf16_t* q = (const bf16_t*)d_in[0];
  const bf16_t* k = (const bf16_t*)d_in[1];
  const bf16_t* v = (const bf16_t*)d_in[2];
  const bf16_t* shift_k = (const bf16_t*)d_in[3];
  const bf16_t* ssm_k = (const bf16_t*)d_in[4];
  const bf16_t* ssm_D = (const bf16_t*)d_in[5];
  const bf16_t* w_in = (const bf16_t*)d_in[7];
  const bf16_t* w_sp = (const bf16_t*)d_in[8];
  const bf16_t* b_sp = (const bf16_t*)d_in[9];
  const bf16_t* w_out = (const bf16_t*)d_in[10];
  bf16_t* out = (bf16_t*)d_out;

  char* ws = (char*)d_ws;
  size_t off = 0;
  auto alloc = [&](size_t bytes) { char* p = ws + off; off += (bytes + 255) & ~(size_t)255; return p; };
  bf16_t* winP = (bf16_t*)alloc((size_t)F_ * D_ * 2);
  bf16_t* woutP = (bf16_t*)alloc((size_t)D_ * FH_ * 2);
  bf16_t* wspP = (bf16_t*)alloc((size_t)S_ * S_ * 2);
  bf16_t* Y = (bf16_t*)alloc((size_t)S_ * D_ * 2);
  bf16_t* kvT = (bf16_t*)alloc((size_t)S_ * D_ * 2);
  bf16_t* YT = (bf16_t*)alloc((size_t)S_ * D_ * 2);
  bf16_t* x1 = (bf16_t*)alloc((size_t)S_ * D_ * 2);
  bf16_t* x2 = (bf16_t*)alloc((size_t)S_ * D_ * 2);
  bf16_t* x3 = (bf16_t*)alloc((size_t)S_ * D_ * 2);
  bf16_t* zP = (bf16_t*)alloc((size_t)S_ * F_ * 2);
  bf16_t* uP = (bf16_t*)alloc((size_t)S_ * FH_ * 2);
  bf16_t* znP = (bf16_t*)alloc((size_t)S_ * FH_ * 2);
  bf16_t* a1 = (bf16_t*)alloc((size_t)S_ * D_ * 2);

  (void)hipFuncSetAttribute((const void*)kconv_kernel, hipFuncAttributeMaxDynamicSharedMemorySize, CV_LDS);
  (void)hipFuncSetAttribute((const void*)g1_kernel, hipFuncAttributeMaxDynamicSharedMemorySize, G1_LDS);
  (void)hipFuncSetAttribute((const void*)g2_kernel, hipFuncAttributeMaxDynamicSharedMemorySize, G2_LDS);
  (void)hipFuncSetAttribute((const void*)g3_kernel, hipFuncAttributeMaxDynamicSharedMemorySize, G3_LDS);

  kpre_kernel<<<320, 256, 0, stream>>>(k, v, shift_k, kvT);
  kconv_kernel<<<640 + 270 + 140 + 512, 128, CV_LDS, stream>>>(kvT, ssm_k, ssm_D, YT, w_in, w_out, w_sp, winP, woutP, wspP TSA(0));
  kt_kernel<<<320, 256, 0, stream>>>(YT, Y);
  xln_kernel<<<512, 256, 0, stream>>>(q, Y, x1, a1);
  g1_kernel<<<256, 512, G1_LDS, stream>>>(a1, winP, zP TSA(1));
  znt_kernel<<<128, 256, 0, stream>>>(zP, znP);
  g2_kernel<<<224, 512, G2_LDS, stream>>>(wspP, zP, znP, b_sp, uP TSA(2));
  g3_kernel<<<256, 512, G3_LDS, stream>>>(uP, woutP, x1, x2 TSA(3));
  xln_kernel<<<512, 256, 0, stream>>>(x2, Y, x3, a1);
  g1_kernel<<<256, 512, G1_LDS, stream>>>(a1, winP, zP TSA(4));
  znt_kernel<<<128, 256, 0, stream>>>(zP, znP);
  g2_kernel<<<224, 512, G2_LDS, stream>>>(wspP, zP, znP, b_sp, uP TSA(5));
  g3_kernel<<<256, 512, G3_LDS, stream>>>(uP, woutP, x3, out TSA(6));
}
```
